```python
import torch

def _softmax(x, axis=-1):
    return torch.softmax(x, dim=axis)
def _gelu(x):
    return torch.nn.functional.gelu(x, approximate="tanh")
def _silu(x):
    return torch.nn.functional.silu(x)
def _sigmoid(x):
    return torch.sigmoid(x)
def _layernorm(x, axis=-1, eps=1e-5):
    m = x.mean(dim=axis, keepdim=True)
    v = x.var(dim=axis, keepdim=True, unbiased=False)
    return (x - m) / torch.sqrt(v + eps)
def _scatter_add(tgt, idx, src, axis=0):
    return tgt.clone().index_add_(axis, idx, src)
def _segment_sum(src, seg, n):
    out = torch.zeros((n,) + src.shape[1:], dtype=src.dtype)
    return out.index_add_(0, seg, src)
def _pad(x, pw):
    flat = tuple(v for pair in reversed(pw) for v in pair)
    return torch.nn.functional.pad(x, flat)

kernel_name = 'recurrent_scan__spectral_fnet__dense_activation_squared_relu__prompt'

def setup_inputs():

    _w = (lambda a: (lambda t: t.to(torch.bfloat16) if t.is_floating_point() else t)(torch.as_tensor(a)))
    def _A_setup():
        B, S, D = 1, 2048, 512
        ns = torch.arange(S)[:, None] * torch.arange(S)[None, :]
        nd = torch.arange(D)[:, None] * torch.arange(D)[None, :]
        Ws_re = torch.cos(-2.0 * torch.pi * ns / S).to(float)
        Ws_im = torch.sin(-2.0 * torch.pi * ns / S).to(float)
        Wd_re = torch.cos(-2.0 * torch.pi * nd / D).to(float)
        Wd_im = torch.sin(-2.0 * torch.pi * nd / D).to(float)
        return {
            "x": _w((lambda *s: torch.randn(*s, dtype=torch.bfloat16) * 0.05)(B, S, D)),
            "dft_seq_re": _w(Ws_re),
            "dft_seq_im": _w(Ws_im),
            "dft_hid_re": _w(Wd_re),
            "dft_hid_im": _w(Wd_im),
        }
    _A_in = _A_setup()

    def _B_setup():
        B, S, D, F = 1, 2048, 512, 1792
        return {
            "x": _w((lambda *s: torch.randn(*s, dtype=torch.bfloat16) * 0.05)(B, S, D)),
            "w1": _w((lambda *s: torch.randn(*s, dtype=torch.bfloat16) * 0.05)(D, F)),
            "b1": _w((lambda *s: torch.randn(*s, dtype=torch.bfloat16) * 0.4)(F)),
            "w2": _w((lambda *s: torch.randn(*s, dtype=torch.bfloat16) * 0.05)(F, D)),
            "b2": _w((lambda *s: torch.randn(*s, dtype=torch.bfloat16) * 0.8)(D)),
        }
    _B_in = _B_setup()

    return {
        "A_x": _A_in["x"],
        "A_dft_seq_re": _A_in["dft_seq_re"],
        "A_dft_seq_im": _A_in["dft_seq_im"],
        "A_dft_hid_re": _A_in["dft_hid_re"],
        "A_dft_hid_im": _A_in["dft_hid_im"],
        "B_x": _B_in["x"],
        "B_w1": _B_in["w1"],
        "B_b1": _B_in["b1"],
        "B_w2": _B_in["w2"],
        "B_b2": _B_in["b2"],
    }

def reference(A_x, A_dft_seq_re, A_dft_seq_im, A_dft_hid_re, A_dft_hid_im, B_x, B_w1, B_b1, B_w2, B_b2):
    def _A_ref(x, dft_seq_re, dft_seq_im, dft_hid_re, dft_hid_im):
        a = torch.einsum("bsd,ed->bse", x, dft_hid_re)
        b = torch.einsum("bsd,ed->bse", x, dft_hid_im)
        y = torch.einsum("ts,bse->bte", dft_seq_re, a) - torch.einsum("ts,bse->bte", dft_seq_im, b)
        return y

    def _B_ref(x, w1, b1, w2, b2):
        h = torch.einsum("bsd,df->bsf", x, w1) + b1
        zero = torch.zeros_like(h)
        r = torch.where(h > zero, h, zero)
        h = r * r
        out = torch.einsum("bsf,fd->bsd", h, w2) + b2
        return out

    x = A_x
    xn = _layernorm(x, axis=-1)
    s = _A_ref(xn, A_dft_seq_re, A_dft_seq_im, A_dft_hid_re, A_dft_hid_im)
    g = _silu(_B_ref(xn, B_w1, B_b1, B_w2, B_b2))
    return x + s * g
```

```cpp
#include <hip/hip_runtime.h>
#include <hip/hip_bf16.h>
typedef unsigned short u16;
typedef short bf16x8 __attribute__((ext_vector_type(8)));
typedef float f32x4 __attribute__((ext_vector_type(4)));
constexpr int S=2048, D=512, F=1792;
__device__ __forceinline__ float bf2f(u16 b){ return __uint_as_float(((unsigned)b)<<16); }
__device__ __forceinline__ u16 f2bf(float f){ __hip_bfloat16 h = __float2bfloat16(f); return *(u16*)&h; }
__device__ __forceinline__ float rbf(float f){ return bf2f(f2bf(f)); }

__global__ void k_ln(const u16* x, u16* xn){
  int row = blockIdx.x*4 + (threadIdx.x>>6), lane = threadIdx.x&63;
  const u16* p = x + row*D + lane*8;
  float v[8]; float s=0;
  for(int i=0;i<8;i++){ v[i]=bf2f(p[i]); s+=v[i]; }
  for(int o=32;o>=1;o>>=1) s+=__shfl_xor(s,o);
  float mean=s/D; float q=0;
  for(int i=0;i<8;i++){ float d=v[i]-mean; q+=d*d; }
  for(int o=32;o>=1;o>>=1) q+=__shfl_xor(q,o);
  float var=rbf(q/D); float m=rbf(mean);
  float sd=rbf(sqrtf(rbf(var+1e-5f)));
  for(int i=0;i<8;i++) xn[row*D+lane*8+i]=f2bf(rbf(v[i]-m)/sd);
}
__global__ void k_tr(const u16* in, u16* out, int R, int C){
  int i = blockIdx.x*256+threadIdx.x; if(i>=R*C) return;
  int r=i/C,c=i%C; out[c*R+r]=in[i];
}
template<int MODE>
__global__ void k_gemm(const u16* A, int lda, const u16* Bt, int ldb, int K, const u16* bias, u16* out, int ldo, int N){
  int wave = blockIdx.x*4 + (threadIdx.x>>6), lane=threadIdx.x&63;
  int tn = N/16; int tm = wave/tn, tnn = wave%tn;
  f32x4 acc={0,0,0,0};
  const u16* ap = A + (tm*16+(lane&15))*lda + (lane>>4)*8;
  const u16* bp = Bt + (tnn*16+(lane&15))*ldb + (lane>>4)*8;
  for(int k=0;k<K;k+=32){
    bf16x8 a=*(const bf16x8*)(ap+k), b=*(const bf16x8*)(bp+k);
    acc=__builtin_amdgcn_mfma_f32_16x16x32_bf16(a,b,acc,0,0,0);
  }
  for(int j=0;j<4;j++){
    int m=tm*16+(lane>>4)*4+j, n=tnn*16+(lane&15);
    float v=acc[j];
    if(MODE==0) out[m*ldo+n]=f2bf(v);
    if(MODE==1) out[n*ldo+m]=f2bf(v);
    if(MODE==2){ float h=rbf(rbf(v)+bf2f(bias[n])); h=h>0?h:0; out[m*ldo+n]=f2bf(h*h);}
    if(MODE==3){ float o=rbf(rbf(v)+bf2f(bias[n])); out[m*ldo+n]=f2bf(o/(1.f+__expf(-o)));}
  }
}
__global__ void k_fin(const u16* x,const u16* y1,const u16* y2,const u16* g,u16* out){
  int i=blockIdx.x*256+threadIdx.x;
  float s=rbf(bf2f(y1[i])-bf2f(y2[i]));
  out[i]=f2bf(bf2f(x[i])+rbf(s*bf2f(g[i])));
}
extern "C" void kernel_launch(void* const* d_in, const int* in_sizes, int n_in, void* d_out, int out_size, void* d_ws, size_t ws_size, hipStream_t stream){
  const u16* x=(const u16*)d_in[0]; const u16* Cr=(const u16*)d_in[1]; const u16* Ci=(const u16*)d_in[2];
  const u16* Wr=(const u16*)d_in[3]; const u16* Wi=(const u16*)d_in[4];
  const u16* w1=(const u16*)d_in[6]; const u16* b1=(const u16*)d_in[7]; const u16* w2=(const u16*)d_in[8]; const u16* b2=(const u16*)d_in[9];
  u16* ws=(u16*)d_ws;
  u16* xn=ws; u16* w1t=xn+S*D; u16* w2t=w1t+D*F; u16* aT=w2t+D*F; u16* bT=aT+S*D; u16* h=bT+S*D; u16* y1=h+S*F; u16* y2=y1+S*D; u16* g=y2+S*D;
  k_ln<<<S/4,256,0,stream>>>(x,xn);
  k_tr<<<D*F/256,256,0,stream>>>(w1,w1t,D,F);
  k_tr<<<D*F/256,256,0,stream>>>(w2,w2t,F,D);
  k_gemm<1><<<(S/16)*(D/16)/4,256,0,stream>>>(xn,D,Wr,D,D,nullptr,aT,S,D);
  k_gemm<1><<<(S/16)*(D/16)/4,256,0,stream>>>(xn,D,Wi,D,D,nullptr,bT,S,D);
  k_gemm<2><<<(S/16)*(F/16)/4,256,0,stream>>>(xn,D,w1t,D,D,b1,h,F,F);
  k_gemm<0><<<(S/16)*(D/16)/4,256,0,stream>>>(Cr,S,aT,S,S,nullptr,y1,D,D);
  k_gemm<0><<<(S/16)*(D/16)/4,256,0,stream>>>(Ci,S,bT,S,S,nullptr,y2,D,D);
  k_gemm<3><<<(S/16)*(D/16)/4,256,0,stream>>>(h,F,w2t,F,F,b2,g,D,D);
  k_fin<<<S*D/256,256,0,stream>>>(x,y1,y2,g,(u16*)d_out);
}
```

```cpp
#include <hip/hip_runtime.h>
typedef unsigned short u16;
typedef unsigned long long u64;
typedef short bf16x8 __attribute__((ext_vector_type(8)));
typedef short s16x4 __attribute__((ext_vector_type(4)));
typedef float f32x4 __attribute__((ext_vector_type(4)));
typedef float f32x2 __attribute__((ext_vector_type(2)));
typedef unsigned u32x4 __attribute__((ext_vector_type(4)));
typedef unsigned u32x2 __attribute__((ext_vector_type(2)));
typedef __bf16 bf2 __attribute__((ext_vector_type(2)));
#define LAS __attribute__((address_space(3)))
#define GAS __attribute__((address_space(1)))
#define DEV __device__ __forceinline__

constexpr int S_ = 2048, D_ = 512, F_ = 1792;
constexpr unsigned WS_EPOCH = 0, WS_FLAG = 4096, FLAG_STRIDE = 128, WS_ROWST = 256u << 10;
constexpr unsigned WS_ABT = 1u << 20, WS_H = 8u << 20, WS_YP = 16u << 20, WS_BYTES = 32u << 20;
constexpr int SLOT = 16384, NSLOT = 9;
#define LEADE 6
#define LOOKE 13
#define LEADL 5
#define LOOKL 10
constexpr int LDS_MEAN = NSLOT * SLOT, LDS_RSTD = LDS_MEAN + 512, LDS_CS = LDS_RSTD + 512;
constexpr int LDS_B1 = LDS_CS + 22 * 64;
constexpr int LDS_TAG = LDS_B1 + 512;
constexpr int LDS_BYTES = LDS_TAG + 16;

template <int N> DEV void wait_vm() { asm volatile("s_waitcnt vmcnt(%0)" ::"n"(N) : "memory"); }
DEV void wait_lgkm0() { asm volatile("s_waitcnt lgkmcnt(0)" ::: "memory"); }
DEV void bar() { asm volatile("" ::: "memory"); __builtin_amdgcn_s_barrier(); asm volatile("" ::: "memory"); }
#define SCHED() __builtin_amdgcn_sched_barrier(0)
#define MFMA(a, b, c) __builtin_amdgcn_mfma_f32_16x16x32_bf16((a), (b), (c), 0, 0, 0)
DEV unsigned pk_bf16(float a, float b) { f32x2 f = {a, b}; return __builtin_bit_cast(unsigned, __builtin_convertvector(f, bf2)); }
DEV float rbf(float a) { return __uint_as_float(pk_bf16(a, 0.f) << 16); }
DEV float bflo(unsigned v) { return __uint_as_float(v << 16); }
DEV float bfhi(unsigned v) { return __uint_as_float(v & 0xffff0000u); }
DEV bf16x8 ldsb128(LAS char* p) { return *(const LAS bf16x8*)p; }
DEV bf16x8 ldstr(LAS char* p, int second) {
  s16x4 a = __builtin_amdgcn_ds_read_tr16_b64_v4i16((LAS s16x4*)p);
  s16x4 b = __builtin_amdgcn_ds_read_tr16_b64_v4i16((LAS s16x4*)(p + second));
  return __builtin_shufflevector(a, b, 0, 1, 2, 3, 4, 5, 6, 7);
}
struct Frag2 { u32x2 p[4]; };
DEV void rd2_b64x4(unsigned a0, unsigned a1, Frag2& f) {
  asm volatile("ds_read_b64 %0, %4\n\tds_read_b64 %1, %4 offset:8\n\tds_read_b64 %2, %5\n\tds_read_b64 %3, %5 offset:8"
               : "=&v"(f.p[0]), "=&v"(f.p[1]), "=&v"(f.p[2]), "=&v"(f.p[3]) : "v"(a0), "v"(a1) : "memory");
}
DEV void rd2_tr(unsigned a, Frag2& f) {
  asm volatile("ds_read_b64_tr_b16 %0, %4\n\tds_read_b64_tr_b16 %1, %4 offset:512\n\tds_read_b64_tr_b16 %2, %4 offset:4096\n\tds_read_b64_tr_b16 %3, %4 offset:4608"
               : "=&v"(f.p[0]), "=&v"(f.p[1]), "=&v"(f.p[2]), "=&v"(f.p[3]) : "v"(a) : "memory");
}
DEV void pin(Frag2& f) { asm volatile("" : "+v"(f.p[0]), "+v"(f.p[1]), "+v"(f.p[2]), "+v"(f.p[3])); }
DEV bf16x8 fr_of(const Frag2& f, int i) { return __builtin_bit_cast(bf16x8, (u32x4){f.p[2 * i].x, f.p[2 * i].y, f.p[2 * i + 1].x, f.p[2 * i + 1].y}); }
DEV unsigned opq(unsigned v) { asm volatile("" : "+v"(v)); return v; }
DEV void glds(const char* src, LAS char* dst) { __builtin_amdgcn_global_load_lds((const unsigned*)src, (LAS unsigned*)dst, 16, 0, 0); }
DEV void glds_h(const char* src, LAS char* dst) { __builtin_amdgcn_global_load_lds((const unsigned*)src, (LAS unsigned*)dst, 16, 0, 16); }
DEV u64 ld8_async(const char* p) { u64 v; asm volatile("global_load_dwordx2 %0, %1, off sc1" : "=v"(v) : "v"(p) : "memory"); return v; }
DEV u32x4 ld16_async(const char* sbase, unsigned voff) { u32x4 v; asm volatile("global_load_dwordx4 %0, %1, %2" : "=v"(v) : "v"(voff), "s"(sbase) : "memory"); return v; }
template <int OFF> DEV void ds_w128(unsigned a, u32x4 v) { asm volatile("ds_write_b128 %0, %1 offset:%2" ::"v"(a), "v"(v), "n"(OFF) : "memory"); }
template <int CTRL> DEV float dpp_add(float x) { return x + __builtin_bit_cast(float, __builtin_amdgcn_update_dpp(0, __builtin_bit_cast(int, x), CTRL, 0xf, 0xf, false)); }
DEV float sum16(float x) { x = dpp_add<0xB1>(x); x = dpp_add<0x4E>(x); x = dpp_add<0x141>(x); return dpp_add<0x140>(x); }
DEV float sum32(float x) {
  x = dpp_add<0xB1>(x); x = dpp_add<0x4E>(x); x = dpp_add<0x141>(x); x = dpp_add<0x140>(x);
  const auto r = __builtin_amdgcn_permlane16_swap(__float_as_uint(x), __float_as_uint(x), false, false);
  return __uint_as_float(r[0]) + __uint_as_float(r[1]);
}

#define STAMP(i) do {} while (0)
#define TR_DECL
#define TR(ph) do {} while (0)
#define TR_END() do {} while (0)

template <int N> struct IC { static constexpr int value = N; };
template <int B, int E, class F> DEV void static_for(F&& f) { if constexpr (B < E) { f(IC<B>{}); static_for<B + 1, E>(f); } }
constexpr int lead1(int k) { return k >= 7 ? LEADL : LEADE; }
constexpr int sched1(int t) { const int k = t / 14, e0 = t - (k >= 7 ? LOOKL : LOOKE), e1 = 14 * k - lead1(k) + (t - 14 * k) / 2; return k == 0 ? (t - 7 > 0 ? (t - 7) / 2 : 0) : (e0 > e1 ? e0 : e1); }
struct Ctx {
  const char *x, *Cr, *Ci, *Wr, *Wi, *w1, *b1, *w2, *b2; char* out; char* ws; LAS char* lds;
  int wid, lane, rp, cp, tp, ec, ks;
};

template <class P> DEV u64 poll(const char* p, P ok) {
  const u64 t0 = __builtin_amdgcn_s_memrealtime();
  u64 v;
  for (;;) {
    v = __hip_atomic_load((GAS u64*)p, __ATOMIC_RELAXED, __HIP_MEMORY_SCOPE_AGENT);
    if (__all(ok(v))) break;
    if (__builtin_amdgcn_s_memrealtime() - t0 > 20000000ull) break;
    __builtin_amdgcn_s_sleep(1);
  }
  return v;
}
DEV void set_flag(char* ws, int kind, int idx, u64 tag) {
  __hip_atomic_store((GAS u64*)(ws + WS_FLAG + (unsigned)(kind * 256 + idx) * FLAG_STRIDE), tag, __ATOMIC_RELAXED, __HIP_MEMORY_SCOPE_AGENT);
}
DEV u64 uniform64(u64 v) {
  const unsigned lo = __builtin_amdgcn_readfirstlane((unsigned)v), hi = __builtin_amdgcn_readfirstlane((unsigned)(v >> 32));
  return ((u64)hi << 32) | lo;
}

DEV void loader(const Ctx& c) {
  const int lw = c.wid - 4, lane = c.lane, wid = c.wid;
  TR_DECL;
  LAS char* lds = c.lds; char* ws = c.ws;
  const bool typeB = c.ec == 3;
  const int G = c.cp >> 2, v = c.cp & 3;
  const int E0 = 16 * G + 3 * v;
  const int PA = 7 * G + 2 * v;
  const int PB = typeB ? PA : PA + 1;
  const int r8 = lane >> 3, p8 = lane & 7;
  const int trow0 = 64 * lw + 8 * (r8 >> 2) + (r8 & 3);
#define PROW(i) (32 * ((i) >> 2) + 16 * ((i) & 1) + 4 * (((i) >> 1) & 1))
  const unsigned swz = (unsigned)((p8 ^ r8) * 16);
  const unsigned swzJ0 = (unsigned)((p8 ^ (r8 & 2)) * 16), swzJ1 = (unsigned)((p8 ^ (r8 & 2) ^ 4) * 16);
  LAS char* ld = lds + lw * 8192;
  const char* Xl = c.x + (size_t)(c.rp * 128 + trow0) * 1024 + swz;
  auto issueX = [&](int s) {
#pragma unroll
    for (int i = 0; i < 8; ++i) glds(Xl + PROW(i) * 1024 + s * 128, ld + (s & 3) * SLOT + i * 1024);
  };
  issueX(0);
  const char* pB[8]; unsigned strB[8];
  {
    const char* Wm = (E0 >= 32 ? c.Wi : c.Wr) + (size_t)(E0 & 31) * 16384;
#pragma unroll
    for (int i = 0; i < 8; ++i) {
      const int p = lw * 8 + i;
      const bool isJ = p >= 14, isW = !isJ && (typeB || p < 6);
      const int pr = isJ ? p - 14 : p - 6;
      const char* b1_ = c.w1 + (size_t)(pr * 8) * (F_ * 2) + (isJ ? PB : PA) * 128;
      const char* bw_ = Wm + (size_t)p * 8192;
      pB[i] = (isW ? bw_ : b1_) + (isW ? r8 * 1024 + swz : r8 * (F_ * 2) + ((i & 1) ? swzJ1 : swzJ0));
      strB[i] = isW ? 128 : (isJ ? 16 * F_ * 2 : 64 * F_ * 2);
    }
  }
  auto issueB1 = [&](int s) {
#pragma unroll
    for (int i = 0; i < 8; ++i) {
      LAS char* d = (i >= 6 && lw == 1) ? lds + 8 * SLOT + (i - 6) * 1024 + s * 2048 : ld + (4 + (s & 3)) * SLOT + i * 1024;
      glds(pB[i], d);
      pB[i] += strB[i];
    }
  };
  issueB1(0); issueX(1); issueB1(1);
  const char* Jl = c.w1 + PB * 128 + (size_t)(64 * lw + r8) * (F_ * 2);
  const char* Cl = (c.ks >> 1 ? c.Ci : c.Cr) + (size_t)(c.tp * 128 + trow0) * 4096 + (c.ks & 1) * 2048 + swz;
  const char* ABb = ws + WS_ABT + (size_t)(((c.ks >> 1) * 4 + c.ec) * 64 + (c.ks & 1) * 32) * 8192 + lw * 8192 + lane * 16;
  const char* Hb = ws + WS_H + (size_t)(2 * c.tp + (c.ks >> 1)) * 56 * 4096 + lw * 8192 + lane * 16;
  const char* W2l = c.w2 + (c.ec * 128 + (c.ks & 1) * 64) * 2 + (64 * lw + r8) * 1024 + (p8 ^ (r8 & 6)) * 16;
  auto issueJ = [&](int j) {
#pragma unroll
    for (int i = 0; i < 8; ++i) glds(Jl + (j * 128 + i * 8) * (F_ * 2) + ((i & 1) ? swzJ1 : swzJ0), ld + (3 + j) * SLOT + i * 1024);
  };
  auto issueC = [&](int s) {
#pragma unroll
    for (int i = 0; i < 8; ++i) glds(Cl + PROW(i) * 4096 + s * 128, ld + (s & 3) * SLOT + i * 1024);
  };
  auto issueAB = [&](int s, int slot) {
#pragma unroll
    for (int i = 0; i < 8; ++i) glds_h(ABb + s * 16384 + i * 1024, ld + slot * SLOT + i * 1024);
  };
  auto issueH = [&](int s) {
#pragma unroll
    for (int i = 0; i < 8; ++i) glds_h(Hb + s * 16384 + i * 1024, ld + (s & 3) * SLOT + i * 1024);
#pragma unroll
    for (int i = 0; i < 8; ++i) glds(W2l + i * 8192 + s * 131072, ld + (4 + (s & 3)) * SLOT + i * 1024);
  };
  wait_vm<16>();
  bar();
  issueX(2); issueB1(2);
#pragma unroll 1
  for (int s = 0; s < 5; ++s) { issueX(s + 3); issueB1(s + 3); TR(1); wait_vm<32>(); TR(1); bar(); TR(1); }
#pragma unroll 1
  for (int s = 5; s < 7; ++s) { issueJ(s - 4); issueC(s - 5); TR(1); wait_vm<32>(); TR(1); bar(); TR(1); }
  issueJ(3); issueC(2);
  wait_vm<40>(); bar();
  issueC(3);
  const u64 tag = uniform64(*(const LAS u64*)(lds + LDS_TAG));
  wait_vm<16>(); bar();
  bar();
  STAMP(1);
  {
    const int G2 = 2 * (c.ks >> 1) + (c.ec >> 1), m = lane & 3;
    const bool act = lane < 32 && ((c.ec & 1) ? (m >= 2) : (m <= 2));
    poll(ws + WS_FLAG + (unsigned)(0 * 256 + (8 * (c.ks & 1) + ((lane >> 2) & 7)) * 16 + 4 * G2 + m) * FLAG_STRIDE, [&](u64 x) { return !act || x == tag; });
  }
  STAMP(2);
  issueAB(0, 7); issueAB(1, 8); issueAB(2, 4);
  wait_vm<16>();
  bar();
  STAMP(3);
  issueAB(3, 5); issueAB(4, 6);
  wait_vm<24>(); bar(); issueC(4); issueAB(5, 7);
  wait_vm<32>(); bar(); issueC(5); issueAB(6, 8);
  const char* hfp = ws + WS_FLAG + (unsigned)(1 * 256 + c.tp * 16 + (lane & 15)) * FLAG_STRIDE;
  u64 fv = 0;
  unsigned bseq = 0x87654;
#pragma unroll 1
  for (int s = 2; s < 11; ++s) {
    TR(2); wait_vm<40>(); TR(2); bar(); TR(2);
    issueC(s + 4); issueAB(s + 5, bseq & 15); bseq = (bseq >> 4) | ((bseq & 15) << 16);
    if (s == 7) fv = ld8_async(hfp);
    TR(2);
  }
  wait_vm<40>(); bar(); issueC(15);
  STAMP(4);
  asm volatile("" : "+v"(fv));
  if (!__all(fv == tag)) poll(hfp, [&](u64 x) { return x == tag; });
  STAMP(5);
  const char* yfp = ws + WS_FLAG + (unsigned)(2 * 256 + (c.tp * 4 + c.ec) * 4 + (lane & 3)) * FLAG_STRIDE;
#pragma unroll 1
  for (int s = 12; s < 24; ++s) { TR(3); wait_vm<32>(); TR(3); bar(); TR(3); issueH(s - 12); if (s == 21) fv = ld8_async(yfp); TR(3); }
  STAMP(6);
  wait_vm<32>();
  asm volatile("" : "+v"(fv));
  if (!__all(fv == tag)) poll(yfp, [&](u64 x) { return x == tag; });
  STAMP(7);
  bar(); issueH(12);
  wait_vm<32>(); bar(); issueH(13);
  wait_vm<32>(); bar();
  wait_vm<16>(); bar();
  wait_vm<0>(); bar();
  TR_END();
  if (blockIdx.x == 0 && lw == 0) {
#pragma unroll 1
    for (int i = 0; i < 4; ++i) poll(ws + WS_FLAG + (unsigned)(2 * 256 + i * 64 + lane) * FLAG_STRIDE, [&](u64 x) { return x == tag; });
    if (lane == 0) __hip_atomic_store((GAS u64*)(ws + WS_EPOCH), tag, __ATOMIC_RELAXED, __HIP_MEMORY_SCOPE_AGENT);
  }
}

DEV void helper(const Ctx& c) {
  const int hw = c.wid - 6, lane = c.lane, wid = c.wid;
  LAS char* lds = c.lds; char* ws = c.ws;
  const bool typeB = c.ec == 3;
  const int G = c.cp >> 2, v = c.cp & 3, PA = 7 * G + 2 * v, PB = typeB ? PA : PA + 1;
  const int q = lane >> 4, cc = lane & 15, qq = cc >> 2, pp = lane & 3;
  u64 tag;
  {
    const u64 tagv = __hip_atomic_load((GAS u64*)(ws + WS_EPOCH), __ATOMIC_RELAXED, __HIP_MEMORY_SCOPE_AGENT);
    const int row = c.rp * 128 + c.cp * 8 + 4 * hw + q;
    u32x4 xv[4];
#pragma unroll
    for (int i = 0; i < 4; ++i) xv[i] = *(const u32x4*)(c.x + (size_t)row * 1024 + cc * 16 + i * 256);
    const float bv = bflo(*(const u16*)(c.b1 + ((hw ? PB : PA) * 64 + lane) * 2));
    float s = 0.f;
#pragma unroll
    for (int i = 0; i < 16; ++i) s += bflo(xv[i >> 2][i & 3]) + bfhi(xv[i >> 2][i & 3]);
    const float m = sum16(s) * (1.f / 512.f);
    float z = 0.f;
#pragma unroll
    for (int i = 0; i < 16; ++i) { const float d0 = bflo(xv[i >> 2][i & 3]) - m, d1 = bfhi(xv[i >> 2][i & 3]) - m; z += d0 * d0; z += d1 * d1; }
    const float var = sum16(z) * (1.f / 512.f);
    const float sd = __builtin_amdgcn_sqrtf(rbf(rbf(var) + 1e-5f));
    tag = uniform64(tagv) + 1;
    if (cc == 0)
      __hip_atomic_store((GAS u64*)(ws + WS_ROWST + (size_t)row * 8), ((u64)(unsigned)tag << 32) | pk_bf16(m, sd), __ATOMIC_RELAXED, __HIP_MEMORY_SCOPE_AGENT);
    *(LAS float*)(lds + LDS_B1 + (hw * 64 + lane) * 4) = bv;
    if (hw == 0 && lane == 0) { *(LAS u64*)(lds + LDS_TAG) = tag; *(LAS u64*)(lds + LDS_TAG + 8) = 0; }
  }
  const unsigned wa0 = cc * 128 + ((q ^ (cc & 7)) * 16), wa1 = cc * 128 + (((4 + q) ^ (cc & 7)) * 16);
  const unsigned wa = hw ? wa1 : wa0;
  unsigned ja[4];
#pragma unroll
  for (int n = 0; n < 4; ++n) ja[n] = (8 * q + qq) * 128 + (((2 * n + (pp >> 1)) ^ ((qq & 2) | ((q & 1) << 2))) * 16) + 8 * (pp & 1) + hw * 4096;
  const bf16x8 ones = {0x3f80, 0x3f80, 0x3f80, 0x3f80, 0x3f80, 0x3f80, 0x3f80, 0x3f80};
  const f32x4 z4 = {0.f, 0.f, 0.f, 0.f};
  f32x4 cs[7] = {z4, z4, z4, z4, z4, z4, z4};
  const int srow = 64 * hw + 32 * (lane >> 5) + 8 * qq + 4 * (q & 1) + (lane & 3);
  const char* stp = ws + WS_ROWST + (size_t)(c.rp * 128 + srow) * 8;
  wait_lgkm0();
  bar();
  auto csum = [&](int s) {
    LAS char* ba = lds + (4 + (s & 3)) * SLOT;
    bf16x8 f[7];
#pragma unroll
    for (int n = 0; n < 3; ++n) f[n] = ldsb128(ba + n * 2048 + wa);
    if (typeB) {
#pragma unroll
      for (int n = 3; n < 7; ++n) f[n] = ldsb128(ba + n * 2048 + wa);
    } else {
#pragma unroll
      for (int n = 3; n < 7; ++n) f[n] = ldstr(ba + 6144 + ja[n - 3], 512);
    }
#pragma unroll
    for (int n = 0; n < 7; ++n) cs[n] = MFMA(ones, f[n], cs[n]);
  };
  u64 stv = 0;
#pragma unroll 1
  for (int s = 0; s < 7; ++s) {
    if (s == 4) stv = ld8_async(stp);
    csum(s);
    wait_lgkm0();
    bar();
  }
  {
    csum(7);
    wait_vm<0>();
    asm volatile("" : "+v"(stv));
    const unsigned t32 = (unsigned)tag;
    if (!__all((unsigned)(stv >> 32) == t32)) stv = poll(stp, [&](u64 x) { return (unsigned)(x >> 32) == t32; });
    *(LAS float*)(lds + LDS_MEAN + (64 * hw + lane) * 4) = bflo((unsigned)stv);
    *(LAS float*)(lds + LDS_RSTD + (64 * hw + lane) * 4) = __builtin_amdgcn_rcpf(bfhi((unsigned)stv));
    if (lane < 16) {
#pragma unroll
      for (int n = 0; n < 7; ++n) *(LAS float*)(lds + LDS_CS + ((hw * 11 + n) * 16 + lane) * 4) = cs[n][0];
    }
    wait_lgkm0();
    bar();
  }
#pragma unroll
  for (int n = 0; n < 4; ++n) cs[n] = z4;
#pragma unroll 1
  for (int h = 0; h < 2; ++h) {
#pragma unroll
    for (int j = 0; j < 2; ++j) {
      LAS char* ba = lds + ((h | j) == 0 ? 8 : 3 + 2 * h + j) * SLOT;
#pragma unroll
      for (int k = 0; k < 2; ++k)
#pragma unroll
        for (int n = 0; n < 4; ++n) cs[n] = MFMA(ones, ldstr(ba + ja[n] + k * 8192, 512), cs[n]);
    }
    if (h == 1 && lane < 16) {
#pragma unroll
      for (int n = 0; n < 4; ++n) *(LAS float*)(lds + LDS_CS + ((hw * 11 + 7 + n) * 16 + lane) * 4) = cs[n][0];
    }
    wait_lgkm0();
    bar();
  }
#pragma unroll 1
  for (int s = 0; s < 30; ++s) bar();
}

DEV void consumer(const Ctx& c) {
  const int w = c.wid, lane = c.lane, wid = c.wid;
  TR_DECL;
  LAS char* lds = c.lds; char* ws = c.ws;
  const int q = lane >> 4, cc = lane & 15, qq = cc >> 2, pp = lane & 3;
  const bool typeB = c.ec == 3;
  const int G = c.cp >> 2, v = c.cp & 3, E0 = 16 * G + 3 * v, PA = 7 * G + 2 * v, PB = typeB ? PA : PA + 1;
  const auto rs = __builtin_amdgcn_make_buffer_rsrc(ws, 0, WS_BYTES, 0x00020000);
  STAMP(0);
  const unsigned wa0 = cc * 128 + ((q ^ (cc & 7)) * 16), wa1 = cc * 128 + (((4 + q) ^ (cc & 7)) * 16);
  unsigned ja[4];
#pragma unroll
  for (int n = 0; n < 4; ++n) ja[n] = opq(4 * SLOT + (8 * q + qq) * 128 + (((2 * n + (pp >> 1)) ^ ((qq & 2) | ((q & 1) << 2))) * 16) + 8 * (pp & 1));
  bf16x8 xf[2][16];
  auto h_epi = [&](f32x4 (&acc)[4][2], int brow, int P, int csrow) {
    float mean[2], rstd[2];
#pragma unroll
    for (int u = 0; u < 2; ++u) { mean[u] = *(const LAS float*)(lds + LDS_MEAN + ((2 * w + u) * 16 + cc) * 4); rstd[u] = *(const LAS float*)(lds + LDS_RSTD + ((2 * w + u) * 16 + cc) * 4); }
    f32x4 cs[4], bv[4];
#pragma unroll
    for (int n = 0; n < 4; ++n) {
      cs[n] = *(const LAS f32x4*)(lds + LDS_CS + ((csrow + n) * 16 + 4 * q) * 4) + *(const LAS f32x4*)(lds + LDS_CS + ((11 + csrow + n) * 16 + 4 * q) * 4);
      bv[n] = *(const LAS f32x4*)(lds + LDS_B1 + (brow * 64 + n * 16 + 4 * q) * 4);
    }
    const unsigned hb = WS_H + (unsigned)((((2 * c.rp + (w >> 1)) * 56 + 2 * P) * 4 + 2 * (w & 1)) * 1024);
#pragma unroll
    for (int b = 0; b < 2; ++b)
#pragma unroll
      for (int u = 0; u < 2; ++u) {
        unsigned d[4];
#pragma unroll
        for (int h = 0; h < 2; ++h) {
          const int n = 2 * b + h;
          f32x4 z = (acc[n][u] - mean[u] * cs[n]) * rstd[u] + bv[n];
          z = __builtin_elementwise_max(z, (f32x4){0.f, 0.f, 0.f, 0.f});
          z = z * z;
          d[2 * h] = pk_bf16(z[0], z[1]); d[2 * h + 1] = pk_bf16(z[2], z[3]);
        }
        __builtin_amdgcn_raw_buffer_store_b128((u32x4){d[0], d[1], d[2], d[3]}, rs, lane * 16 + u * 1024 + hb + b * 4096, 0, 16);
      }
  };
  constexpr int L2 = 8;
  bf16x8 g[64];
  auto rd2 = [&](auto t_) {
    constexpr int t = decltype(t_)::value, j = t / 16, k = (t / 4) & 3, n = t & 3;
    if constexpr (j == 0) g[t] = ldstr(lds + 4 * SLOT + ja[n] + k * 4096, 512); else g[t] = ldstr(lds + (j - 1) * SLOT + ja[n] + k * 4096, 512);
  };
  bar();
  STAMP(1);
  auto pass1 = [&](auto na_) {
    constexpr int NA = decltype(na_)::value;
    f32x4 acc[7][2];
#pragma unroll
    for (int n = 0; n < 7; ++n) { acc[n][0] = (f32x4){0, 0, 0, 0}; acc[n][1] = (f32x4){0, 0, 0, 0}; }
    bf16x8 fr[112];
    const unsigned bx0 = opq(wa0 + w * 4096), bx1 = opq(wa1 + w * 4096), bw0 = opq(wa0 + 4 * SLOT), bw1 = opq(wa1 + 4 * SLOT);
    auto rd = [&](auto t_) {
      constexpr int t = decltype(t_)::value, kk = t / 7, n = t % 7, so = ((kk >> 1) & 3) * SLOT;
      if constexpr (n == 0) { xf[0][kk] = ldsb128(lds + ((kk & 1) ? bx1 : bx0) + so); xf[1][kk] = ldsb128(lds + ((kk & 1) ? bx1 : bx0) + so + 2048); }
      if constexpr (n < NA) fr[t] = ldsb128(lds + ((kk & 1) ? bw1 : bw0) + so + n * 2048);
      else fr[t] = ldstr(lds + ja[(n - 3) & 3] + so + 6144 + (kk & 1) * 4096, 512);
    };
    static_for<0, 7>(rd);
    SCHED();
    static_for<0, 112>([&](auto p_) {
      constexpr int p = decltype(p_)::value, kk = p / 7, n = p % 7;
      constexpr int kn = p / 14 + 1;
      if constexpr (p == 14 * kn - lead1(kn)) {
        TR(1);
        wait_lgkm0();
        TR(1);
        bar();
        TR(1);
        SCHED();
      }
      const bf16x8 f = fr[p];
      if constexpr (n < NA) acc[n][0] = MFMA(xf[0][kk], f, acc[n][0]); else acc[n][0] = MFMA(f, xf[0][kk], acc[n][0]);
      SCHED();
      static_for<p + 1, p + 15>([&](auto t_) {
        constexpr int t = decltype(t_)::value;
        if constexpr (t < 112 && t >= 7 && sched1(t) == p) { rd(t_); SCHED(); }
      });
      if constexpr (n < NA) acc[n][1] = MFMA(xf[1][kk], f, acc[n][1]); else acc[n][1] = MFMA(f, xf[1][kk], acc[n][1]);
      SCHED();
    });
    STAMP(2);
    f32x4 mr[2], rr[2];
    const float sgn = E0 >= 32 ? -1.f : 1.f;
#pragma unroll
    for (int u = 0; u < 2; ++u) {
      mr[u] = *(const LAS f32x4*)(lds + LDS_MEAN + ((2 * w + u) * 16 + 4 * q) * 4);
      rr[u] = *(const LAS f32x4*)(lds + LDS_RSTD + ((2 * w + u) * 16 + 4 * q) * 4) * sgn;
    }
    float cs[NA];
#pragma unroll
    for (int n = 0; n < NA; ++n) cs[n] = *(const LAS float*)(lds + LDS_CS + (n * 16 + cc) * 4) + *(const LAS float*)(lds + LDS_CS + ((11 + n) * 16 + cc) * 4);
#pragma unroll
    for (int n = 0; n < NA; ++n) {
      const f32x4 o0 = (acc[n][0] - mr[0] * cs[n]) * rr[0], o1 = (acc[n][1] - mr[1] * cs[n]) * rr[1];
      u32x4 d = {pk_bf16(o0[0], o0[1]), pk_bf16(o0[2], o0[3]), pk_bf16(o1[0], o1[1]), pk_bf16(o1[2], o1[3])};
      const int E = E0 + n;
      __builtin_amdgcn_raw_buffer_store_b128(d, rs, lane * 16 + WS_ABT + (unsigned)((((E >> 3) * 64 + c.rp * 4 + w) * 8 + (E & 7)) * 1024), 0, 16);
    }
    SCHED();
    if (NA == 3) {
      f32x4 (&ah)[4][2] = *(f32x4(*)[4][2]) & acc[3];
      h_epi(ah, 0, PA, 3);
    }
  };
  if (typeB) pass1(IC<7>{}); else pass1(IC<3>{});
  SCHED();
  static_for<0, L2>(rd2);
  const u64 tag = uniform64(*(const LAS u64*)(lds + LDS_TAG));
  STAMP(3);
  {
    f32x4 acc[4][2];
#pragma unroll
    for (int n = 0; n < 4; ++n) { acc[n][0] = (f32x4){0, 0, 0, 0}; acc[n][1] = (f32x4){0, 0, 0, 0}; }
    SCHED();
    static_for<0, 64>([&](auto p_) {
      constexpr int p = decltype(p_)::value, kk = p / 4, n = p & 3;
      if constexpr (p == 16) {
        wait_vm<0>();
        bar();
        if (w == 0 && lane == 0) set_flag(ws, 0, c.rp * 16 + c.cp, tag);
        SCHED();
      }
      if constexpr (p == 32) { bar(); SCHED(); }
      acc[n][0] = MFMA(g[p], xf[0][kk], acc[n][0]);
      SCHED();
      if constexpr (p + L2 < 64) { rd2(IC<p + L2>{}); SCHED(); }
      acc[n][1] = MFMA(g[p], xf[1][kk], acc[n][1]);
      SCHED();
    });
    STAMP(11);
    h_epi(acc, 1, PB, 7);
  }
  STAMP(4);
  const int wt = w >> 1, we = w & 1;
  unsigned wb[2];
#pragma unroll
  for (int n = 0; n < 2; ++n) wb[n] = 4 * SLOT + (4 * q + qq) * 128 + (((2 * (2 * we + n) + (pp >> 1)) ^ ((4 * q + qq) & 6)) * 16) + 8 * (pp & 1);
  const unsigned hb = wt * 2048 + lane * 16;
  bf16x8 hA[4][2], wA[4][2], hB[4][2], wB[4][2];
  wait_lgkm0();
  bar();
  STAMP(5);
#define PREAD(HN, WN, SO) if (i < 12) { if (i % 3 == 2) { HN[i / 3][0] = ldsb128(lds + (SO) + hb + (i / 3) * 4096); HN[i / 3][1] = ldsb128(lds + (SO) + hb + (i / 3) * 4096 + 1024); } \
                                        else WN[i / 3][i % 3] = ldstr(lds + (SO) + wb[i % 3] + (i / 3) * 4096, 2048); SCHED(); }
  {
    f32x4 acc[4][4];
#pragma unroll
    for (int m = 0; m < 4; ++m)
#pragma unroll
      for (int n = 0; n < 4; ++n) acc[m][n] = (f32x4){0, 0, 0, 0};
    const unsigned a0 = wt * 8192 + wa0, a1 = wt * 8192 + wa1, b0 = we * 4096 + lane * 16;
    bf16x8 fa0[4], fb0[4], fa1[4], fb1[4];
#pragma unroll
    for (int m = 0; m < 4; ++m) { fa0[m] = ldsb128(lds + a0 + m * 2048); fb0[m] = ldsb128(lds + b0 + 7 * SLOT + m * 1024); }
#define HALF(FA, FB, NA_, NB_, AOFF, BOFF) do { _Pragma("unroll") for (int i = 0; i < 16; ++i) { \
      acc[i >> 2][i & 3] = MFMA(FB[i & 3], FA[i >> 2], acc[i >> 2][i & 3]); SCHED(); \
      if (i < 8) { if (i == 0) NB_[0] = ldsb128(lds + (BOFF)); else if (i == 1) NA_[0] = ldsb128(lds + (AOFF)); \
        else if (i < 5) NB_[i - 1] = ldsb128(lds + (BOFF) + (i - 1) * 1024); else NA_[i - 4] = ldsb128(lds + (AOFF) + (i - 4) * 2048); SCHED(); } } } while (0)
    unsigned so = 0, sb = 7 * SLOT, bseq = 0x76548;
#pragma unroll 1
    for (int seg = 0; seg < 2; ++seg) {
      const int n = seg ? 12 : 3;
#pragma unroll 1
      for (int s = 0; s < n; ++s) {
        HALF(fa0, fb0, fa1, fb1, so + a1, sb + b0 + 8192);
        TR(2);
        wait_lgkm0();
        TR(2);
        bar();
        TR(2);
        sb = (bseq & 15) * SLOT; bseq = (bseq >> 4) | ((bseq & 15) << 16); so = (so + SLOT) & (4 * SLOT - 1);
        HALF(fa1, fb1, fa0, fb0, so + a0, sb + b0);
      }
      if (seg == 0) {
        wait_vm<0>();
        unsigned old = 0;
        if (lane == 0) old = __hip_atomic_fetch_add((LAS unsigned*)(lds + LDS_TAG + 8), 1u, __ATOMIC_RELAXED, __HIP_MEMORY_SCOPE_WORKGROUP);
        if (lane == 0 && old == 3) set_flag(ws, 1, c.rp * 16 + c.cp, tag);
      }
    }
    HALF(fa0, fb0, fa1, fb1, so + a1, sb + b0 + 8192);
    wait_lgkm0();
    bar();
#pragma unroll
    for (int i = 0; i < 16; ++i) {
      acc[i >> 2][i & 3] = MFMA(fb1[i & 3], fa1[i >> 2], acc[i >> 2][i & 3]); SCHED();
      PREAD(hA, wA, 0)
    }
    STAMP(6);
    if (w == c.ks) {
#pragma unroll
      for (int m = 0; m < 4; ++m)
#pragma unroll
        for (int n = 0; n < 4; ++n) *(LAS f32x4*)(lds + 8 * SLOT + (m * 4 + n) * 1024 + lane * 16) = acc[m][n];
    } else {
      const unsigned yb = WS_YP + (unsigned)((((c.tp * 4 + c.ec) * 4 + c.ks) * 4 + w) * 8192);
#pragma unroll
      for (int m = 0; m < 4; ++m)
#pragma unroll
        for (int j = 0; j < 2; ++j) {
          const f32x4 e = acc[m][2 * j], o = acc[m][2 * j + 1];
          __builtin_amdgcn_raw_buffer_store_b128((u32x4){pk_bf16(e[0], e[1]), pk_bf16(e[2], e[3]), pk_bf16(o[0], o[1]), pk_bf16(o[2], o[3])}, rs, lane * 16 + (m * 2 + j) * 1024 + yb, 0, 16);
        }
    }
  }
  STAMP(7);
  {
    f32x4 acc[2][2];
#pragma unroll
    for (int m = 0; m < 2; ++m) { acc[m][0] = (f32x4){0, 0, 0, 0}; acc[m][1] = (f32x4){0, 0, 0, 0}; }
    u32x4 yp[3][2];
    u32x2 xr[2][2], b2r[2];
    const int trow = c.tp * 128 + (c.ks >> 1) * 64 + 32 * wt + 8 * qq + (cc & 3);
    const int ecol = c.ec * 128 + (c.ks & 1) * 64 + 32 * we + 4 * q;
    unsigned so = 0;
#define PSTEP(HC, WC, HN, WN) do { wait_lgkm0(); TR(3); bar(); TR(3); so = (so + SLOT) & (4 * SLOT - 1); SCHED(); \
      _Pragma("unroll") for (int i = 0; i < 16; ++i) { \
        acc[(i >> 1) & 1][i & 1] = MFMA(WC[i >> 2][i & 1], HC[i >> 2][(i >> 1) & 1], acc[(i >> 1) & 1][i & 1]); SCHED(); \
        PREAD(HN, WN, so) } } while (0)
#pragma unroll 1
    for (int seg = 0; seg < 2; ++seg) {
#pragma unroll 1
      for (int s = 0; s < 2; ++s) { PSTEP(hA, wA, hB, wB); PSTEP(hB, wB, hA, wA); }
      if (seg == 0) {
        wait_vm<0>();
        unsigned old = 0;
        if (lane == 0) old = __hip_atomic_fetch_add((LAS unsigned*)(lds + LDS_TAG + 12), 1u, __ATOMIC_RELAXED, __HIP_MEMORY_SCOPE_WORKGROUP);
        if (lane == 0 && old == 3) set_flag(ws, 2, (c.tp * 4 + c.ec) * 4 + c.ks, tag);
      }
    }
    PSTEP(hA, wA, hB, wB);
    STAMP(8);
    {
      const unsigned yb = WS_YP + (unsigned)(((c.tp * 4 + c.ec) * 16 + c.ks) * 8192);
#pragma unroll
      for (int i = 0; i < 3; ++i) {
        const int src = (c.ks + 1 + i) & 3;
#pragma unroll
        for (int m = 0; m < 2; ++m) yp[i][m] = __builtin_amdgcn_raw_buffer_load_b128(rs, lane * 16 + ((2 * wt + m) * 2 + we) * 1024, yb + src * 32768, 16);
      }
#pragma unroll
      for (int m = 0; m < 2; ++m)
#pragma unroll
        for (int n = 0; n < 2; ++n) xr[m][n] = *(const u32x2*)(c.x + (size_t)(trow + 4 * m) * 1024 + (ecol + 16 * n) * 2);
#pragma unroll
      for (int n = 0; n < 2; ++n) b2r[n] = *(const u32x2*)(c.b2 + (ecol + 16 * n) * 2);
    }
    SCHED();
    PSTEP(hB, wB, hA, wA);
    PSTEP(hA, wA, hB, wB);
    PSTEP(hB, wB, hA, wA);
    PSTEP(hA, wA, hB, wB);
    f32x4 yo[2][2];
#pragma unroll
    for (int m = 0; m < 2; ++m)
#pragma unroll
      for (int n = 0; n < 2; ++n) yo[m][n] = *(const LAS f32x4*)(lds + 8 * SLOT + ((2 * wt + m) * 4 + 2 * we + n) * 1024 + lane * 16);
#pragma unroll
    for (int i = 0; i < 16; ++i) acc[(i >> 1) & 1][i & 1] = MFMA(wB[i >> 2][i & 1], hB[i >> 2][(i >> 1) & 1], acc[(i >> 1) & 1][i & 1]);
    STAMP(9);
#pragma unroll
    for (int m = 0; m < 2; ++m)
#pragma unroll
      for (int n = 0; n < 2; ++n) {
        f32x4 y = yo[m][n];
#pragma unroll
        for (int i = 0; i < 3; ++i) y += (f32x4){bflo(yp[i][m][2 * n]), bfhi(yp[i][m][2 * n]), bflo(yp[i][m][2 * n + 1]), bfhi(yp[i][m][2 * n + 1])};
        const float bv[4] = {bflo(b2r[n].x), bfhi(b2r[n].x), bflo(b2r[n].y), bfhi(b2r[n].y)};
        const float xv[4] = {bflo(xr[m][n].x), bfhi(xr[m][n].x), bflo(xr[m][n].y), bfhi(xr[m][n].y)};
        float o[4];
#pragma unroll
        for (int r = 0; r < 4; ++r) {
          const float z = acc[m][n][r] + bv[r];
          const float g = z * __builtin_amdgcn_rcpf(1.f + __expf(-z));
          o[r] = xv[r] + y[r] * g;
        }
        u32x2 d = {pk_bf16(o[0], o[1]), pk_bf16(o[2], o[3])};
        *(u32x2*)(c.out + (size_t)(trow + 4 * m) * 1024 + (ecol + 16 * n) * 2) = d;
      }
    STAMP(10);
    TR_END();
  }
}

__global__ __launch_bounds__(512, 2) void fnet_mlp_fused(const char* x, const char* Cr, const char* Ci, const char* Wr, const char* Wi,
                                                         const char* w1, const char* b1, const char* w2, const char* b2, char* out, char* ws) {
  extern __shared__ __attribute__((aligned(16))) char shm[];
  Ctx c;
  c.x = x; c.Cr = Cr; c.Ci = Ci; c.Wr = Wr; c.Wi = Wi; c.w1 = w1; c.b1 = b1; c.w2 = w2; c.b2 = b2; c.out = out; c.ws = ws;
  c.lds = (LAS char*)shm;
  c.wid = __builtin_amdgcn_readfirstlane(threadIdx.x >> 6); c.lane = threadIdx.x & 63;
  const int b = blockIdx.x, xcd = b & 7, r = b >> 3;
  c.ks = xcd >> 1; c.tp = (xcd & 1) * 8 + (r >> 2); c.ec = r & 3;
  c.rp = c.tp; c.cp = c.ks * 4 + c.ec;
  if (c.wid >= 6) helper(c); else if (c.wid >= 4) loader(c); else consumer(c);
}

extern "C" void kernel_launch(void* const* d_in, const int* in_sizes, int n_in, void* d_out, int out_size, void* d_ws, size_t ws_size, hipStream_t stream) {
  (void)hipFuncSetAttribute((const void*)fnet_mlp_fused, hipFuncAttributeMaxDynamicSharedMemorySize, LDS_BYTES);
  fnet_mlp_fused<<<256, 512, LDS_BYTES, stream>>>((const char*)d_in[0], (const char*)d_in[1], (const char*)d_in[2], (const char*)d_in[3], (const char*)d_in[4],
                                                  (const char*)d_in[6], (const char*)d_in[7], (const char*)d_in[8], (const char*)d_in[9], (char*)d_out, (char*)d_ws);
}
```

```cpp
#define WSOFF(n) (((n) - (32u << 20)) & ~(size_t)4095)
#define PREFC 0
#define PREFS 0
#include <hip/hip_runtime.h>
typedef unsigned short u16;
typedef unsigned long long u64;
typedef short bf16x8 __attribute__((ext_vector_type(8)));
typedef short s16x4 __attribute__((ext_vector_type(4)));
typedef float f32x4 __attribute__((ext_vector_type(4)));
typedef float f32x2 __attribute__((ext_vector_type(2)));
typedef unsigned u32x4 __attribute__((ext_vector_type(4)));
typedef unsigned u32x2 __attribute__((ext_vector_type(2)));
typedef __bf16 bf2 __attribute__((ext_vector_type(2)));
#define LAS __attribute__((address_space(3)))
#define GAS __attribute__((address_space(1)))
#define DEV __device__ __forceinline__

constexpr int S_ = 2048, D_ = 512, F_ = 1792;
constexpr unsigned WS_EPOCH = 0, WS_FLAG = 4096, FLAG_STRIDE = 128, WS_ROWST = 256u << 10;
constexpr unsigned WS_ABT = 1u << 20, WS_H = 8u << 20, WS_YP = 16u << 20, WS_BYTES = 32u << 20;
constexpr int SLOT = 16384, NSLOT = 9;
#define LEADE 6
#define LOOKE 13
#define LEADL 5
#define LOOKL 10
constexpr int LDS_MEAN = NSLOT * SLOT, LDS_RSTD = LDS_MEAN + 512, LDS_CS = LDS_RSTD + 512;
constexpr int LDS_B1 = LDS_CS + 22 * 64;
constexpr int LDS_TAG = LDS_B1 + 512;
constexpr int LDS_BYTES = LDS_TAG + 16;

template <int N> DEV void wait_vm() { asm volatile("s_waitcnt vmcnt(%0)" ::"n"(N) : "memory"); }
DEV void wait_lgkm0() { asm volatile("s_waitcnt lgkmcnt(0)" ::: "memory"); }
DEV void bar() { asm volatile("" ::: "memory"); __builtin_amdgcn_s_barrier(); asm volatile("" ::: "memory"); }
#define SCHED() __builtin_amdgcn_sched_barrier(0)
#define MFMA(a, b, c) __builtin_amdgcn_mfma_f32_16x16x32_bf16((a), (b), (c), 0, 0, 0)
DEV unsigned pk_bf16(float a, float b) { f32x2 f = {a, b}; return __builtin_bit_cast(unsigned, __builtin_convertvector(f, bf2)); }
DEV float rbf(float a) { return __uint_as_float(pk_bf16(a, 0.f) << 16); }
DEV float bflo(unsigned v) { return __uint_as_float(v << 16); }
DEV float bfhi(unsigned v) { return __uint_as_float(v & 0xffff0000u); }
DEV bf16x8 ldsb128(LAS char* p) { return *(const LAS bf16x8*)p; }
DEV bf16x8 ldstr(LAS char* p, int second) {
  s16x4 a = __builtin_amdgcn_ds_read_tr16_b64_v4i16((LAS s16x4*)p);
  s16x4 b = __builtin_amdgcn_ds_read_tr16_b64_v4i16((LAS s16x4*)(p + second));
  return __builtin_shufflevector(a, b, 0, 1, 2, 3, 4, 5, 6, 7);
}
struct Frag2 { u32x2 p[4]; };
DEV void rd2_b64x4(unsigned a0, unsigned a1, Frag2& f) {
  asm volatile("ds_read_b64 %0, %4\n\tds_read_b64 %1, %4 offset:8\n\tds_read_b64 %2, %5\n\tds_read_b64 %3, %5 offset:8"
               : "=&v"(f.p[0]), "=&v"(f.p[1]), "=&v"(f.p[2]), "=&v"(f.p[3]) : "v"(a0), "v"(a1) : "memory");
}
DEV void rd2_tr(unsigned a, Frag2& f) {
  asm volatile("ds_read_b64_tr_b16 %0, %4\n\tds_read_b64_tr_b16 %1, %4 offset:512\n\tds_read_b64_tr_b16 %2, %4 offset:4096\n\tds_read_b64_tr_b16 %3, %4 offset:4608"
               : "=&v"(f.p[0]), "=&v"(f.p[1]), "=&v"(f.p[2]), "=&v"(f.p[3]) : "v"(a) : "memory");
}
DEV void pin(Frag2& f) { asm volatile("" : "+v"(f.p[0]), "+v"(f.p[1]), "+v"(f.p[2]), "+v"(f.p[3])); }
DEV bf16x8 fr_of(const Frag2& f, int i) { return __builtin_bit_cast(bf16x8, (u32x4){f.p[2 * i].x, f.p[2 * i].y, f.p[2 * i + 1].x, f.p[2 * i + 1].y}); }
DEV unsigned opq(unsigned v) { asm volatile("" : "+v"(v)); return v; }
DEV void glds(const char* src, LAS char* dst) { __builtin_amdgcn_global_load_lds((const unsigned*)src, (LAS unsigned*)dst, 16, 0, 0); }
#define AUXC 2
#define AUXP1 0
#define AUXW2 0
template <int AUX> DEV void gldsa(const char* src, LAS char* dst) { __builtin_amdgcn_global_load_lds((const unsigned*)src, (LAS unsigned*)dst, 16, 0, AUX); }
DEV void glds_h(const char* src, LAS char* dst) { __builtin_amdgcn_global_load_lds((const unsigned*)src, (LAS unsigned*)dst, 16, 0, 16); }
DEV u64 ld8_async(const char* p) { u64 v; asm volatile("global_load_dwordx2 %0, %1, off sc1" : "=v"(v) : "v"(p) : "memory"); return v; }
DEV u32x4 ld16_async(const char* sbase, unsigned voff) { u32x4 v; asm volatile("global_load_dwordx4 %0, %1, %2" : "=v"(v) : "v"(voff), "s"(sbase) : "memory"); return v; }
template <int OFF> DEV void ds_w128(unsigned a, u32x4 v) { asm volatile("ds_write_b128 %0, %1 offset:%2" ::"v"(a), "v"(v), "n"(OFF) : "memory"); }
template <int CTRL> DEV float dpp_add(float x) { return x + __builtin_bit_cast(float, __builtin_amdgcn_update_dpp(0, __builtin_bit_cast(int, x), CTRL, 0xf, 0xf, false)); }
DEV float sum16(float x) { x = dpp_add<0xB1>(x); x = dpp_add<0x4E>(x); x = dpp_add<0x141>(x); return dpp_add<0x140>(x); }
DEV float sum32(float x) {
  x = dpp_add<0xB1>(x); x = dpp_add<0x4E>(x); x = dpp_add<0x141>(x); x = dpp_add<0x140>(x);
  const auto r = __builtin_amdgcn_permlane16_swap(__float_as_uint(x), __float_as_uint(x), false, false);
  return __uint_as_float(r[0]) + __uint_as_float(r[1]);
}

#define STAMP(i) do {} while (0)
#define TR_DECL
#define TR(ph) do {} while (0)
#define TR_END() do {} while (0)

template <int N> struct IC { static constexpr int value = N; };
template <int B, int E, class F> DEV void static_for(F&& f) { if constexpr (B < E) { f(IC<B>{}); static_for<B + 1, E>(f); } }
constexpr int lead1(int k) { return k >= 7 ? LEADL : LEADE; }
constexpr int sched1(int t) { const int k = t / 14, e0 = t - (k >= 7 ? LOOKL : LOOKE), e1 = 14 * k - lead1(k) + (t - 14 * k) / 2; return k == 0 ? (t - 7 > 0 ? (t - 7) / 2 : 0) : (e0 > e1 ? e0 : e1); }
struct Ctx {
  const char *x, *Cr, *Ci, *Wr, *Wi, *w1, *b1, *w2, *b2; char* out; char* ws; LAS char* lds;
  int wid, lane, rp, cp, tp, ec, ks;
};

template <class P> DEV u64 poll(const char* p, P ok) {
  const u64 t0 = __builtin_amdgcn_s_memrealtime();
  u64 v;
  for (;;) {
    v = __hip_atomic_load((GAS u64*)p, __ATOMIC_RELAXED, __HIP_MEMORY_SCOPE_AGENT);
    if (__all(ok(v))) break;
    if (__builtin_amdgcn_s_memrealtime() - t0 > 20000000ull) break;
    __builtin_amdgcn_s_sleep(1);
  }
  return v;
}
DEV void set_flag(char* ws, int kind, int idx, u64 tag) {
  __hip_atomic_store((GAS u64*)(ws + WS_FLAG + (unsigned)(kind * 256 + idx) * FLAG_STRIDE), tag, __ATOMIC_RELAXED, __HIP_MEMORY_SCOPE_AGENT);
}
DEV u64 uniform64(u64 v) {
  const unsigned lo = __builtin_amdgcn_readfirstlane((unsigned)v), hi = __builtin_amdgcn_readfirstlane((unsigned)(v >> 32));
  return ((u64)hi << 32) | lo;
}

DEV void loader(const Ctx& c) {
  const int lw = c.wid - 4, lane = c.lane, wid = c.wid;
  TR_DECL;
  LAS char* lds = c.lds; char* ws = c.ws;
  const bool typeB = c.ec == 3;
  const int G = c.cp >> 2, v = c.cp & 3;
  const int E0 = 16 * G + 3 * v;
  const int PA = 7 * G + 2 * v;
  const int PB = typeB ? PA : PA + 1;
  const int r8 = lane >> 3, p8 = lane & 7;
  const int trow0 = 64 * lw + 8 * (r8 >> 2) + (r8 & 3);
#define PROW(i) (32 * ((i) >> 2) + 16 * ((i) & 1) + 4 * (((i) >> 1) & 1))
  const unsigned swz = (unsigned)((p8 ^ r8) * 16);
  const unsigned swzJ0 = (unsigned)((p8 ^ (r8 & 2)) * 16), swzJ1 = (unsigned)((p8 ^ (r8 & 2) ^ 4) * 16);
  LAS char* ld = lds + lw * 8192;
  const char* Xl = c.x + (size_t)(c.rp * 128 + trow0) * 1024 + swz;
  auto issueX = [&](int s) {
#pragma unroll
    for (int i = 0; i < 8; ++i) gldsa<AUXP1>(Xl + PROW(i) * 1024 + s * 128, ld + (s & 3) * SLOT + i * 1024);
  };
  issueX(0);
  const char* pB[8]; unsigned strB[8];
  {
    const char* Wm = (E0 >= 32 ? c.Wi : c.Wr) + (size_t)(E0 & 31) * 16384;
#pragma unroll
    for (int i = 0; i < 8; ++i) {
      const int p = lw * 8 + i;
      const bool isJ = p >= 14, isW = !isJ && (typeB || p < 6);
      const int pr = isJ ? p - 14 : p - 6;
      const char* b1_ = c.w1 + (size_t)(pr * 8) * (F_ * 2) + (isJ ? PB : PA) * 128;
      const char* bw_ = Wm + (size_t)p * 8192;
      pB[i] = (isW ? bw_ : b1_) + (isW ? r8 * 1024 + swz : r8 * (F_ * 2) + ((i & 1) ? swzJ1 : swzJ0));
      strB[i] = isW ? 128 : (isJ ? 16 * F_ * 2 : 64 * F_ * 2);
    }
  }
  auto issueB1 = [&](int s) {
#pragma unroll
    for (int i = 0; i < 8; ++i) {
      LAS char* d = (i >= 6 && lw == 1) ? lds + 8 * SLOT + (i - 6) * 1024 + s * 2048 : ld + (4 + (s & 3)) * SLOT + i * 1024;
      gldsa<AUXP1>(pB[i], d);
      pB[i] += strB[i];
    }
  };
  issueB1(0); issueX(1); issueB1(1);
  const char* Jl = c.w1 + PB * 128 + (size_t)(64 * lw + r8) * (F_ * 2);
  const char* Cl = (c.ks >> 1 ? c.Ci : c.Cr) + (size_t)(c.tp * 128 + trow0) * 4096 + (c.ks & 1) * 2048 + swz;
  const char* ABb = ws + WS_ABT + (size_t)(((c.ks >> 1) * 4 + c.ec) * 64 + (c.ks & 1) * 32) * 8192 + lw * 8192 + lane * 16;
  const char* Hb = ws + WS_H + (size_t)(2 * c.tp + (c.ks >> 1)) * 56 * 4096 + lw * 8192 + lane * 16;
  const char* W2l = c.w2 + (c.ec * 128 + (c.ks & 1) * 64) * 2 + (64 * lw + r8) * 1024 + (p8 ^ (r8 & 6)) * 16;
  auto issueJ = [&](int j) {
#pragma unroll
    for (int i = 0; i < 8; ++i) gldsa<AUXP1>(Jl + (j * 128 + i * 8) * (F_ * 2) + ((i & 1) ? swzJ1 : swzJ0), ld + (3 + j) * SLOT + i * 1024);
  };
  auto issueC = [&](int s) {
#pragma unroll
    for (int i = 0; i < 8; ++i) gldsa<AUXC>(Cl + PROW(i) * 4096 + s * 128, ld + (s & 3) * SLOT + i * 1024);
  };
  auto issueAB = [&](int s, int slot) {
#pragma unroll
    for (int i = 0; i < 8; ++i) glds_h(ABb + s * 16384 + i * 1024, ld + slot * SLOT + i * 1024);
  };
  auto issueH = [&](int s) {
#pragma unroll
    for (int i = 0; i < 8; ++i) glds_h(Hb + s * 16384 + i * 1024, ld + (s & 3) * SLOT + i * 1024);
#pragma unroll
    for (int i = 0; i < 8; ++i) gldsa<AUXW2>(W2l + i * 8192 + s * 131072, ld + (4 + (s & 3)) * SLOT + i * 1024);
  };
  wait_vm<16>();
  bar();
  issueX(2); issueB1(2);
#pragma unroll 1
  for (int s = 0; s < 5; ++s) { issueX(s + 3); issueB1(s + 3); TR(1); wait_vm<32>(); TR(1); bar(); TR(1); }
#pragma unroll 1
  for (int s = 5; s < 7; ++s) { issueJ(s - 4); issueC(s - 5); TR(1); wait_vm<32>(); TR(1); bar(); TR(1); }
  issueJ(3); issueC(2);
  wait_vm<40>(); bar();
  issueC(3);
  const u64 tag = uniform64(*(const LAS u64*)(lds + LDS_TAG));
  wait_vm<16>(); bar();
  bar();
  STAMP(1);
  {
    const int G2 = 2 * (c.ks >> 1) + (c.ec >> 1), m = lane & 3;
    const bool act = lane < 32 && ((c.ec & 1) ? (m >= 2) : (m <= 2));
    poll(ws + WS_FLAG + (unsigned)(0 * 256 + (8 * (c.ks & 1) + ((lane >> 2) & 7)) * 16 + 4 * G2 + m) * FLAG_STRIDE, [&](u64 x) { return !act || x == tag; });
  }
  STAMP(2);
  issueAB(0, 7); issueAB(1, 8); issueAB(2, 4);
  wait_vm<16>();
  bar();
  STAMP(3);
  issueAB(3, 5); issueAB(4, 6);
  wait_vm<24>(); bar(); issueC(4); issueAB(5, 7);
  wait_vm<32>(); bar(); issueC(5); issueAB(6, 8);
  const char* hfp = ws + WS_FLAG + (unsigned)(1 * 256 + c.tp * 16 + (lane & 15)) * FLAG_STRIDE;
  u64 fv = 0;
  unsigned bseq = 0x87654;
#pragma unroll 1
  for (int s = 2; s < 11; ++s) {
    TR(2); wait_vm<40>(); TR(2); bar(); TR(2);
    issueC(s + 4); issueAB(s + 5, bseq & 15); bseq = (bseq >> 4) | ((bseq & 15) << 16);
    if (s == 7) fv = ld8_async(hfp);
    TR(2);
  }
  wait_vm<40>(); bar(); issueC(15);
  STAMP(4);
  asm volatile("" : "+v"(fv));
  if (!__all(fv == tag)) poll(hfp, [&](u64 x) { return x == tag; });
  STAMP(5);
  const char* yfp = ws + WS_FLAG + (unsigned)(2 * 256 + (c.tp * 4 + c.ec) * 4 + (lane & 3)) * FLAG_STRIDE;
#pragma unroll 1
  for (int s = 12; s < 24; ++s) { TR(3); wait_vm<32>(); TR(3); bar(); TR(3); issueH(s - 12); if (s == 21) fv = ld8_async(yfp); TR(3); }
  STAMP(6);
  wait_vm<32>();
  asm volatile("" : "+v"(fv));
  if (!__all(fv == tag)) poll(yfp, [&](u64 x) { return x == tag; });
  STAMP(7);
  bar(); issueH(12);
  wait_vm<32>(); bar(); issueH(13);
  wait_vm<32>(); bar();
  wait_vm<16>(); bar();
  wait_vm<0>(); bar();
  TR_END();
}

DEV void helper(const Ctx& c) {
  const int hw = c.wid - 6, lane = c.lane, wid = c.wid;
  LAS char* lds = c.lds; char* ws = c.ws;
  const bool typeB = c.ec == 3;
  const int G = c.cp >> 2, v = c.cp & 3, PA = 7 * G + 2 * v, PB = typeB ? PA : PA + 1;
  const int q = lane >> 4, cc = lane & 15, qq = cc >> 2, pp = lane & 3;
  u64 tag;
  {
    const u64 tagv = __hip_atomic_load((GAS u64*)(ws + WS_EPOCH), __ATOMIC_RELAXED, __HIP_MEMORY_SCOPE_AGENT);
    const int row = c.rp * 128 + c.cp * 8 + 4 * hw + q;
    u32x4 xv[4];
#pragma unroll
    for (int i = 0; i < 4; ++i) xv[i] = *(const u32x4*)(c.x + (size_t)row * 1024 + cc * 16 + i * 256);
    const float bv = bflo(*(const u16*)(c.b1 + ((hw ? PB : PA) * 64 + lane) * 2));
    float s = 0.f;
#pragma unroll
    for (int i = 0; i < 16; ++i) s += bflo(xv[i >> 2][i & 3]) + bfhi(xv[i >> 2][i & 3]);
    const float m = sum16(s) * (1.f / 512.f);
    float z = 0.f;
#pragma unroll
    for (int i = 0; i < 16; ++i) { const float d0 = bflo(xv[i >> 2][i & 3]) - m, d1 = bfhi(xv[i >> 2][i & 3]) - m; z += d0 * d0; z += d1 * d1; }
    const float var = sum16(z) * (1.f / 512.f);
    const float sd = __builtin_amdgcn_sqrtf(rbf(rbf(var) + 1e-5f));
    tag = uniform64(tagv) + 1;
    if (cc == 0)
      __hip_atomic_store((GAS u64*)(ws + WS_ROWST + (size_t)row * 8), ((u64)(unsigned)tag << 32) | pk_bf16(m, sd), __ATOMIC_RELAXED, __HIP_MEMORY_SCOPE_AGENT);
    *(LAS float*)(lds + LDS_B1 + (hw * 64 + lane) * 4) = bv;
    if (hw == 0 && lane == 0) { *(LAS u64*)(lds + LDS_TAG) = tag; *(LAS u64*)(lds + LDS_TAG + 8) = 0; }
  }
  const unsigned wa0 = cc * 128 + ((q ^ (cc & 7)) * 16), wa1 = cc * 128 + (((4 + q) ^ (cc & 7)) * 16);
  const unsigned wa = hw ? wa1 : wa0;
  unsigned ja[4];
#pragma unroll
  for (int n = 0; n < 4; ++n) ja[n] = (8 * q + qq) * 128 + (((2 * n + (pp >> 1)) ^ ((qq & 2) | ((q & 1) << 2))) * 16) + 8 * (pp & 1) + hw * 4096;
  const bf16x8 ones = {0x3f80, 0x3f80, 0x3f80, 0x3f80, 0x3f80, 0x3f80, 0x3f80, 0x3f80};
  const f32x4 z4 = {0.f, 0.f, 0.f, 0.f};
  f32x4 cs[7] = {z4, z4, z4, z4, z4, z4, z4};
  const int srow = 64 * hw + 32 * (lane >> 5) + 8 * qq + 4 * (q & 1) + (lane & 3);
  const char* stp = ws + WS_ROWST + (size_t)(c.rp * 128 + srow) * 8;
  wait_lgkm0();
  bar();
  auto csum = [&](int s) {
    LAS char* ba = lds + (4 + (s & 3)) * SLOT;
    bf16x8 f[7];
#pragma unroll
    for (int n = 0; n < 3; ++n) f[n] = ldsb128(ba + n * 2048 + wa);
    if (typeB) {
#pragma unroll
      for (int n = 3; n < 7; ++n) f[n] = ldsb128(ba + n * 2048 + wa);
    } else {
#pragma unroll
      for (int n = 3; n < 7; ++n) f[n] = ldstr(ba + 6144 + ja[n - 3], 512);
    }
#pragma unroll
    for (int n = 0; n < 7; ++n) cs[n] = MFMA(ones, f[n], cs[n]);
  };
  u64 stv = 0;
  unsigned dummy = 0;
  const char* Cp = (c.ks >> 1 ? c.Ci : c.Cr) + (size_t)(c.tp * 128 + c.ec * 32 + hw * 16 + q) * 4096 + (c.ks & 1) * 2048 + cc * 128;
#pragma unroll 1
  for (int s = 0; s < 7; ++s) {
    if (s == 4) stv = ld8_async(stp);
    csum(s);
    wait_lgkm0();
    bar();
  }
  {
    csum(7);
    wait_vm<0>();
    asm volatile("" : "+v"(stv), "+v"(dummy));
    const unsigned t32 = (unsigned)tag;
    if (!__all((unsigned)(stv >> 32) == t32)) stv = poll(stp, [&](u64 x) { return (unsigned)(x >> 32) == t32; });
    *(LAS float*)(lds + LDS_MEAN + (64 * hw + lane) * 4) = bflo((unsigned)stv);
    *(LAS float*)(lds + LDS_RSTD + (64 * hw + lane) * 4) = __builtin_amdgcn_rcpf(bfhi((unsigned)stv));
    if (lane < 16) {
#pragma unroll
      for (int n = 0; n < 7; ++n) *(LAS float*)(lds + LDS_CS + ((hw * 11 + n) * 16 + lane) * 4) = cs[n][0];
    }
    wait_lgkm0();
    bar();
  }
#pragma unroll
  for (int n = 0; n < 4; ++n) cs[n] = z4;
#pragma unroll 1
  for (int h = 0; h < 2; ++h) {
#pragma unroll
    for (int j = 0; j < 2; ++j) {
      LAS char* ba = lds + ((h | j) == 0 ? 8 : 3 + 2 * h + j) * SLOT;
#pragma unroll
      for (int k = 0; k < 2; ++k)
#pragma unroll
        for (int n = 0; n < 4; ++n) cs[n] = MFMA(ones, ldstr(ba + ja[n] + k * 8192, 512), cs[n]);
    }
    if (h == 1 && lane < 16) {
#pragma unroll
      for (int n = 0; n < 4; ++n) *(LAS float*)(lds + LDS_CS + ((hw * 11 + 7 + n) * 16 + lane) * 4) = cs[n][0];
    }
    wait_lgkm0();
    bar();
  }
#pragma unroll 1
  for (int s = 0; s < 30; ++s) bar();
  if (blockIdx.x < 2) {
    if (blockIdx.x == 0 && hw == 0) {
      const char* fp = ws + WS_FLAG + (unsigned)lane * FLAG_STRIDE;
      const u64 t0 = __builtin_amdgcn_s_memrealtime();
      for (;;) {
        u64 f[4];
#pragma unroll
        for (int i = 0; i < 4; ++i) f[i] = __hip_atomic_load((GAS u64*)(fp + i * 64 * FLAG_STRIDE), __ATOMIC_RELAXED, __HIP_MEMORY_SCOPE_AGENT);
        if (__all(f[0] == tag && f[1] == tag && f[2] == tag && f[3] == tag)) break;
        if (__builtin_amdgcn_s_memrealtime() - t0 > 20000000ull) break;
        __builtin_amdgcn_s_sleep(1);
      }
      if (lane == 0) __hip_atomic_store((GAS u64*)(ws + WS_EPOCH), tag, __ATOMIC_RELAXED, __HIP_MEMORY_SCOPE_AGENT);
    }
  }
}

DEV void consumer(const Ctx& c) {
  const int w = c.wid, lane = c.lane, wid = c.wid;
  TR_DECL;
  LAS char* lds = c.lds; char* ws = c.ws;
  const int q = lane >> 4, cc = lane & 15, qq = cc >> 2, pp = lane & 3;
  const bool typeB = c.ec == 3;
  const int G = c.cp >> 2, v = c.cp & 3, E0 = 16 * G + 3 * v, PA = 7 * G + 2 * v, PB = typeB ? PA : PA + 1;
  const auto rs = __builtin_amdgcn_make_buffer_rsrc(ws, 0, WS_BYTES, 0x00020000);
  STAMP(0);
  const unsigned wa0 = cc * 128 + ((q ^ (cc & 7)) * 16), wa1 = cc * 128 + (((4 + q) ^ (cc & 7)) * 16);
  unsigned ja[4];
#pragma unroll
  for (int n = 0; n < 4; ++n) ja[n] = opq(4 * SLOT + (8 * q + qq) * 128 + (((2 * n + (pp >> 1)) ^ ((qq & 2) | ((q & 1) << 2))) * 16) + 8 * (pp & 1));
  bf16x8 xf[2][16];
  auto h_epi = [&](f32x4 (&acc)[4][2], int brow, int P, int csrow) {
    float mean[2], rstd[2];
#pragma unroll
    for (int u = 0; u < 2; ++u) { mean[u] = *(const LAS float*)(lds + LDS_MEAN + ((2 * w + u) * 16 + cc) * 4); rstd[u] = *(const LAS float*)(lds + LDS_RSTD + ((2 * w + u) * 16 + cc) * 4); }
    f32x4 cs[4], bv[4];
#pragma unroll
    for (int n = 0; n < 4; ++n) {
      cs[n] = *(const LAS f32x4*)(lds + LDS_CS + ((csrow + n) * 16 + 4 * q) * 4) + *(const LAS f32x4*)(lds + LDS_CS + ((11 + csrow + n) * 16 + 4 * q) * 4);
      bv[n] = *(const LAS f32x4*)(lds + LDS_B1 + (brow * 64 + n * 16 + 4 * q) * 4);
    }
    const unsigned hb = WS_H + (unsigned)((((2 * c.rp + (w >> 1)) * 56 + 2 * P) * 4 + 2 * (w & 1)) * 1024);
#pragma unroll
    for (int b = 0; b < 2; ++b)
#pragma unroll
      for (int u = 0; u < 2; ++u) {
        unsigned d[4];
#pragma unroll
        for (int h = 0; h < 2; ++h) {
          const int n = 2 * b + h;
          f32x4 z = (acc[n][u] - mean[u] * cs[n]) * rstd[u] + bv[n];
          z = __builtin_elementwise_max(z, (f32x4){0.f, 0.f, 0.f, 0.f});
          z = z * z;
          d[2 * h] = pk_bf16(z[0], z[1]); d[2 * h + 1] = pk_bf16(z[2], z[3]);
        }
        __builtin_amdgcn_raw_buffer_store_b128((u32x4){d[0], d[1], d[2], d[3]}, rs, lane * 16 + u * 1024 + hb + b * 4096, 0, 16);
      }
  };
  constexpr int L2 = 8;
  bf16x8 g[64];
  auto rd2 = [&](auto t_) {
    constexpr int t = decltype(t_)::value, j = t / 16, k = (t / 4) & 3, n = t & 3;
    if constexpr (j == 0) g[t] = ldstr(lds + 4 * SLOT + ja[n] + k * 4096, 512); else g[t] = ldstr(lds + (j - 1) * SLOT + ja[n] + k * 4096, 512);
  };
  bar();
  STAMP(1);
  auto pass1 = [&](auto na_) {
    constexpr int NA = decltype(na_)::value;
    f32x4 acc[7][2];
#pragma unroll
    for (int n = 0; n < 7; ++n) { acc[n][0] = (f32x4){0, 0, 0, 0}; acc[n][1] = (f32x4){0, 0, 0, 0}; }
    bf16x8 fr[112];
    const unsigned bx0 = opq(wa0 + w * 4096), bx1 = opq(wa1 + w * 4096), bw0 = opq(wa0 + 4 * SLOT), bw1 = opq(wa1 + 4 * SLOT);
    auto rd = [&](auto t_) {
      constexpr int t = decltype(t_)::value, kk = t / 7, n = t % 7, so = ((kk >> 1) & 3) * SLOT;
      if constexpr (n == 0) { xf[0][kk] = ldsb128(lds + ((kk & 1) ? bx1 : bx0) + so); xf[1][kk] = ldsb128(lds + ((kk & 1) ? bx1 : bx0) + so + 2048); }
      if constexpr (n < NA) fr[t] = ldsb128(lds + ((kk & 1) ? bw1 : bw0) + so + n * 2048);
      else fr[t] = ldstr(lds + ja[(n - 3) & 3] + so + 6144 + (kk & 1) * 4096, 512);
    };
    static_for<0, 7>(rd);
    SCHED();
    static_for<0, 112>([&](auto p_) {
      constexpr int p = decltype(p_)::value, kk = p / 7, n = p % 7;
      constexpr int kn = p / 14 + 1;
      if constexpr (p == 14 * kn - lead1(kn)) {
        TR(1);
        wait_lgkm0();
        TR(1);
        bar();
        TR(1);
        SCHED();
      }
      const bf16x8 f = fr[p];
      if constexpr (n < NA) acc[n][0] = MFMA(xf[0][kk], f, acc[n][0]); else acc[n][0] = MFMA(f, xf[0][kk], acc[n][0]);
      SCHED();
      static_for<p + 1, p + 15>([&](auto t_) {
        constexpr int t = decltype(t_)::value;
        if constexpr (t < 112 && t >= 7 && sched1(t) == p) { rd(t_); SCHED(); }
      });
      if constexpr (n < NA) acc[n][1] = MFMA(xf[1][kk], f, acc[n][1]); else acc[n][1] = MFMA(f, xf[1][kk], acc[n][1]);
      SCHED();
    });
    STAMP(2);
    f32x4 mr[2], rr[2];
    const float sgn = E0 >= 32 ? -1.f : 1.f;
#pragma unroll
    for (int u = 0; u < 2; ++u) {
      mr[u] = *(const LAS f32x4*)(lds + LDS_MEAN + ((2 * w + u) * 16 + 4 * q) * 4);
      rr[u] = *(const LAS f32x4*)(lds + LDS_RSTD + ((2 * w + u) * 16 + 4 * q) * 4) * sgn;
    }
    float cs[NA];
#pragma unroll
    for (int n = 0; n < NA; ++n) cs[n] = *(const LAS float*)(lds + LDS_CS + (n * 16 + cc) * 4) + *(const LAS float*)(lds + LDS_CS + ((11 + n) * 16 + cc) * 4);
#pragma unroll
    for (int n = 0; n < NA; ++n) {
      const f32x4 o0 = (acc[n][0] - mr[0] * cs[n]) * rr[0], o1 = (acc[n][1] - mr[1] * cs[n]) * rr[1];
      u32x4 d = {pk_bf16(o0[0], o0[1]), pk_bf16(o0[2], o0[3]), pk_bf16(o1[0], o1[1]), pk_bf16(o1[2], o1[3])};
      const int E = E0 + n;
      __builtin_amdgcn_raw_buffer_store_b128(d, rs, lane * 16 + WS_ABT + (unsigned)((((E >> 3) * 64 + c.rp * 4 + w) * 8 + (E & 7)) * 1024), 0, 16);
    }
    SCHED();
    if (NA == 3) {
      f32x4 (&ah)[4][2] = *(f32x4(*)[4][2]) & acc[3];
      h_epi(ah, 0, PA, 3);
    }
  };
  if (typeB) pass1(IC<7>{}); else pass1(IC<3>{});
  SCHED();
  static_for<0, L2>(rd2);
  const u64 tag = uniform64(*(const LAS u64*)(lds + LDS_TAG));
  STAMP(3);
  {
    f32x4 acc[4][2];
#pragma unroll
    for (int n = 0; n < 4; ++n) { acc[n][0] = (f32x4){0, 0, 0, 0}; acc[n][1] = (f32x4){0, 0, 0, 0}; }
    SCHED();
    static_for<0, 64>([&](auto p_) {
      constexpr int p = decltype(p_)::value, kk = p / 4, n = p & 3;
      if constexpr (p == 16) {
        wait_vm<0>();
        bar();
        if (w == 0 && lane == 0) set_flag(ws, 0, c.rp * 16 + c.cp, tag);
        SCHED();
      }
      if constexpr (p == 32) { bar(); SCHED(); }
      acc[n][0] = MFMA(g[p], xf[0][kk], acc[n][0]);
      SCHED();
      if constexpr (p + L2 < 64) { rd2(IC<p + L2>{}); SCHED(); }
      acc[n][1] = MFMA(g[p], xf[1][kk], acc[n][1]);
      SCHED();
    });
    STAMP(11);
    h_epi(acc, 1, PB, 7);
  }
  STAMP(4);
  const int wt = w >> 1, we = w & 1;
  unsigned wb[2];
#pragma unroll
  for (int n = 0; n < 2; ++n) wb[n] = 4 * SLOT + (4 * q + qq) * 128 + (((2 * (2 * we + n) + (pp >> 1)) ^ ((4 * q + qq) & 6)) * 16) + 8 * (pp & 1);
  const unsigned hb = wt * 2048 + lane * 16;
  bf16x8 hA[4][2], wA[4][2], hB[4][2], wB[4][2];
  wait_lgkm0();
  bar();
  STAMP(5);
#define PREAD(HN, WN, SO) if (i < 12) { if (i % 3 == 2) { HN[i / 3][0] = ldsb128(lds + (SO) + hb + (i / 3) * 4096); HN[i / 3][1] = ldsb128(lds + (SO) + hb + (i / 3) * 4096 + 1024); } \
                                        else WN[i / 3][i % 3] = ldstr(lds + (SO) + wb[i % 3] + (i / 3) * 4096, 2048); SCHED(); }
  {
    f32x4 acc[4][4];
#pragma unroll
    for (int m = 0; m < 4; ++m)
#pragma unroll
      for (int n = 0; n < 4; ++n) acc[m][n] = (f32x4){0, 0, 0, 0};
    const unsigned a0 = wt * 8192 + wa0, a1 = wt * 8192 + wa1, b0 = we * 4096 + lane * 16;
    bf16x8 fa0[4], fb0[4], fa1[4], fb1[4];
#pragma unroll
    for (int m = 0; m < 4; ++m) { fa0[m] = ldsb128(lds + a0 + m * 2048); fb0[m] = ldsb128(lds + b0 + 7 * SLOT + m * 1024); }
#define HALF(FA, FB, NA_, NB_, AOFF, BOFF) do { _Pragma("unroll") for (int i = 0; i < 16; ++i) { \
      acc[i >> 2][i & 3] = MFMA(FB[i & 3], FA[i >> 2], acc[i >> 2][i & 3]); SCHED(); \
      if (i < 8) { if (i == 0) NB_[0] = ldsb128(lds + (BOFF)); else if (i == 1) NA_[0] = ldsb128(lds + (AOFF)); \
        else if (i < 5) NB_[i - 1] = ldsb128(lds + (BOFF) + (i - 1) * 1024); else NA_[i - 4] = ldsb128(lds + (AOFF) + (i - 4) * 2048); SCHED(); } } } while (0)
    unsigned so = 0, sb = 7 * SLOT, bseq = 0x76548;
#pragma unroll 1
    for (int seg = 0; seg < 2; ++seg) {
      const int n = seg ? 12 : 3;
#pragma unroll 1
      for (int s = 0; s < n; ++s) {
        HALF(fa0, fb0, fa1, fb1, so + a1, sb + b0 + 8192);
        TR(2);
        wait_lgkm0();
        TR(2);
        bar();
        TR(2);
        sb = (bseq & 15) * SLOT; bseq = (bseq >> 4) | ((bseq & 15) << 16); so = (so + SLOT) & (4 * SLOT - 1);
        HALF(fa1, fb1, fa0, fb0, so + a0, sb + b0);
      }
      if (seg == 0) {
        wait_vm<0>();
        unsigned old = 0;
        if (lane == 0) old = __hip_atomic_fetch_add((LAS unsigned*)(lds + LDS_TAG + 8), 1u, __ATOMIC_RELAXED, __HIP_MEMORY_SCOPE_WORKGROUP);
        if (lane == 0 && old == 3) set_flag(ws, 1, c.rp * 16 + c.cp, tag);
      }
    }
    HALF(fa0, fb0, fa1, fb1, so + a1, sb + b0 + 8192);
    wait_lgkm0();
    bar();
#pragma unroll
    for (int i = 0; i < 16; ++i) {
      acc[i >> 2][i & 3] = MFMA(fb1[i & 3], fa1[i >> 2], acc[i >> 2][i & 3]); SCHED();
      PREAD(hA, wA, 0)
    }
    STAMP(6);
    if (w == c.ks) {
#pragma unroll
      for (int m = 0; m < 4; ++m)
#pragma unroll
        for (int n = 0; n < 4; ++n) *(LAS f32x4*)(lds + 8 * SLOT + (m * 4 + n) * 1024 + lane * 16) = acc[m][n];
    } else {
      const unsigned yb = WS_YP + (unsigned)((((c.tp * 4 + c.ec) * 4 + c.ks) * 4 + w) * 8192);
#pragma unroll
      for (int m = 0; m < 4; ++m)
#pragma unroll
        for (int j = 0; j < 2; ++j) {
          const f32x4 e = acc[m][2 * j], o = acc[m][2 * j + 1];
          __builtin_amdgcn_raw_buffer_store_b128((u32x4){pk_bf16(e[0], e[1]), pk_bf16(e[2], e[3]), pk_bf16(o[0], o[1]), pk_bf16(o[2], o[3])}, rs, lane * 16 + (m * 2 + j) * 1024 + yb, 0, 16);
        }
    }
  }
  STAMP(7);
  {
    f32x4 acc[2][2];
#pragma unroll
    for (int m = 0; m < 2; ++m) { acc[m][0] = (f32x4){0, 0, 0, 0}; acc[m][1] = (f32x4){0, 0, 0, 0}; }
    u32x4 yp[3][2];
    u32x2 xr[2][2], b2r[2];
    const int trow = c.tp * 128 + (c.ks >> 1) * 64 + 32 * wt + 8 * qq + (cc & 3);
    const int ecol = c.ec * 128 + (c.ks & 1) * 64 + 32 * we + 4 * q;
    unsigned so = 0;
#define PSTEP(HC, WC, HN, WN) do { wait_lgkm0(); TR(3); bar(); TR(3); so = (so + SLOT) & (4 * SLOT - 1); SCHED(); \
      _Pragma("unroll") for (int i = 0; i < 16; ++i) { \
        acc[(i >> 1) & 1][i & 1] = MFMA(WC[i >> 2][i & 1], HC[i >> 2][(i >> 1) & 1], acc[(i >> 1) & 1][i & 1]); SCHED(); \
        PREAD(HN, WN, so) } } while (0)
#pragma unroll 1
    for (int seg = 0; seg < 2; ++seg) {
#pragma unroll 1
      for (int s = 0; s < 2; ++s) { PSTEP(hA, wA, hB, wB); PSTEP(hB, wB, hA, wA); }
      if (seg == 0) {
        wait_vm<0>();
        unsigned old = 0;
        if (lane == 0) old = __hip_atomic_fetch_add((LAS unsigned*)(lds + LDS_TAG + 12), 1u, __ATOMIC_RELAXED, __HIP_MEMORY_SCOPE_WORKGROUP);
        if (lane == 0 && old == 3) set_flag(ws, 2, (c.tp * 4 + c.ec) * 4 + c.ks, tag);
      }
    }
    PSTEP(hA, wA, hB, wB);
    STAMP(8);
    {
      const unsigned yb = WS_YP + (unsigned)(((c.tp * 4 + c.ec) * 16 + c.ks) * 8192);
#pragma unroll
      for (int i = 0; i < 3; ++i) {
        const int src = (c.ks + 1 + i) & 3;
#pragma unroll
        for (int m = 0; m < 2; ++m) yp[i][m] = __builtin_amdgcn_raw_buffer_load_b128(rs, lane * 16 + ((2 * wt + m) * 2 + we) * 1024, yb + src * 32768, 16);
      }
#pragma unroll
      for (int m = 0; m < 2; ++m)
#pragma unroll
        for (int n = 0; n < 2; ++n) xr[m][n] = *(const u32x2*)(c.x + (size_t)(trow + 4 * m) * 1024 + (ecol + 16 * n) * 2);
#pragma unroll
      for (int n = 0; n < 2; ++n) b2r[n] = *(const u32x2*)(c.b2 + (ecol + 16 * n) * 2);
    }
    SCHED();
    PSTEP(hB, wB, hA, wA);
    PSTEP(hA, wA, hB, wB);
    PSTEP(hB, wB, hA, wA);
    PSTEP(hA, wA, hB, wB);
    f32x4 yo[2][2];
#pragma unroll
    for (int m = 0; m < 2; ++m)
#pragma unroll
      for (int n = 0; n < 2; ++n) yo[m][n] = *(const LAS f32x4*)(lds + 8 * SLOT + ((2 * wt + m) * 4 + 2 * we + n) * 1024 + lane * 16);
#pragma unroll
    for (int i = 0; i < 16; ++i) acc[(i >> 1) & 1][i & 1] = MFMA(wB[i >> 2][i & 1], hB[i >> 2][(i >> 1) & 1], acc[(i >> 1) & 1][i & 1]);
    STAMP(9);
#pragma unroll
    for (int m = 0; m < 2; ++m)
#pragma unroll
      for (int n = 0; n < 2; ++n) {
        f32x4 y = yo[m][n];
#pragma unroll
        for (int i = 0; i < 3; ++i) y += (f32x4){bflo(yp[i][m][2 * n]), bfhi(yp[i][m][2 * n]), bflo(yp[i][m][2 * n + 1]), bfhi(yp[i][m][2 * n + 1])};
        const float bv[4] = {bflo(b2r[n].x), bfhi(b2r[n].x), bflo(b2r[n].y), bfhi(b2r[n].y)};
        const float xv[4] = {bflo(xr[m][n].x), bfhi(xr[m][n].x), bflo(xr[m][n].y), bfhi(xr[m][n].y)};
        float o[4];
#pragma unroll
        for (int r = 0; r < 4; ++r) {
          const float z = acc[m][n][r] + bv[r];
          const float g = z * __builtin_amdgcn_rcpf(1.f + __expf(-z));
          o[r] = xv[r] + y[r] * g;
        }
        u32x2 d = {pk_bf16(o[0], o[1]), pk_bf16(o[2], o[3])};
        *(u32x2*)(c.out + (size_t)(trow + 4 * m) * 1024 + (ecol + 16 * n) * 2) = d;
      }
    STAMP(10);
    TR_END();
  }
}

__global__ __launch_bounds__(512, 2) void fnet_mlp_fused(const char* x, const char* Cr, const char* Ci, const char* Wr, const char* Wi,
                                                         const char* w1, const char* b1, const char* w2, const char* b2, char* out, char* ws) {
  extern __shared__ __attribute__((aligned(16))) char shm[];
  Ctx c;
  c.x = x; c.Cr = Cr; c.Ci = Ci; c.Wr = Wr; c.Wi = Wi; c.w1 = w1; c.b1 = b1; c.w2 = w2; c.b2 = b2; c.out = out; c.ws = ws;
  c.lds = (LAS char*)shm;
  c.wid = __builtin_amdgcn_readfirstlane(threadIdx.x >> 6); c.lane = threadIdx.x & 63;
  const int b = blockIdx.x, xcd = b & 7, r = b >> 3;
  c.ks = xcd >> 1; c.tp = (xcd & 1) * 8 + (r >> 2); c.ec = r & 3;
  c.rp = c.tp; c.cp = c.ks * 4 + c.ec;
  if (c.wid >= 6) helper(c); else if (c.wid >= 4) loader(c); else consumer(c);
}

extern "C" void kernel_launch(void* const* d_in, const int* in_sizes, int n_in, void* d_out, int out_size, void* d_ws, size_t ws_size, hipStream_t stream) {
  (void)hipFuncSetAttribute((const void*)fnet_mlp_fused, hipFuncAttributeMaxDynamicSharedMemorySize, LDS_BYTES);
  fnet_mlp_fused<<<256, 512, LDS_BYTES, stream>>>((const char*)d_in[0], (const char*)d_in[1], (const char*)d_in[2], (const char*)d_in[3], (const char*)d_in[4],
                                                  (const char*)d_in[6], (const char*)d_in[7], (const char*)d_in[8], (const char*)d_in[9], (char*)d_out, (char*)d_ws + WSOFF(ws_size));
}
```

```cpp
#define AUXC 0
#define PREFC 0
#define PREFS 0
#define PROBE 1
#define PWG 0
#define PMODE 2
#include <hip/hip_runtime.h>
typedef unsigned short u16;
typedef unsigned long long u64;
typedef short bf16x8 __attribute__((ext_vector_type(8)));
typedef short s16x4 __attribute__((ext_vector_type(4)));
typedef float f32x4 __attribute__((ext_vector_type(4)));
typedef float f32x2 __attribute__((ext_vector_type(2)));
typedef unsigned u32x4 __attribute__((ext_vector_type(4)));
typedef unsigned u32x2 __attribute__((ext_vector_type(2)));
typedef __bf16 bf2 __attribute__((ext_vector_type(2)));
#define LAS __attribute__((address_space(3)))
#define GAS __attribute__((address_space(1)))
#define DEV __device__ __forceinline__

constexpr int S_ = 2048, D_ = 512, F_ = 1792;
constexpr unsigned WS_EPOCH = 0, WS_FLAG = 4096, FLAG_STRIDE = 128, WS_ROWST = 256u << 10;
constexpr unsigned WS_ABT = 1u << 20, WS_H = 8u << 20, WS_YP = 16u << 20, WS_BYTES = 32u << 20;
constexpr int SLOT = 16384, NSLOT = 9;
#define LEADE 6
#define LOOKE 13
#define LEADL 5
#define LOOKL 10
constexpr int LDS_MEAN = NSLOT * SLOT, LDS_RSTD = LDS_MEAN + 512, LDS_CS = LDS_RSTD + 512;
constexpr int LDS_B1 = LDS_CS + 22 * 64;
constexpr int LDS_TAG = LDS_B1 + 512;
constexpr int LDS_BYTES = LDS_TAG + 16;

template <int N> DEV void wait_vm() { asm volatile("s_waitcnt vmcnt(%0)" ::"n"(N) : "memory"); }
DEV void wait_lgkm0() { asm volatile("s_waitcnt lgkmcnt(0)" ::: "memory"); }
DEV void bar() { asm volatile("" ::: "memory"); __builtin_amdgcn_s_barrier(); asm volatile("" ::: "memory"); }
#define SCHED() __builtin_amdgcn_sched_barrier(0)
#define MFMA(a, b, c) __builtin_amdgcn_mfma_f32_16x16x32_bf16((a), (b), (c), 0, 0, 0)
DEV unsigned pk_bf16(float a, float b) { f32x2 f = {a, b}; return __builtin_bit_cast(unsigned, __builtin_convertvector(f, bf2)); }
DEV float rbf(float a) { return __uint_as_float(pk_bf16(a, 0.f) << 16); }
DEV float bflo(unsigned v) { return __uint_as_float(v << 16); }
DEV float bfhi(unsigned v) { return __uint_as_float(v & 0xffff0000u); }
DEV bf16x8 ldsb128(LAS char* p) { return *(const LAS bf16x8*)p; }
DEV bf16x8 ldstr(LAS char* p, int second) {
  s16x4 a = __builtin_amdgcn_ds_read_tr16_b64_v4i16((LAS s16x4*)p);
  s16x4 b = __builtin_amdgcn_ds_read_tr16_b64_v4i16((LAS s16x4*)(p + second));
  return __builtin_shufflevector(a, b, 0, 1, 2, 3, 4, 5, 6, 7);
}
struct Frag2 { u32x2 p[4]; };
DEV void rd2_b64x4(unsigned a0, unsigned a1, Frag2& f) {
  asm volatile("ds_read_b64 %0, %4\n\tds_read_b64 %1, %4 offset:8\n\tds_read_b64 %2, %5\n\tds_read_b64 %3, %5 offset:8"
               : "=&v"(f.p[0]), "=&v"(f.p[1]), "=&v"(f.p[2]), "=&v"(f.p[3]) : "v"(a0), "v"(a1) : "memory");
}
DEV void rd2_tr(unsigned a, Frag2& f) {
  asm volatile("ds_read_b64_tr_b16 %0, %4\n\tds_read_b64_tr_b16 %1, %4 offset:512\n\tds_read_b64_tr_b16 %2, %4 offset:4096\n\tds_read_b64_tr_b16 %3, %4 offset:4608"
               : "=&v"(f.p[0]), "=&v"(f.p[1]), "=&v"(f.p[2]), "=&v"(f.p[3]) : "v"(a) : "memory");
}
DEV void pin(Frag2& f) { asm volatile("" : "+v"(f.p[0]), "+v"(f.p[1]), "+v"(f.p[2]), "+v"(f.p[3])); }
DEV bf16x8 fr_of(const Frag2& f, int i) { return __builtin_bit_cast(bf16x8, (u32x4){f.p[2 * i].x, f.p[2 * i].y, f.p[2 * i + 1].x, f.p[2 * i + 1].y}); }
DEV unsigned opq(unsigned v) { asm volatile("" : "+v"(v)); return v; }
DEV void glds(const char* src, LAS char* dst) { __builtin_amdgcn_global_load_lds((const unsigned*)src, (LAS unsigned*)dst, 16, 0, 0); }
#define AUXP1 0
#define AUXW2 0
template <int AUX> DEV void gldsa(const char* src, LAS char* dst) { __builtin_amdgcn_global_load_lds((const unsigned*)src, (LAS unsigned*)dst, 16, 0, AUX); }
DEV void glds_h(const char* src, LAS char* dst) { __builtin_amdgcn_global_load_lds((const unsigned*)src, (LAS unsigned*)dst, 16, 0, 16); }
DEV u64 ld8_async(const char* p) { u64 v; asm volatile("global_load_dwordx2 %0, %1, off sc1" : "=v"(v) : "v"(p) : "memory"); return v; }
DEV u32x4 ld16_async(const char* sbase, unsigned voff) { u32x4 v; asm volatile("global_load_dwordx4 %0, %1, %2" : "=v"(v) : "v"(voff), "s"(sbase) : "memory"); return v; }
template <int OFF> DEV void ds_w128(unsigned a, u32x4 v) { asm volatile("ds_write_b128 %0, %1 offset:%2" ::"v"(a), "v"(v), "n"(OFF) : "memory"); }
template <int CTRL> DEV float dpp_add(float x) { return x + __builtin_bit_cast(float, __builtin_amdgcn_update_dpp(0, __builtin_bit_cast(int, x), CTRL, 0xf, 0xf, false)); }
DEV float sum16(float x) { x = dpp_add<0xB1>(x); x = dpp_add<0x4E>(x); x = dpp_add<0x141>(x); return dpp_add<0x140>(x); }
DEV float sum32(float x) {
  x = dpp_add<0xB1>(x); x = dpp_add<0x4E>(x); x = dpp_add<0x141>(x); x = dpp_add<0x140>(x);
  const auto r = __builtin_amdgcn_permlane16_swap(__float_as_uint(x), __float_as_uint(x), false, false);
  return __uint_as_float(r[0]) + __uint_as_float(r[1]);
}

#define STAMP(i) do { if (wid < 4) { if ((i) == 0) pr0 = __builtin_amdgcn_s_memrealtime(); if ((i) == PROBE) pr1 = __builtin_amdgcn_s_memrealtime(); \
    if ((i) == 10 && blockIdx.x == PWG && wid == 0) { const u64 e = (PMODE == 0) ? __builtin_amdgcn_s_memrealtime() + 4 * (pr1 - pr0) : (PMODE == 1) ? __builtin_amdgcn_s_memrealtime() + 10000 : pr0 + 10000; while (__builtin_amdgcn_s_memrealtime() < e) __builtin_amdgcn_s_sleep(1); } } } while (0)
#define TR_DECL
#define TR(ph) do {} while (0)
#define TR_END() do {} while (0)

template <int N> struct IC { static constexpr int value = N; };
template <int B, int E, class F> DEV void static_for(F&& f) { if constexpr (B < E) { f(IC<B>{}); static_for<B + 1, E>(f); } }
constexpr int lead1(int k) { return k >= 7 ? LEADL : LEADE; }
constexpr int sched1(int t) { const int k = t / 14, e0 = t - (k >= 7 ? LOOKL : LOOKE), e1 = 14 * k - lead1(k) + (t - 14 * k) / 2; return k == 0 ? (t - 7 > 0 ? (t - 7) / 2 : 0) : (e0 > e1 ? e0 : e1); }
struct Ctx {
  const char *x, *Cr, *Ci, *Wr, *Wi, *w1, *b1, *w2, *b2; char* out; char* ws; LAS char* lds;
  int wid, lane, rp, cp, tp, ec, ks;
};

template <class P> DEV u64 poll(const char* p, P ok) {
  const u64 t0 = __builtin_amdgcn_s_memrealtime();
  u64 v;
  for (;;) {
    v = __hip_atomic_load((GAS u64*)p, __ATOMIC_RELAXED, __HIP_MEMORY_SCOPE_AGENT);
    if (__all(ok(v))) break;
    if (__builtin_amdgcn_s_memrealtime() - t0 > 20000000ull) break;
    __builtin_amdgcn_s_sleep(1);
  }
  return v;
}
DEV void set_flag(char* ws, int kind, int idx, u64 tag) {
  __hip_atomic_store((GAS u64*)(ws + WS_FLAG + (unsigned)(kind * 256 + idx) * FLAG_STRIDE), tag, __ATOMIC_RELAXED, __HIP_MEMORY_SCOPE_AGENT);
}
DEV u64 uniform64(u64 v) {
  const unsigned lo = __builtin_amdgcn_readfirstlane((unsigned)v), hi = __builtin_amdgcn_readfirstlane((unsigned)(v >> 32));
  return ((u64)hi << 32) | lo;
}

DEV void loader(const Ctx& c) {
  const int lw = c.wid - 4, lane = c.lane, wid = c.wid;
  TR_DECL;
  u64 pr0 = 0, pr1 = 0; (void)pr0; (void)pr1;
  LAS char* lds = c.lds; char* ws = c.ws;
  const bool typeB = c.ec == 3;
  const int G = c.cp >> 2, v = c.cp & 3;
  const int E0 = 16 * G + 3 * v;
  const int PA = 7 * G + 2 * v;
  const int PB = typeB ? PA : PA + 1;
  const int r8 = lane >> 3, p8 = lane & 7;
  const int trow0 = 64 * lw + 8 * (r8 >> 2) + (r8 & 3);
#define PROW(i) (32 * ((i) >> 2) + 16 * ((i) & 1) + 4 * (((i) >> 1) & 1))
  const unsigned swz = (unsigned)((p8 ^ r8) * 16);
  const unsigned swzJ0 = (unsigned)((p8 ^ (r8 & 2)) * 16), swzJ1 = (unsigned)((p8 ^ (r8 & 2) ^ 4) * 16);
  LAS char* ld = lds + lw * 8192;
  const char* Xl = c.x + (size_t)(c.rp * 128 + trow0) * 1024 + swz;
  auto issueX = [&](int s) {
#pragma unroll
    for (int i = 0; i < 8; ++i) gldsa<AUXP1>(Xl + PROW(i) * 1024 + s * 128, ld + (s & 3) * SLOT + i * 1024);
  };
  issueX(0);
  const char* pB[8]; unsigned strB[8];
  {
    const char* Wm = (E0 >= 32 ? c.Wi : c.Wr) + (size_t)(E0 & 31) * 16384;
#pragma unroll
    for (int i = 0; i < 8; ++i) {
      const int p = lw * 8 + i;
      const bool isJ = p >= 14, isW = !isJ && (typeB || p < 6);
      const int pr = isJ ? p - 14 : p - 6;
      const char* b1_ = c.w1 + (size_t)(pr * 8) * (F_ * 2) + (isJ ? PB : PA) * 128;
      const char* bw_ = Wm + (size_t)p * 8192;
      pB[i] = (isW ? bw_ : b1_) + (isW ? r8 * 1024 + swz : r8 * (F_ * 2) + ((i & 1) ? swzJ1 : swzJ0));
      strB[i] = isW ? 128 : (isJ ? 16 * F_ * 2 : 64 * F_ * 2);
    }
  }
  auto issueB1 = [&](int s) {
#pragma unroll
    for (int i = 0; i < 8; ++i) {
      LAS char* d = (i >= 6 && lw == 1) ? lds + 8 * SLOT + (i - 6) * 1024 + s * 2048 : ld + (4 + (s & 3)) * SLOT + i * 1024;
      gldsa<AUXP1>(pB[i], d);
      pB[i] += strB[i];
    }
  };
  issueB1(0); issueX(1); issueB1(1);
  const char* Jl = c.w1 + PB * 128 + (size_t)(64 * lw + r8) * (F_ * 2);
  const char* Cl = (c.ks >> 1 ? c.Ci : c.Cr) + (size_t)(c.tp * 128 + trow0) * 4096 + (c.ks & 1) * 2048 + swz;
  const char* ABb = ws + WS_ABT + (size_t)(((c.ks >> 1) * 4 + c.ec) * 64 + (c.ks & 1) * 32) * 8192 + lw * 8192 + lane * 16;
  const char* Hb = ws + WS_H + (size_t)(2 * c.tp + (c.ks >> 1)) * 56 * 4096 + lw * 8192 + lane * 16;
  const char* W2l = c.w2 + (c.ec * 128 + (c.ks & 1) * 64) * 2 + (64 * lw + r8) * 1024 + (p8 ^ (r8 & 6)) * 16;
  auto issueJ = [&](int j) {
#pragma unroll
    for (int i = 0; i < 8; ++i) gldsa<AUXP1>(Jl + (j * 128 + i * 8) * (F_ * 2) + ((i & 1) ? swzJ1 : swzJ0), ld + (3 + j) * SLOT + i * 1024);
  };
  auto issueC = [&](int s) {
#pragma unroll
    for (int i = 0; i < 8; ++i) gldsa<AUXC>(Cl + PROW(i) * 4096 + s * 128, ld + (s & 3) * SLOT + i * 1024);
  };
  auto issueAB = [&](int s, int slot) {
#pragma unroll
    for (int i = 0; i < 8; ++i) glds_h(ABb + s * 16384 + i * 1024, ld + slot * SLOT + i * 1024);
  };
  auto issueH = [&](int s) {
#pragma unroll
    for (int i = 0; i < 8; ++i) glds_h(Hb + s * 16384 + i * 1024, ld + (s & 3) * SLOT + i * 1024);
#pragma unroll
    for (int i = 0; i < 8; ++i) gldsa<AUXW2>(W2l + i * 8192 + s * 131072, ld + (4 + (s & 3)) * SLOT + i * 1024);
  };
  wait_vm<16>();
  bar();
  issueX(2); issueB1(2);
#pragma unroll 1
  for (int s = 0; s < 5; ++s) { issueX(s + 3); issueB1(s + 3); TR(1); wait_vm<32>(); TR(1); bar(); TR(1); }
#pragma unroll 1
  for (int s = 5; s < 7; ++s) { issueJ(s - 4); issueC(s - 5); TR(1); wait_vm<32>(); TR(1); bar(); TR(1); }
  issueJ(3); issueC(2);
  wait_vm<40>(); bar();
  issueC(3);
  const u64 tag = uniform64(*(const LAS u64*)(lds + LDS_TAG));
  wait_vm<16>(); bar();
  bar();
  STAMP(1);
  {
    const int G2 = 2 * (c.ks >> 1) + (c.ec >> 1), m = lane & 3;
    const bool act = lane < 32 && ((c.ec & 1) ? (m >= 2) : (m <= 2));
    poll(ws + WS_FLAG + (unsigned)(0 * 256 + (8 * (c.ks & 1) + ((lane >> 2) & 7)) * 16 + 4 * G2 + m) * FLAG_STRIDE, [&](u64 x) { return !act || x == tag; });
  }
  STAMP(2);
  issueAB(0, 7); issueAB(1, 8); issueAB(2, 4);
  wait_vm<16>();
  bar();
  STAMP(3);
  issueAB(3, 5); issueAB(4, 6);
  wait_vm<24>(); bar(); issueC(4); issueAB(5, 7);
  wait_vm<32>(); bar(); issueC(5); issueAB(6, 8);
  const char* hfp = ws + WS_FLAG + (unsigned)(1 * 256 + c.tp * 16 + (lane & 15)) * FLAG_STRIDE;
  u64 fv = 0;
  unsigned bseq = 0x87654;
#pragma unroll 1
  for (int s = 2; s < 11; ++s) {
    TR(2); wait_vm<40>(); TR(2); bar(); TR(2);
    issueC(s + 4); issueAB(s + 5, bseq & 15); bseq = (bseq >> 4) | ((bseq & 15) << 16);
    if (s == 7) fv = ld8_async(hfp);
    TR(2);
  }
  wait_vm<40>(); bar(); issueC(15);
  STAMP(4);
  asm volatile("" : "+v"(fv));
  if (!__all(fv == tag)) poll(hfp, [&](u64 x) { return x == tag; });
  STAMP(5);
  const char* yfp = ws + WS_FLAG + (unsigned)(2 * 256 + (c.tp * 4 + c.ec) * 4 + (lane & 3)) * FLAG_STRIDE;
#pragma unroll 1
  for (int s = 12; s < 24; ++s) { TR(3); wait_vm<32>(); TR(3); bar(); TR(3); issueH(s - 12); if (s == 21) fv = ld8_async(yfp); TR(3); }
  STAMP(6);
  wait_vm<32>();
  asm volatile("" : "+v"(fv));
  if (!__all(fv == tag)) poll(yfp, [&](u64 x) { return x == tag; });
  STAMP(7);
  bar(); issueH(12);
  wait_vm<32>(); bar(); issueH(13);
  wait_vm<32>(); bar();
  wait_vm<16>(); bar();
  wait_vm<0>(); bar();
  TR_END();
}

DEV void helper(const Ctx& c) {
  const int hw = c.wid - 6, lane = c.lane, wid = c.wid;
  LAS char* lds = c.lds; char* ws = c.ws;
  const bool typeB = c.ec == 3;
  const int G = c.cp >> 2, v = c.cp & 3, PA = 7 * G + 2 * v, PB = typeB ? PA : PA + 1;
  const int q = lane >> 4, cc = lane & 15, qq = cc >> 2, pp = lane & 3;
  u64 tag;
  {
    const u64 tagv = __hip_atomic_load((GAS u64*)(ws + WS_EPOCH), __ATOMIC_RELAXED, __HIP_MEMORY_SCOPE_AGENT);
    const int row = c.rp * 128 + c.cp * 8 + 4 * hw + q;
    u32x4 xv[4];
#pragma unroll
    for (int i = 0; i < 4; ++i) xv[i] = *(const u32x4*)(c.x + (size_t)row * 1024 + cc * 16 + i * 256);
    const float bv = bflo(*(const u16*)(c.b1 + ((hw ? PB : PA) * 64 + lane) * 2));
    float s = 0.f;
#pragma unroll
    for (int i = 0; i < 16; ++i) s += bflo(xv[i >> 2][i & 3]) + bfhi(xv[i >> 2][i & 3]);
    const float m = sum16(s) * (1.f / 512.f);
    float z = 0.f;
#pragma unroll
    for (int i = 0; i < 16; ++i) { const float d0 = bflo(xv[i >> 2][i & 3]) - m, d1 = bfhi(xv[i >> 2][i & 3]) - m; z += d0 * d0; z += d1 * d1; }
    const float var = sum16(z) * (1.f / 512.f);
    const float sd = __builtin_amdgcn_sqrtf(rbf(rbf(var) + 1e-5f));
    tag = uniform64(tagv) + 1;
    if (cc == 0)
      __hip_atomic_store((GAS u64*)(ws + WS_ROWST + (size_t)row * 8), ((u64)(unsigned)tag << 32) | pk_bf16(m, sd), __ATOMIC_RELAXED, __HIP_MEMORY_SCOPE_AGENT);
    *(LAS float*)(lds + LDS_B1 + (hw * 64 + lane) * 4) = bv;
    if (hw == 0 && lane == 0) { *(LAS u64*)(lds + LDS_TAG) = tag; *(LAS u64*)(lds + LDS_TAG + 8) = 0; }
  }
  const unsigned wa0 = cc * 128 + ((q ^ (cc & 7)) * 16), wa1 = cc * 128 + (((4 + q) ^ (cc & 7)) * 16);
  const unsigned wa = hw ? wa1 : wa0;
  unsigned ja[4];
#pragma unroll
  for (int n = 0; n < 4; ++n) ja[n] = (8 * q + qq) * 128 + (((2 * n + (pp >> 1)) ^ ((qq & 2) | ((q & 1) << 2))) * 16) + 8 * (pp & 1) + hw * 4096;
  const bf16x8 ones = {0x3f80, 0x3f80, 0x3f80, 0x3f80, 0x3f80, 0x3f80, 0x3f80, 0x3f80};
  const f32x4 z4 = {0.f, 0.f, 0.f, 0.f};
  f32x4 cs[7] = {z4, z4, z4, z4, z4, z4, z4};
  const int srow = 64 * hw + 32 * (lane >> 5) + 8 * qq + 4 * (q & 1) + (lane & 3);
  const char* stp = ws + WS_ROWST + (size_t)(c.rp * 128 + srow) * 8;
  wait_lgkm0();
  bar();
  auto csum = [&](int s) {
    LAS char* ba = lds + (4 + (s & 3)) * SLOT;
    bf16x8 f[7];
#pragma unroll
    for (int n = 0; n < 3; ++n) f[n] = ldsb128(ba + n * 2048 + wa);
    if (typeB) {
#pragma unroll
      for (int n = 3; n < 7; ++n) f[n] = ldsb128(ba + n * 2048 + wa);
    } else {
#pragma unroll
      for (int n = 3; n < 7; ++n) f[n] = ldstr(ba + 6144 + ja[n - 3], 512);
    }
#pragma unroll
    for (int n = 0; n < 7; ++n) cs[n] = MFMA(ones, f[n], cs[n]);
  };
  u64 stv = 0;
  unsigned dummy = 0;
  const char* Cp = (c.ks >> 1 ? c.Ci : c.Cr) + (size_t)(c.tp * 128 + c.ec * 32 + hw * 16 + q) * 4096 + (c.ks & 1) * 2048 + cc * 128;
#pragma unroll 1
  for (int s = 0; s < 7; ++s) {
    if (s == 4) stv = ld8_async(stp);
    csum(s);
    wait_lgkm0();
    bar();
  }
  {
    csum(7);
    wait_vm<0>();
    asm volatile("" : "+v"(stv), "+v"(dummy));
    const unsigned t32 = (unsigned)tag;
    if (!__all((unsigned)(stv >> 32) == t32)) stv = poll(stp, [&](u64 x) { return (unsigned)(x >> 32) == t32; });
    *(LAS float*)(lds + LDS_MEAN + (64 * hw + lane) * 4) = bflo((unsigned)stv);
    *(LAS float*)(lds + LDS_RSTD + (64 * hw + lane) * 4) = __builtin_amdgcn_rcpf(bfhi((unsigned)stv));
    if (lane < 16) {
#pragma unroll
      for (int n = 0; n < 7; ++n) *(LAS float*)(lds + LDS_CS + ((hw * 11 + n) * 16 + lane) * 4) = cs[n][0];
    }
    wait_lgkm0();
    bar();
  }
#pragma unroll
  for (int n = 0; n < 4; ++n) cs[n] = z4;
#pragma unroll 1
  for (int h = 0; h < 2; ++h) {
#pragma unroll
    for (int j = 0; j < 2; ++j) {
      LAS char* ba = lds + ((h | j) == 0 ? 8 : 3 + 2 * h + j) * SLOT;
#pragma unroll
      for (int k = 0; k < 2; ++k)
#pragma unroll
        for (int n = 0; n < 4; ++n) cs[n] = MFMA(ones, ldstr(ba + ja[n] + k * 8192, 512), cs[n]);
    }
    if (h == 1 && lane < 16) {
#pragma unroll
      for (int n = 0; n < 4; ++n) *(LAS float*)(lds + LDS_CS + ((hw * 11 + 7 + n) * 16 + lane) * 4) = cs[n][0];
    }
    wait_lgkm0();
    bar();
  }
#pragma unroll 1
  for (int s = 0; s < 30; ++s) bar();
  if (blockIdx.x < 2) {
    if (blockIdx.x == 0 && hw == 0) {
      const char* fp = ws + WS_FLAG + (unsigned)lane * FLAG_STRIDE;
      const u64 t0 = __builtin_amdgcn_s_memrealtime();
      for (;;) {
        u64 f[4];
#pragma unroll
        for (int i = 0; i < 4; ++i) f[i] = __hip_atomic_load((GAS u64*)(fp + i * 64 * FLAG_STRIDE), __ATOMIC_RELAXED, __HIP_MEMORY_SCOPE_AGENT);
        if (__all(f[0] == tag && f[1] == tag && f[2] == tag && f[3] == tag)) break;
        if (__builtin_amdgcn_s_memrealtime() - t0 > 20000000ull) break;
        __builtin_amdgcn_s_sleep(1);
      }
      if (lane == 0) __hip_atomic_store((GAS u64*)(ws + WS_EPOCH), tag, __ATOMIC_RELAXED, __HIP_MEMORY_SCOPE_AGENT);
    }
  }
}

DEV void consumer(const Ctx& c) {
  const int w = c.wid, lane = c.lane, wid = c.wid;
  TR_DECL;
  u64 pr0 = 0, pr1 = 0; (void)pr0; (void)pr1;
  LAS char* lds = c.lds; char* ws = c.ws;
  const int q = lane >> 4, cc = lane & 15, qq = cc >> 2, pp = lane & 3;
  const bool typeB = c.ec == 3;
  const int G = c.cp >> 2, v = c.cp & 3, E0 = 16 * G + 3 * v, PA = 7 * G + 2 * v, PB = typeB ? PA : PA + 1;
  const auto rs = __builtin_amdgcn_make_buffer_rsrc(ws, 0, WS_BYTES, 0x00020000);
  STAMP(0);
  const unsigned wa0 = cc * 128 + ((q ^ (cc & 7)) * 16), wa1 = cc * 128 + (((4 + q) ^ (cc & 7)) * 16);
  unsigned ja[4];
#pragma unroll
  for (int n = 0; n < 4; ++n) ja[n] = opq(4 * SLOT + (8 * q + qq) * 128 + (((2 * n + (pp >> 1)) ^ ((qq & 2) | ((q & 1) << 2))) * 16) + 8 * (pp & 1));
  bf16x8 xf[2][16];
  auto h_epi = [&](f32x4 (&acc)[4][2], int brow, int P, int csrow) {
    float mean[2], rstd[2];
#pragma unroll
    for (int u = 0; u < 2; ++u) { mean[u] = *(const LAS float*)(lds + LDS_MEAN + ((2 * w + u) * 16 + cc) * 4); rstd[u] = *(const LAS float*)(lds + LDS_RSTD + ((2 * w + u) * 16 + cc) * 4); }
    f32x4 cs[4], bv[4];
#pragma unroll
    for (int n = 0; n < 4; ++n) {
      cs[n] = *(const LAS f32x4*)(lds + LDS_CS + ((csrow + n) * 16 + 4 * q) * 4) + *(const LAS f32x4*)(lds + LDS_CS + ((11 + csrow + n) * 16 + 4 * q) * 4);
      bv[n] = *(const LAS f32x4*)(lds + LDS_B1 + (brow * 64 + n * 16 + 4 * q) * 4);
    }
    const unsigned hb = WS_H + (unsigned)((((2 * c.rp + (w >> 1)) * 56 + 2 * P) * 4 + 2 * (w & 1)) * 1024);
#pragma unroll
    for (int b = 0; b < 2; ++b)
#pragma unroll
      for (int u = 0; u < 2; ++u) {
        unsigned d[4];
#pragma unroll
        for (int h = 0; h < 2; ++h) {
          const int n = 2 * b + h;
          f32x4 z = (acc[n][u] - mean[u] * cs[n]) * rstd[u] + bv[n];
          z = __builtin_elementwise_max(z, (f32x4){0.f, 0.f, 0.f, 0.f});
          z = z * z;
          d[2 * h] = pk_bf16(z[0], z[1]); d[2 * h + 1] = pk_bf16(z[2], z[3]);
        }
        __builtin_amdgcn_raw_buffer_store_b128((u32x4){d[0], d[1], d[2], d[3]}, rs, lane * 16 + u * 1024 + hb + b * 4096, 0, 16);
      }
  };
  constexpr int L2 = 8;
  bf16x8 g[64];
  auto rd2 = [&](auto t_) {
    constexpr int t = decltype(t_)::value, j = t / 16, k = (t / 4) & 3, n = t & 3;
    if constexpr (j == 0) g[t] = ldstr(lds + 4 * SLOT + ja[n] + k * 4096, 512); else g[t] = ldstr(lds + (j - 1) * SLOT + ja[n] + k * 4096, 512);
  };
  bar();
  STAMP(1);
  auto pass1 = [&](auto na_) {
    constexpr int NA = decltype(na_)::value;
    f32x4 acc[7][2];
#pragma unroll
    for (int n = 0; n < 7; ++n) { acc[n][0] = (f32x4){0, 0, 0, 0}; acc[n][1] = (f32x4){0, 0, 0, 0}; }
    bf16x8 fr[112];
    const unsigned bx0 = opq(wa0 + w * 4096), bx1 = opq(wa1 + w * 4096), bw0 = opq(wa0 + 4 * SLOT), bw1 = opq(wa1 + 4 * SLOT);
    auto rd = [&](auto t_) {
      constexpr int t = decltype(t_)::value, kk = t / 7, n = t % 7, so = ((kk >> 1) & 3) * SLOT;
      if constexpr (n == 0) { xf[0][kk] = ldsb128(lds + ((kk & 1) ? bx1 : bx0) + so); xf[1][kk] = ldsb128(lds + ((kk & 1) ? bx1 : bx0) + so + 2048); }
      if constexpr (n < NA) fr[t] = ldsb128(lds + ((kk & 1) ? bw1 : bw0) + so + n * 2048);
      else fr[t] = ldstr(lds + ja[(n - 3) & 3] + so + 6144 + (kk & 1) * 4096, 512);
    };
    static_for<0, 7>(rd);
    SCHED();
    static_for<0, 112>([&](auto p_) {
      constexpr int p = decltype(p_)::value, kk = p / 7, n = p % 7;
      constexpr int kn = p / 14 + 1;
      if constexpr (p == 14 * kn - lead1(kn)) {
        TR(1);
        wait_lgkm0();
        TR(1);
        bar();
        TR(1);
        SCHED();
      }
      const bf16x8 f = fr[p];
      if constexpr (n < NA) acc[n][0] = MFMA(xf[0][kk], f, acc[n][0]); else acc[n][0] = MFMA(f, xf[0][kk], acc[n][0]);
      SCHED();
      static_for<p + 1, p + 15>([&](auto t_) {
        constexpr int t = decltype(t_)::value;
        if constexpr (t < 112 && t >= 7 && sched1(t) == p) { rd(t_); SCHED(); }
      });
      if constexpr (n < NA) acc[n][1] = MFMA(xf[1][kk], f, acc[n][1]); else acc[n][1] = MFMA(f, xf[1][kk], acc[n][1]);
      SCHED();
    });
    STAMP(2);
    f32x4 mr[2], rr[2];
    const float sgn = E0 >= 32 ? -1.f : 1.f;
#pragma unroll
    for (int u = 0; u < 2; ++u) {
      mr[u] = *(const LAS f32x4*)(lds + LDS_MEAN + ((2 * w + u) * 16 + 4 * q) * 4);
      rr[u] = *(const LAS f32x4*)(lds + LDS_RSTD + ((2 * w + u) * 16 + 4 * q) * 4) * sgn;
    }
    float cs[NA];
#pragma unroll
    for (int n = 0; n < NA; ++n) cs[n] = *(const LAS float*)(lds + LDS_CS + (n * 16 + cc) * 4) + *(const LAS float*)(lds + LDS_CS + ((11 + n) * 16 + cc) * 4);
#pragma unroll
    for (int n = 0; n < NA; ++n) {
      const f32x4 o0 = (acc[n][0] - mr[0] * cs[n]) * rr[0], o1 = (acc[n][1] - mr[1] * cs[n]) * rr[1];
      u32x4 d = {pk_bf16(o0[0], o0[1]), pk_bf16(o0[2], o0[3]), pk_bf16(o1[0], o1[1]), pk_bf16(o1[2], o1[3])};
      const int E = E0 + n;
      __builtin_amdgcn_raw_buffer_store_b128(d, rs, lane * 16 + WS_ABT + (unsigned)((((E >> 3) * 64 + c.rp * 4 + w) * 8 + (E & 7)) * 1024), 0, 16);
    }
    SCHED();
    if (NA == 3) {
      f32x4 (&ah)[4][2] = *(f32x4(*)[4][2]) & acc[3];
      h_epi(ah, 0, PA, 3);
    }
  };
  if (typeB) pass1(IC<7>{}); else pass1(IC<3>{});
  SCHED();
  static_for<0, L2>(rd2);
  const u64 tag = uniform64(*(const LAS u64*)(lds + LDS_TAG));
  STAMP(3);
  {
    f32x4 acc[4][2];
#pragma unroll
    for (int n = 0; n < 4; ++n) { acc[n][0] = (f32x4){0, 0, 0, 0}; acc[n][1] = (f32x4){0, 0, 0, 0}; }
    SCHED();
    static_for<0, 64>([&](auto p_) {
      constexpr int p = decltype(p_)::value, kk = p / 4, n = p & 3;
      if constexpr (p == 16) {
        wait_vm<0>();
        bar();
        if (w == 0 && lane == 0) set_flag(ws, 0, c.rp * 16 + c.cp, tag);
        SCHED();
      }
      if constexpr (p == 32) { bar(); SCHED(); }
      acc[n][0] = MFMA(g[p], xf[0][kk], acc[n][0]);
      SCHED();
      if constexpr (p + L2 < 64) { rd2(IC<p + L2>{}); SCHED(); }
      acc[n][1] = MFMA(g[p], xf[1][kk], acc[n][1]);
      SCHED();
    });
    STAMP(11);
    h_epi(acc, 1, PB, 7);
  }
  STAMP(4);
  const int wt = w >> 1, we = w & 1;
  unsigned wb[2];
#pragma unroll
  for (int n = 0; n < 2; ++n) wb[n] = 4 * SLOT + (4 * q + qq) * 128 + (((2 * (2 * we + n) + (pp >> 1)) ^ ((4 * q + qq) & 6)) * 16) + 8 * (pp & 1);
  const unsigned hb = wt * 2048 + lane * 16;
  bf16x8 hA[4][2], wA[4][2], hB[4][2], wB[4][2];
  wait_lgkm0();
  bar();
  STAMP(5);
#define PREAD(HN, WN, SO) if (i < 12) { if (i % 3 == 2) { HN[i / 3][0] = ldsb128(lds + (SO) + hb + (i / 3) * 4096); HN[i / 3][1] = ldsb128(lds + (SO) + hb + (i / 3) * 4096 + 1024); } \
                                        else WN[i / 3][i % 3] = ldstr(lds + (SO) + wb[i % 3] + (i / 3) * 4096, 2048); SCHED(); }
  {
    f32x4 acc[4][4];
#pragma unroll
    for (int m = 0; m < 4; ++m)
#pragma unroll
      for (int n = 0; n < 4; ++n) acc[m][n] = (f32x4){0, 0, 0, 0};
    const unsigned a0 = wt * 8192 + wa0, a1 = wt * 8192 + wa1, b0 = we * 4096 + lane * 16;
    bf16x8 fa0[4], fb0[4], fa1[4], fb1[4];
#pragma unroll
    for (int m = 0; m < 4; ++m) { fa0[m] = ldsb128(lds + a0 + m * 2048); fb0[m] = ldsb128(lds + b0 + 7 * SLOT + m * 1024); }
#define HALF(FA, FB, NA_, NB_, AOFF, BOFF) do { _Pragma("unroll") for (int i = 0; i < 16; ++i) { \
      acc[i >> 2][i & 3] = MFMA(FB[i & 3], FA[i >> 2], acc[i >> 2][i & 3]); SCHED(); \
      if (i < 8) { if (i == 0) NB_[0] = ldsb128(lds + (BOFF)); else if (i == 1) NA_[0] = ldsb128(lds + (AOFF)); \
        else if (i < 5) NB_[i - 1] = ldsb128(lds + (BOFF) + (i - 1) * 1024); else NA_[i - 4] = ldsb128(lds + (AOFF) + (i - 4) * 2048); SCHED(); } } } while (0)
    unsigned so = 0, sb = 7 * SLOT, bseq = 0x76548;
#pragma unroll 1
    for (int seg = 0; seg < 2; ++seg) {
      const int n = seg ? 12 : 3;
#pragma unroll 1
      for (int s = 0; s < n; ++s) {
        HALF(fa0, fb0, fa1, fb1, so + a1, sb + b0 + 8192);
        TR(2);
        wait_lgkm0();
        TR(2);
        bar();
        TR(2);
        sb = (bseq & 15) * SLOT; bseq = (bseq >> 4) | ((bseq & 15) << 16); so = (so + SLOT) & (4 * SLOT - 1);
        HALF(fa1, fb1, fa0, fb0, so + a0, sb + b0);
      }
      if (seg == 0) {
        wait_vm<0>();
        unsigned old = 0;
        if (lane == 0) old = __hip_atomic_fetch_add((LAS unsigned*)(lds + LDS_TAG + 8), 1u, __ATOMIC_RELAXED, __HIP_MEMORY_SCOPE_WORKGROUP);
        if (lane == 0 && old == 3) set_flag(ws, 1, c.rp * 16 + c.cp, tag);
      }
    }
    HALF(fa0, fb0, fa1, fb1, so + a1, sb + b0 + 8192);
    wait_lgkm0();
    bar();
#pragma unroll
    for (int i = 0; i < 16; ++i) {
      acc[i >> 2][i & 3] = MFMA(fb1[i & 3], fa1[i >> 2], acc[i >> 2][i & 3]); SCHED();
      PREAD(hA, wA, 0)
    }
    STAMP(6);
    if (w == c.ks) {
#pragma unroll
      for (int m = 0; m < 4; ++m)
#pragma unroll
        for (int n = 0; n < 4; ++n) *(LAS f32x4*)(lds + 8 * SLOT + (m * 4 + n) * 1024 + lane * 16) = acc[m][n];
    } else {
      const unsigned yb = WS_YP + (unsigned)((((c.tp * 4 + c.ec) * 4 + c.ks) * 4 + w) * 8192);
#pragma unroll
      for (int m = 0; m < 4; ++m)
#pragma unroll
        for (int j = 0; j < 2; ++j) {
          const f32x4 e = acc[m][2 * j], o = acc[m][2 * j + 1];
          __builtin_amdgcn_raw_buffer_store_b128((u32x4){pk_bf16(e[0], e[1]), pk_bf16(e[2], e[3]), pk_bf16(o[0], o[1]), pk_bf16(o[2], o[3])}, rs, lane * 16 + (m * 2 + j) * 1024 + yb, 0, 16);
        }
    }
  }
  STAMP(7);
  {
    f32x4 acc[2][2];
#pragma unroll
    for (int m = 0; m < 2; ++m) { acc[m][0] = (f32x4){0, 0, 0, 0}; acc[m][1] = (f32x4){0, 0, 0, 0}; }
    u32x4 yp[3][2];
    u32x2 xr[2][2], b2r[2];
    const int trow = c.tp * 128 + (c.ks >> 1) * 64 + 32 * wt + 8 * qq + (cc & 3);
    const int ecol = c.ec * 128 + (c.ks & 1) * 64 + 32 * we + 4 * q;
    unsigned so = 0;
#define PSTEP(HC, WC, HN, WN) do { wait_lgkm0(); TR(3); bar(); TR(3); so = (so + SLOT) & (4 * SLOT - 1); SCHED(); \
      _Pragma("unroll") for (int i = 0; i < 16; ++i) { \
        acc[(i >> 1) & 1][i & 1] = MFMA(WC[i >> 2][i & 1], HC[i >> 2][(i >> 1) & 1], acc[(i >> 1) & 1][i & 1]); SCHED(); \
        PREAD(HN, WN, so) } } while (0)
#pragma unroll 1
    for (int seg = 0; seg < 2; ++seg) {
#pragma unroll 1
      for (int s = 0; s < 2; ++s) { PSTEP(hA, wA, hB, wB); PSTEP(hB, wB, hA, wA); }
      if (seg == 0) {
        wait_vm<0>();
        unsigned old = 0;
        if (lane == 0) old = __hip_atomic_fetch_add((LAS unsigned*)(lds + LDS_TAG + 12), 1u, __ATOMIC_RELAXED, __HIP_MEMORY_SCOPE_WORKGROUP);
        if (lane == 0 && old == 3) set_flag(ws, 2, (c.tp * 4 + c.ec) * 4 + c.ks, tag);
      }
    }
    PSTEP(hA, wA, hB, wB);
    STAMP(8);
    {
      const unsigned yb = WS_YP + (unsigned)(((c.tp * 4 + c.ec) * 16 + c.ks) * 8192);
#pragma unroll
      for (int i = 0; i < 3; ++i) {
        const int src = (c.ks + 1 + i) & 3;
#pragma unroll
        for (int m = 0; m < 2; ++m) yp[i][m] = __builtin_amdgcn_raw_buffer_load_b128(rs, lane * 16 + ((2 * wt + m) * 2 + we) * 1024, yb + src * 32768, 16);
      }
#pragma unroll
      for (int m = 0; m < 2; ++m)
#pragma unroll
        for (int n = 0; n < 2; ++n) xr[m][n] = *(const u32x2*)(c.x + (size_t)(trow + 4 * m) * 1024 + (ecol + 16 * n) * 2);
#pragma unroll
      for (int n = 0; n < 2; ++n) b2r[n] = *(const u32x2*)(c.b2 + (ecol + 16 * n) * 2);
    }
    SCHED();
    PSTEP(hB, wB, hA, wA);
    PSTEP(hA, wA, hB, wB);
    PSTEP(hB, wB, hA, wA);
    PSTEP(hA, wA, hB, wB);
    f32x4 yo[2][2];
#pragma unroll
    for (int m = 0; m < 2; ++m)
#pragma unroll
      for (int n = 0; n < 2; ++n) yo[m][n] = *(const LAS f32x4*)(lds + 8 * SLOT + ((2 * wt + m) * 4 + 2 * we + n) * 1024 + lane * 16);
#pragma unroll
    for (int i = 0; i < 16; ++i) acc[(i >> 1) & 1][i & 1] = MFMA(wB[i >> 2][i & 1], hB[i >> 2][(i >> 1) & 1], acc[(i >> 1) & 1][i & 1]);
    STAMP(9);
#pragma unroll
    for (int m = 0; m < 2; ++m)
#pragma unroll
      for (int n = 0; n < 2; ++n) {
        f32x4 y = yo[m][n];
#pragma unroll
        for (int i = 0; i < 3; ++i) y += (f32x4){bflo(yp[i][m][2 * n]), bfhi(yp[i][m][2 * n]), bflo(yp[i][m][2 * n + 1]), bfhi(yp[i][m][2 * n + 1])};
        const float bv[4] = {bflo(b2r[n].x), bfhi(b2r[n].x), bflo(b2r[n].y), bfhi(b2r[n].y)};
        const float xv[4] = {bflo(xr[m][n].x), bfhi(xr[m][n].x), bflo(xr[m][n].y), bfhi(xr[m][n].y)};
        float o[4];
#pragma unroll
        for (int r = 0; r < 4; ++r) {
          const float z = acc[m][n][r] + bv[r];
          const float g = z * __builtin_amdgcn_rcpf(1.f + __expf(-z));
          o[r] = xv[r] + y[r] * g;
        }
        u32x2 d = {pk_bf16(o[0], o[1]), pk_bf16(o[2], o[3])};
        *(u32x2*)(c.out + (size_t)(trow + 4 * m) * 1024 + (ecol + 16 * n) * 2) = d;
      }
    STAMP(10);
    TR_END();
  }
}

__global__ __launch_bounds__(512, 2) void fnet_mlp_fused(const char* x, const char* Cr, const char* Ci, const char* Wr, const char* Wi,
                                                         const char* w1, const char* b1, const char* w2, const char* b2, char* out, char* ws) {
  extern __shared__ __attribute__((aligned(16))) char shm[];
  Ctx c;
  c.x = x; c.Cr = Cr; c.Ci = Ci; c.Wr = Wr; c.Wi = Wi; c.w1 = w1; c.b1 = b1; c.w2 = w2; c.b2 = b2; c.out = out; c.ws = ws;
  c.lds = (LAS char*)shm;
  c.wid = __builtin_amdgcn_readfirstlane(threadIdx.x >> 6); c.lane = threadIdx.x & 63;
  const int b = blockIdx.x, xcd = b & 7, r = b >> 3;
  c.ks = xcd >> 1; c.tp = (xcd & 1) * 8 + (r >> 2); c.ec = r & 3;
  c.rp = c.tp; c.cp = c.ks * 4 + c.ec;
  if (c.wid >= 6) helper(c); else if (c.wid >= 4) loader(c); else consumer(c);
}

#define WSOFF(n) 0
extern "C" void kernel_launch(void* const* d_in, const int* in_sizes, int n_in, void* d_out, int out_size, void* d_ws, size_t ws_size, hipStream_t stream) {
  (void)hipFuncSetAttribute((const void*)fnet_mlp_fused, hipFuncAttributeMaxDynamicSharedMemorySize, LDS_BYTES);
  fnet_mlp_fused<<<256, 512, LDS_BYTES, stream>>>((const char*)d_in[0], (const char*)d_in[1], (const char*)d_in[2], (const char*)d_in[3], (const char*)d_in[4],
                                                  (const char*)d_in[6], (const char*)d_in[7], (const char*)d_in[8], (const char*)d_in[9], (char*)d_out, (char*)d_ws + WSOFF(ws_size));
}
```

```cpp
#define AUXC 0
#define PREFC 0
#define PREFS 0
#define PREFI 4
#include <hip/hip_runtime.h>
typedef unsigned short u16;
typedef unsigned long long u64;
typedef short bf16x8 __attribute__((ext_vector_type(8)));
typedef short s16x4 __attribute__((ext_vector_type(4)));
typedef float f32x4 __attribute__((ext_vector_type(4)));
typedef float f32x2 __attribute__((ext_vector_type(2)));
typedef unsigned u32x4 __attribute__((ext_vector_type(4)));
typedef unsigned u32x2 __attribute__((ext_vector_type(2)));
typedef __bf16 bf2 __attribute__((ext_vector_type(2)));
#define LAS __attribute__((address_space(3)))
#define GAS __attribute__((address_space(1)))
#define DEV __device__ __forceinline__

constexpr int S_ = 2048, D_ = 512, F_ = 1792;
constexpr unsigned WS_EPOCH = 0, WS_FLAG = 4096, FLAG_STRIDE = 128, WS_ROWST = 256u << 10;
constexpr unsigned WS_ABT = 1u << 20, WS_H = 8u << 20, WS_YP = 16u << 20, WS_BYTES = 32u << 20;
constexpr int SLOT = 16384, NSLOT = 9;
#define LEADE 6
#define LOOKE 13
#define LEADL 5
#define LOOKL 10
constexpr int LDS_MEAN = NSLOT * SLOT, LDS_RSTD = LDS_MEAN + 512, LDS_CS = LDS_RSTD + 512;
constexpr int LDS_B1 = LDS_CS + 22 * 64;
constexpr int LDS_TAG = LDS_B1 + 512;
constexpr int LDS_BYTES = LDS_TAG + 16;

template <int N> DEV void wait_vm() { asm volatile("s_waitcnt vmcnt(%0)" ::"n"(N) : "memory"); }
DEV void wait_lgkm0() { asm volatile("s_waitcnt lgkmcnt(0)" ::: "memory"); }
DEV void bar() { asm volatile("" ::: "memory"); __builtin_amdgcn_s_barrier(); asm volatile("" ::: "memory"); }
#define SCHED() __builtin_amdgcn_sched_barrier(0)
#define MFMA(a, b, c) __builtin_amdgcn_mfma_f32_16x16x32_bf16((a), (b), (c), 0, 0, 0)
DEV unsigned pk_bf16(float a, float b) { f32x2 f = {a, b}; return __builtin_bit_cast(unsigned, __builtin_convertvector(f, bf2)); }
DEV float rbf(float a) { return __uint_as_float(pk_bf16(a, 0.f) << 16); }
DEV float bflo(unsigned v) { return __uint_as_float(v << 16); }
DEV float bfhi(unsigned v) { return __uint_as_float(v & 0xffff0000u); }
DEV bf16x8 ldsb128(LAS char* p) { return *(const LAS bf16x8*)p; }
DEV bf16x8 ldstr(LAS char* p, int second) {
  s16x4 a = __builtin_amdgcn_ds_read_tr16_b64_v4i16((LAS s16x4*)p);
  s16x4 b = __builtin_amdgcn_ds_read_tr16_b64_v4i16((LAS s16x4*)(p + second));
  return __builtin_shufflevector(a, b, 0, 1, 2, 3, 4, 5, 6, 7);
}
struct Frag2 { u32x2 p[4]; };
DEV void rd2_b64x4(unsigned a0, unsigned a1, Frag2& f) {
  asm volatile("ds_read_b64 %0, %4\n\tds_read_b64 %1, %4 offset:8\n\tds_read_b64 %2, %5\n\tds_read_b64 %3, %5 offset:8"
               : "=&v"(f.p[0]), "=&v"(f.p[1]), "=&v"(f.p[2]), "=&v"(f.p[3]) : "v"(a0), "v"(a1) : "memory");
}
DEV void rd2_tr(unsigned a, Frag2& f) {
  asm volatile("ds_read_b64_tr_b16 %0, %4\n\tds_read_b64_tr_b16 %1, %4 offset:512\n\tds_read_b64_tr_b16 %2, %4 offset:4096\n\tds_read_b64_tr_b16 %3, %4 offset:4608"
               : "=&v"(f.p[0]), "=&v"(f.p[1]), "=&v"(f.p[2]), "=&v"(f.p[3]) : "v"(a) : "memory");
}
DEV void pin(Frag2& f) { asm volatile("" : "+v"(f.p[0]), "+v"(f.p[1]), "+v"(f.p[2]), "+v"(f.p[3])); }
DEV bf16x8 fr_of(const Frag2& f, int i) { return __builtin_bit_cast(bf16x8, (u32x4){f.p[2 * i].x, f.p[2 * i].y, f.p[2 * i + 1].x, f.p[2 * i + 1].y}); }
DEV unsigned opq(unsigned v) { asm volatile("" : "+v"(v)); return v; }
DEV void glds(const char* src, LAS char* dst) { __builtin_amdgcn_global_load_lds((const unsigned*)src, (LAS unsigned*)dst, 16, 0, 0); }
#define AUXP1 0
#define AUXW2 0
template <int AUX> DEV void gldsa(const char* src, LAS char* dst) { __builtin_amdgcn_global_load_lds((const unsigned*)src, (LAS unsigned*)dst, 16, 0, AUX); }
DEV void glds_h(const char* src, LAS char* dst) { __builtin_amdgcn_global_load_lds((const unsigned*)src, (LAS unsigned*)dst, 16, 0, 16); }
DEV u64 ld8_async(const char* p) { u64 v; asm volatile("global_load_dwordx2 %0, %1, off sc1" : "=v"(v) : "v"(p) : "memory"); return v; }
DEV u32x4 ld16_async(const char* sbase, unsigned voff) { u32x4 v; asm volatile("global_load_dwordx4 %0, %1, %2" : "=v"(v) : "v"(voff), "s"(sbase) : "memory"); return v; }
template <int OFF> DEV void ds_w128(unsigned a, u32x4 v) { asm volatile("ds_write_b128 %0, %1 offset:%2" ::"v"(a), "v"(v), "n"(OFF) : "memory"); }
#define PREFPOL ""
#define PREFW 0
DEV void touch(const char* p, unsigned& dummy) { asm volatile("global_load_dword %0, %1, off " PREFPOL : "+v"(dummy) : "v"(p) : "memory"); }
template <int CTRL> DEV float dpp_add(float x) { return x + __builtin_bit_cast(float, __builtin_amdgcn_update_dpp(0, __builtin_bit_cast(int, x), CTRL, 0xf, 0xf, false)); }
DEV float sum16(float x) { x = dpp_add<0xB1>(x); x = dpp_add<0x4E>(x); x = dpp_add<0x141>(x); return dpp_add<0x140>(x); }
DEV float sum32(float x) {
  x = dpp_add<0xB1>(x); x = dpp_add<0x4E>(x); x = dpp_add<0x141>(x); x = dpp_add<0x140>(x);
  const auto r = __builtin_amdgcn_permlane16_swap(__float_as_uint(x), __float_as_uint(x), false, false);
  return __uint_as_float(r[0]) + __uint_as_float(r[1]);
}

#define STAMP(i) do {} while (0)
#define TR_DECL
#define TR(ph) do {} while (0)
#define TR_END() do {} while (0)

template <int N> struct IC { static constexpr int value = N; };
template <int B, int E, class F> DEV void static_for(F&& f) { if constexpr (B < E) { f(IC<B>{}); static_for<B + 1, E>(f); } }
constexpr int lead1(int k) { return k >= 7 ? LEADL : LEADE; }
constexpr int sched1(int t) { const int k = t / 14, e0 = t - (k >= 7 ? LOOKL : LOOKE), e1 = 14 * k - lead1(k) + (t - 14 * k) / 2; return k == 0 ? (t - 7 > 0 ? (t - 7) / 2 : 0) : (e0 > e1 ? e0 : e1); }
struct Ctx {
  const char *x, *Cr, *Ci, *Wr, *Wi, *w1, *b1, *w2, *b2; char* out; char* ws; LAS char* lds;
  const char* pc;
  int wid, lane, rp, cp, tp, ec, ks;
};

template <class P> DEV u64 poll(const char* p, P ok) {
  const u64 t0 = __builtin_amdgcn_s_memrealtime();
  u64 v;
  for (;;) {
    v = __hip_atomic_load((GAS u64*)p, __ATOMIC_RELAXED, __HIP_MEMORY_SCOPE_AGENT);
    if (__all(ok(v))) break;
    if (__builtin_amdgcn_s_memrealtime() - t0 > 20000000ull) break;
    __builtin_amdgcn_s_sleep(1);
  }
  return v;
}
DEV void set_flag(char* ws, int kind, int idx, u64 tag) {
  __hip_atomic_store((GAS u64*)(ws + WS_FLAG + (unsigned)(kind * 256 + idx) * FLAG_STRIDE), tag, __ATOMIC_RELAXED, __HIP_MEMORY_SCOPE_AGENT);
}
DEV u64 uniform64(u64 v) {
  const unsigned lo = __builtin_amdgcn_readfirstlane((unsigned)v), hi = __builtin_amdgcn_readfirstlane((unsigned)(v >> 32));
  return ((u64)hi << 32) | lo;
}

DEV void loader(const Ctx& c) {
  const int lw = c.wid - 4, lane = c.lane, wid = c.wid;
  TR_DECL;
  u64 pr0 = 0, pr1 = 0; (void)pr0; (void)pr1;
  LAS char* lds = c.lds; char* ws = c.ws;
  const bool typeB = c.ec == 3;
  const int G = c.cp >> 2, v = c.cp & 3;
  const int E0 = 16 * G + 3 * v;
  const int PA = 7 * G + 2 * v;
  const int PB = typeB ? PA : PA + 1;
  const int r8 = lane >> 3, p8 = lane & 7;
  const int trow0 = 64 * lw + 8 * (r8 >> 2) + (r8 & 3);
#define PROW(i) (32 * ((i) >> 2) + 16 * ((i) & 1) + 4 * (((i) >> 1) & 1))
  const unsigned swz = (unsigned)((p8 ^ r8) * 16);
  const unsigned swzJ0 = (unsigned)((p8 ^ (r8 & 2)) * 16), swzJ1 = (unsigned)((p8 ^ (r8 & 2) ^ 4) * 16);
  LAS char* ld = lds + lw * 8192;
  const char* Xl = c.x + (size_t)(c.rp * 128 + trow0) * 1024 + swz;
  auto issueX = [&](int s) {
#pragma unroll
    for (int i = 0; i < 8; ++i) gldsa<AUXP1>(Xl + PROW(i) * 1024 + s * 128, ld + (s & 3) * SLOT + i * 1024);
  };
  issueX(0);
  const char* pB[8]; unsigned strB[8];
  {
    const char* Wm = (E0 >= 32 ? c.Wi : c.Wr) + (size_t)(E0 & 31) * 16384;
#pragma unroll
    for (int i = 0; i < 8; ++i) {
      const int p = lw * 8 + i;
      const bool isJ = p >= 14, isW = !isJ && (typeB || p < 6);
      const int pr = isJ ? p - 14 : p - 6;
      const char* b1_ = c.w1 + (size_t)(pr * 8) * (F_ * 2) + (isJ ? PB : PA) * 128;
      const char* bw_ = Wm + (size_t)p * 8192;
      pB[i] = (isW ? bw_ : b1_) + (isW ? r8 * 1024 + swz : r8 * (F_ * 2) + ((i & 1) ? swzJ1 : swzJ0));
      strB[i] = isW ? 128 : (isJ ? 16 * F_ * 2 : 64 * F_ * 2);
    }
  }
  auto issueB1 = [&](int s) {
#pragma unroll
    for (int i = 0; i < 8; ++i) {
      LAS char* d = (i >= 6 && lw == 1) ? lds + 8 * SLOT + (i - 6) * 1024 + s * 2048 : ld + (4 + (s & 3)) * SLOT + i * 1024;
      gldsa<AUXP1>(pB[i], d);
      pB[i] += strB[i];
    }
  };
  issueB1(0); issueX(1); issueB1(1);
  const char* Jl = c.w1 + PB * 128 + (size_t)(64 * lw + r8) * (F_ * 2);
  const char* Cl = (c.ks >> 1 ? c.Ci : c.Cr) + (size_t)(c.tp * 128 + trow0) * 4096 + (c.ks & 1) * 2048 + swz;
  const char* ABb = ws + WS_ABT + (size_t)(((c.ks >> 1) * 4 + c.ec) * 64 + (c.ks & 1) * 32) * 8192 + lw * 8192 + lane * 16;
  const char* Hb = ws + WS_H + (size_t)(2 * c.tp + (c.ks >> 1)) * 56 * 4096 + lw * 8192 + lane * 16;
  const char* W2l = c.w2 + (c.ec * 128 + (c.ks & 1) * 64) * 2 + (64 * lw + r8) * 1024 + (p8 ^ (r8 & 6)) * 16;
  auto issueJ = [&](int j) {
#pragma unroll
    for (int i = 0; i < 8; ++i) gldsa<AUXP1>(Jl + (j * 128 + i * 8) * (F_ * 2) + ((i & 1) ? swzJ1 : swzJ0), ld + (3 + j) * SLOT + i * 1024);
  };
  auto issueC = [&](int s) {
#pragma unroll
    for (int i = 0; i < 8; ++i) gldsa<AUXC>(Cl + PROW(i) * 4096 + s * 128, ld + (s & 3) * SLOT + i * 1024);
  };
  auto issueAB = [&](int s, int slot) {
#pragma unroll
    for (int i = 0; i < 8; ++i) glds_h(ABb + s * 16384 + i * 1024, ld + slot * SLOT + i * 1024);
  };
  auto issueH = [&](int s) {
#pragma unroll
    for (int i = 0; i < 8; ++i) glds_h(Hb + s * 16384 + i * 1024, ld + (s & 3) * SLOT + i * 1024);
#pragma unroll
    for (int i = 0; i < 8; ++i) gldsa<AUXW2>(W2l + i * 8192 + s * 131072, ld + (4 + (s & 3)) * SLOT + i * 1024);
  };
  wait_vm<16>();
  bar();
  issueX(2); issueB1(2);
#pragma unroll 1
  for (int s = 0; s < 5; ++s) { issueX(s + 3); issueB1(s + 3); TR(1); wait_vm<32>(); TR(1); bar(); TR(1); }
#pragma unroll 1
  for (int s = 5; s < 7; ++s) { issueJ(s - 4); issueC(s - 5); TR(1); wait_vm<32>(); TR(1); bar(); TR(1); }
  issueJ(3); issueC(2);
  wait_vm<40>(); bar();
  issueC(3);
  const u64 tag = uniform64(*(const LAS u64*)(lds + LDS_TAG));
  wait_vm<16>(); bar();
  bar();
  STAMP(1);
  {
    const int G2 = 2 * (c.ks >> 1) + (c.ec >> 1), m = lane & 3;
    const bool act = lane < 32 && ((c.ec & 1) ? (m >= 2) : (m <= 2));
    poll(ws + WS_FLAG + (unsigned)(0 * 256 + (8 * (c.ks & 1) + ((lane >> 2) & 7)) * 16 + 4 * G2 + m) * FLAG_STRIDE, [&](u64 x) { return !act || x == tag; });
  }
  STAMP(2);
  issueAB(0, 7); issueAB(1, 8); issueAB(2, 4);
  wait_vm<16>();
  bar();
  STAMP(3);
  issueAB(3, 5); issueAB(4, 6);
  wait_vm<24>(); bar(); issueC(4); issueAB(5, 7);
  wait_vm<32>(); bar(); issueC(5); issueAB(6, 8);
  const char* hfp = ws + WS_FLAG + (unsigned)(1 * 256 + c.tp * 16 + (lane & 15)) * FLAG_STRIDE;
  u64 fv = 0;
  unsigned bseq = 0x87654;
#pragma unroll 1
  for (int s = 2; s < 11; ++s) {
    TR(2); wait_vm<40>(); TR(2); bar(); TR(2);
    issueC(s + 4); issueAB(s + 5, bseq & 15); bseq = (bseq >> 4) | ((bseq & 15) << 16);
    if (s == 7) fv = ld8_async(hfp);
    TR(2);
  }
  wait_vm<40>(); bar(); issueC(15);
  STAMP(4);
  asm volatile("" : "+v"(fv));
  if (!__all(fv == tag)) poll(hfp, [&](u64 x) { return x == tag; });
  STAMP(5);
  const char* yfp = ws + WS_FLAG + (unsigned)(2 * 256 + (c.tp * 4 + c.ec) * 4 + (lane & 3)) * FLAG_STRIDE;
#pragma unroll 1
  for (int s = 12; s < 24; ++s) { TR(3); wait_vm<32>(); TR(3); bar(); TR(3); issueH(s - 12); if (s == 21) fv = ld8_async(yfp); TR(3); }
  STAMP(6);
  wait_vm<32>();
  asm volatile("" : "+v"(fv));
  if (!__all(fv == tag)) poll(yfp, [&](u64 x) { return x == tag; });
  STAMP(7);
  bar(); issueH(12);
  wait_vm<32>(); bar(); issueH(13);
  wait_vm<32>(); bar();
  wait_vm<16>(); bar();
  wait_vm<0>(); bar();
  TR_END();
}

DEV void helper(const Ctx& c) {
  const int hw = c.wid - 6, lane = c.lane, wid = c.wid;
  LAS char* lds = c.lds; char* ws = c.ws;
  const bool typeB = c.ec == 3;
  const int G = c.cp >> 2, v = c.cp & 3, PA = 7 * G + 2 * v, PB = typeB ? PA : PA + 1;
  const int q = lane >> 4, cc = lane & 15, qq = cc >> 2, pp = lane & 3;
  unsigned dummy = 0;
  if (hw == 1) {
#pragma unroll 1
    for (int i = 0; i < PREFI; ++i) touch(c.pc + i * 8192 + lane * 128, dummy);
  }
  u64 tag;
  {
    const u64 tagv = __hip_atomic_load((GAS u64*)(ws + WS_EPOCH), __ATOMIC_RELAXED, __HIP_MEMORY_SCOPE_AGENT);
    const int row = c.rp * 128 + c.cp * 8 + 4 * hw + q;
    u32x4 xv[4];
#pragma unroll
    for (int i = 0; i < 4; ++i) xv[i] = *(const u32x4*)(c.x + (size_t)row * 1024 + cc * 16 + i * 256);
    const float bv = bflo(*(const u16*)(c.b1 + ((hw ? PB : PA) * 64 + lane) * 2));
    float s = 0.f;
#pragma unroll
    for (int i = 0; i < 16; ++i) s += bflo(xv[i >> 2][i & 3]) + bfhi(xv[i >> 2][i & 3]);
    const float m = sum16(s) * (1.f / 512.f);
    float z = 0.f;
#pragma unroll
    for (int i = 0; i < 16; ++i) { const float d0 = bflo(xv[i >> 2][i & 3]) - m, d1 = bfhi(xv[i >> 2][i & 3]) - m; z += d0 * d0; z += d1 * d1; }
    const float var = sum16(z) * (1.f / 512.f);
    const float sd = __builtin_amdgcn_sqrtf(rbf(rbf(var) + 1e-5f));
    tag = uniform64(tagv) + 1;
    if (cc == 0)
      __hip_atomic_store((GAS u64*)(ws + WS_ROWST + (size_t)row * 8), ((u64)(unsigned)tag << 32) | pk_bf16(m, sd), __ATOMIC_RELAXED, __HIP_MEMORY_SCOPE_AGENT);
    *(LAS float*)(lds + LDS_B1 + (hw * 64 + lane) * 4) = bv;
    if (hw == 0 && lane == 0) { *(LAS u64*)(lds + LDS_TAG) = tag; *(LAS u64*)(lds + LDS_TAG + 8) = 0; }
  }
  const unsigned wa0 = cc * 128 + ((q ^ (cc & 7)) * 16), wa1 = cc * 128 + (((4 + q) ^ (cc & 7)) * 16);
  const unsigned wa = hw ? wa1 : wa0;
  unsigned ja[4];
#pragma unroll
  for (int n = 0; n < 4; ++n) ja[n] = (8 * q + qq) * 128 + (((2 * n + (pp >> 1)) ^ ((qq & 2) | ((q & 1) << 2))) * 16) + 8 * (pp & 1) + hw * 4096;
  const bf16x8 ones = {0x3f80, 0x3f80, 0x3f80, 0x3f80, 0x3f80, 0x3f80, 0x3f80, 0x3f80};
  const f32x4 z4 = {0.f, 0.f, 0.f, 0.f};
  f32x4 cs[7] = {z4, z4, z4, z4, z4, z4, z4};
  const int srow = 32 * (lane >> 5) + 8 * qq + 4 * (q & 1) + (lane & 3);
  const char* stp = ws + WS_ROWST + (size_t)(c.rp * 128 + srow) * 8;
  wait_lgkm0();
  bar();
  auto csum = [&](int s) {
    LAS char* ba = lds + (4 + (s & 3)) * SLOT;
    bf16x8 f[7];
#pragma unroll
    for (int n = 0; n < 3; ++n) f[n] = ldsb128(ba + n * 2048 + wa);
    if (typeB) {
#pragma unroll
      for (int n = 3; n < 7; ++n) f[n] = ldsb128(ba + n * 2048 + wa);
    } else {
#pragma unroll
      for (int n = 3; n < 7; ++n) f[n] = ldstr(ba + 6144 + ja[n - 3], 512);
    }
#pragma unroll
    for (int n = 0; n < 7; ++n) cs[n] = MFMA(ones, f[n], cs[n]);
  };
  u64 st0 = 0, st1 = 0;
  const char* Cp = (c.ks >> 1 ? c.Ci : c.Cr) + (size_t)(c.tp * 128 + c.ec * 32 + q) * 4096 + (c.ks & 1) * 2048 + cc * 128;
#pragma unroll 1
  for (int s = 0; s < 7; ++s) {
    if (hw == 0) { if (s == 4) { st0 = ld8_async(stp); st1 = ld8_async(stp + 512); } }
    csum(s);
    wait_lgkm0();
    bar();
  }
  {
    csum(7);
    if (hw == 0) {
      wait_vm<0>();
      asm volatile("" : "+v"(st0), "+v"(st1));
      const unsigned t32 = (unsigned)tag;
      if (!__all((unsigned)(st0 >> 32) == t32)) st0 = poll(stp, [&](u64 x) { return (unsigned)(x >> 32) == t32; });
      if (!__all((unsigned)(st1 >> 32) == t32)) st1 = poll(stp + 512, [&](u64 x) { return (unsigned)(x >> 32) == t32; });
      *(LAS float*)(lds + LDS_MEAN + lane * 4) = bflo((unsigned)st0);
      *(LAS float*)(lds + LDS_RSTD + lane * 4) = __builtin_amdgcn_rcpf(bfhi((unsigned)st0));
      *(LAS float*)(lds + LDS_MEAN + 256 + lane * 4) = bflo((unsigned)st1);
      *(LAS float*)(lds + LDS_RSTD + 256 + lane * 4) = __builtin_amdgcn_rcpf(bfhi((unsigned)st1));
    }
    if (lane < 16) {
#pragma unroll
      for (int n = 0; n < 7; ++n) *(LAS float*)(lds + LDS_CS + ((hw * 11 + n) * 16 + lane) * 4) = cs[n][0];
    }
    wait_lgkm0();
    bar();
  }
#pragma unroll
  for (int n = 0; n < 4; ++n) cs[n] = z4;
#pragma unroll 1
  for (int h = 0; h < 2; ++h) {
#pragma unroll
    for (int j = 0; j < 2; ++j) {
      LAS char* ba = lds + ((h | j) == 0 ? 8 : 3 + 2 * h + j) * SLOT;
#pragma unroll
      for (int k = 0; k < 2; ++k)
#pragma unroll
        for (int n = 0; n < 4; ++n) cs[n] = MFMA(ones, ldstr(ba + ja[n] + k * 8192, 512), cs[n]);
    }
    if (h == 1 && lane < 16) {
#pragma unroll
      for (int n = 0; n < 4; ++n) *(LAS float*)(lds + LDS_CS + ((hw * 11 + 7 + n) * 16 + lane) * 4) = cs[n][0];
    }
    wait_lgkm0();
    bar();
  }
#pragma unroll 1
  for (int s = 0; s < 30; ++s) {
    bar();
  }
  wait_vm<0>();
  asm volatile("" : "+v"(dummy));
  if (blockIdx.x < 2) {
    if (blockIdx.x == 0 && hw == 0) {
      const char* fp = ws + WS_FLAG + (unsigned)lane * FLAG_STRIDE;
      const u64 t0 = __builtin_amdgcn_s_memrealtime();
      for (;;) {
        u64 f[4];
#pragma unroll
        for (int i = 0; i < 4; ++i) f[i] = __hip_atomic_load((GAS u64*)(fp + i * 64 * FLAG_STRIDE), __ATOMIC_RELAXED, __HIP_MEMORY_SCOPE_AGENT);
        if (__all(f[0] == tag && f[1] == tag && f[2] == tag && f[3] == tag)) break;
        if (__builtin_amdgcn_s_memrealtime() - t0 > 20000000ull) break;
        __builtin_amdgcn_s_sleep(1);
      }
      if (lane == 0) __hip_atomic_store((GAS u64*)(ws + WS_EPOCH), tag, __ATOMIC_RELAXED, __HIP_MEMORY_SCOPE_AGENT);
    }
  }
}

DEV void consumer(const Ctx& c) {
  const int w = c.wid, lane = c.lane, wid = c.wid;
  TR_DECL;
  u64 pr0 = 0, pr1 = 0; (void)pr0; (void)pr1;
  LAS char* lds = c.lds; char* ws = c.ws;
  const int q = lane >> 4, cc = lane & 15, qq = cc >> 2, pp = lane & 3;
  const bool typeB = c.ec == 3;
  const int G = c.cp >> 2, v = c.cp & 3, E0 = 16 * G + 3 * v, PA = 7 * G + 2 * v, PB = typeB ? PA : PA + 1;
  const auto rs = __builtin_amdgcn_make_buffer_rsrc(ws, 0, WS_BYTES, 0x00020000);
  STAMP(0);
  const unsigned wa0 = cc * 128 + ((q ^ (cc & 7)) * 16), wa1 = cc * 128 + (((4 + q) ^ (cc & 7)) * 16);
  unsigned ja[4];
#pragma unroll
  for (int n = 0; n < 4; ++n) ja[n] = opq(4 * SLOT + (8 * q + qq) * 128 + (((2 * n + (pp >> 1)) ^ ((qq & 2) | ((q & 1) << 2))) * 16) + 8 * (pp & 1));
  bf16x8 xf[2][16];
  auto h_epi = [&](f32x4 (&acc)[4][2], int brow, int P, int csrow) {
    float mean[2], rstd[2];
#pragma unroll
    for (int u = 0; u < 2; ++u) { mean[u] = *(const LAS float*)(lds + LDS_MEAN + ((2 * w + u) * 16 + cc) * 4); rstd[u] = *(const LAS float*)(lds + LDS_RSTD + ((2 * w + u) * 16 + cc) * 4); }
    f32x4 cs[4], bv[4];
#pragma unroll
    for (int n = 0; n < 4; ++n) {
      cs[n] = *(const LAS f32x4*)(lds + LDS_CS + ((csrow + n) * 16 + 4 * q) * 4) + *(const LAS f32x4*)(lds + LDS_CS + ((11 + csrow + n) * 16 + 4 * q) * 4);
      bv[n] = *(const LAS f32x4*)(lds + LDS_B1 + (brow * 64 + n * 16 + 4 * q) * 4);
    }
    const unsigned hb = WS_H + (unsigned)((((2 * c.rp + (w >> 1)) * 56 + 2 * P) * 4 + 2 * (w & 1)) * 1024);
#pragma unroll
    for (int b = 0; b < 2; ++b)
#pragma unroll
      for (int u = 0; u < 2; ++u) {
        unsigned d[4];
#pragma unroll
        for (int h = 0; h < 2; ++h) {
          const int n = 2 * b + h;
          f32x4 z = (acc[n][u] - mean[u] * cs[n]) * rstd[u] + bv[n];
          z = __builtin_elementwise_max(z, (f32x4){0.f, 0.f, 0.f, 0.f});
          z = z * z;
          d[2 * h] = pk_bf16(z[0], z[1]); d[2 * h + 1] = pk_bf16(z[2], z[3]);
        }
        __builtin_amdgcn_raw_buffer_store_b128((u32x4){d[0], d[1], d[2], d[3]}, rs, lane * 16 + u * 1024 + hb + b * 4096, 0, 16);
      }
  };
  constexpr int L2 = 8;
  bf16x8 g[64];
  auto rd2 = [&](auto t_) {
    constexpr int t = decltype(t_)::value, j = t / 16, k = (t / 4) & 3, n = t & 3;
    if constexpr (j == 0) g[t] = ldstr(lds + 4 * SLOT + ja[n] + k * 4096, 512); else g[t] = ldstr(lds + (j - 1) * SLOT + ja[n] + k * 4096, 512);
  };
  bar();
  STAMP(1);
  auto pass1 = [&](auto na_) {
    constexpr int NA = decltype(na_)::value;
    f32x4 acc[7][2];
#pragma unroll
    for (int n = 0; n < 7; ++n) { acc[n][0] = (f32x4){0, 0, 0, 0}; acc[n][1] = (f32x4){0, 0, 0, 0}; }
    bf16x8 fr[112];
    const unsigned bx0 = opq(wa0 + w * 4096), bx1 = opq(wa1 + w * 4096), bw0 = opq(wa0 + 4 * SLOT), bw1 = opq(wa1 + 4 * SLOT);
    auto rd = [&](auto t_) {
      constexpr int t = decltype(t_)::value, kk = t / 7, n = t % 7, so = ((kk >> 1) & 3) * SLOT;
      if constexpr (n == 0) { xf[0][kk] = ldsb128(lds + ((kk & 1) ? bx1 : bx0) + so); xf[1][kk] = ldsb128(lds + ((kk & 1) ? bx1 : bx0) + so + 2048); }
      if constexpr (n < NA) fr[t] = ldsb128(lds + ((kk & 1) ? bw1 : bw0) + so + n * 2048);
      else fr[t] = ldstr(lds + ja[(n - 3) & 3] + so + 6144 + (kk & 1) * 4096, 512);
    };
    static_for<0, 7>(rd);
    SCHED();
    static_for<0, 112>([&](auto p_) {
      constexpr int p = decltype(p_)::value, kk = p / 7, n = p % 7;
      constexpr int kn = p / 14 + 1;
      if constexpr (p == 14 * kn - lead1(kn)) {
        TR(1);
        wait_lgkm0();
        TR(1);
        bar();
        TR(1);
        SCHED();
      }
      const bf16x8 f = fr[p];
      if constexpr (n < NA) acc[n][0] = MFMA(xf[0][kk], f, acc[n][0]); else acc[n][0] = MFMA(f, xf[0][kk], acc[n][0]);
      SCHED();
      static_for<p + 1, p + 15>([&](auto t_) {
        constexpr int t = decltype(t_)::value;
        if constexpr (t < 112 && t >= 7 && sched1(t) == p) { rd(t_); SCHED(); }
      });
      if constexpr (n < NA) acc[n][1] = MFMA(xf[1][kk], f, acc[n][1]); else acc[n][1] = MFMA(f, xf[1][kk], acc[n][1]);
      SCHED();
    });
    STAMP(2);
    f32x4 mr[2], rr[2];
    const float sgn = E0 >= 32 ? -1.f : 1.f;
#pragma unroll
    for (int u = 0; u < 2; ++u) {
      mr[u] = *(const LAS f32x4*)(lds + LDS_MEAN + ((2 * w + u) * 16 + 4 * q) * 4);
      rr[u] = *(const LAS f32x4*)(lds + LDS_RSTD + ((2 * w + u) * 16 + 4 * q) * 4) * sgn;
    }
    float cs[NA];
#pragma unroll
    for (int n = 0; n < NA; ++n) cs[n] = *(const LAS float*)(lds + LDS_CS + (n * 16 + cc) * 4) + *(const LAS float*)(lds + LDS_CS + ((11 + n) * 16 + cc) * 4);
#pragma unroll
    for (int n = 0; n < NA; ++n) {
      const f32x4 o0 = (acc[n][0] - mr[0] * cs[n]) * rr[0], o1 = (acc[n][1] - mr[1] * cs[n]) * rr[1];
      u32x4 d = {pk_bf16(o0[0], o0[1]), pk_bf16(o0[2], o0[3]), pk_bf16(o1[0], o1[1]), pk_bf16(o1[2], o1[3])};
      const int E = E0 + n;
      __builtin_amdgcn_raw_buffer_store_b128(d, rs, lane * 16 + WS_ABT + (unsigned)((((E >> 3) * 64 + c.rp * 4 + w) * 8 + (E & 7)) * 1024), 0, 16);
    }
    SCHED();
    if (NA == 3) {
      f32x4 (&ah)[4][2] = *(f32x4(*)[4][2]) & acc[3];
      h_epi(ah, 0, PA, 3);
    }
  };
  if (typeB) pass1(IC<7>{}); else pass1(IC<3>{});
  SCHED();
  static_for<0, L2>(rd2);
  const u64 tag = uniform64(*(const LAS u64*)(lds + LDS_TAG));
  STAMP(3);
  {
    f32x4 acc[4][2];
#pragma unroll
    for (int n = 0; n < 4; ++n) { acc[n][0] = (f32x4){0, 0, 0, 0}; acc[n][1] = (f32x4){0, 0, 0, 0}; }
    SCHED();
    static_for<0, 64>([&](auto p_) {
      constexpr int p = decltype(p_)::value, kk = p / 4, n = p & 3;
      if constexpr (p == 16) {
        wait_vm<0>();
        bar();
        if (w == 0 && lane == 0) set_flag(ws, 0, c.rp * 16 + c.cp, tag);
        SCHED();
      }
      if constexpr (p == 32) { bar(); SCHED(); }
      acc[n][0] = MFMA(g[p], xf[0][kk], acc[n][0]);
      SCHED();
      if constexpr (p + L2 < 64) { rd2(IC<p + L2>{}); SCHED(); }
      acc[n][1] = MFMA(g[p], xf[1][kk], acc[n][1]);
      SCHED();
    });
    STAMP(11);
    h_epi(acc, 1, PB, 7);
  }
  STAMP(4);
  const int wt = w >> 1, we = w & 1;
  unsigned wb[2];
#pragma unroll
  for (int n = 0; n < 2; ++n) wb[n] = 4 * SLOT + (4 * q + qq) * 128 + (((2 * (2 * we + n) + (pp >> 1)) ^ ((4 * q + qq) & 6)) * 16) + 8 * (pp & 1);
  const unsigned hb = wt * 2048 + lane * 16;
  bf16x8 hA[4][2], wA[4][2], hB[4][2], wB[4][2];
  wait_lgkm0();
  bar();
  STAMP(5);
#define PREAD(HN, WN, SO) if (i < 12) { if (i % 3 == 2) { HN[i / 3][0] = ldsb128(lds + (SO) + hb + (i / 3) * 4096); HN[i / 3][1] = ldsb128(lds + (SO) + hb + (i / 3) * 4096 + 1024); } \
                                        else WN[i / 3][i % 3] = ldstr(lds + (SO) + wb[i % 3] + (i / 3) * 4096, 2048); SCHED(); }
  {
    f32x4 acc[4][4];
#pragma unroll
    for (int m = 0; m < 4; ++m)
#pragma unroll
      for (int n = 0; n < 4; ++n) acc[m][n] = (f32x4){0, 0, 0, 0};
    const unsigned a0 = wt * 8192 + wa0, a1 = wt * 8192 + wa1, b0 = we * 4096 + lane * 16;
    bf16x8 fa0[4], fb0[4], fa1[4], fb1[4];
#pragma unroll
    for (int m = 0; m < 4; ++m) { fa0[m] = ldsb128(lds + a0 + m * 2048); fb0[m] = ldsb128(lds + b0 + 7 * SLOT + m * 1024); }
#define HALF(FA, FB, NA_, NB_, AOFF, BOFF) do { _Pragma("unroll") for (int i = 0; i < 16; ++i) { \
      acc[i >> 2][i & 3] = MFMA(FB[i & 3], FA[i >> 2], acc[i >> 2][i & 3]); SCHED(); \
      if (i < 8) { if (i == 0) NB_[0] = ldsb128(lds + (BOFF)); else if (i == 1) NA_[0] = ldsb128(lds + (AOFF)); \
        else if (i < 5) NB_[i - 1] = ldsb128(lds + (BOFF) + (i - 1) * 1024); else NA_[i - 4] = ldsb128(lds + (AOFF) + (i - 4) * 2048); SCHED(); } } } while (0)
    unsigned so = 0, sb = 7 * SLOT, bseq = 0x76548;
#pragma unroll 1
    for (int seg = 0; seg < 2; ++seg) {
      const int n = seg ? 12 : 3;
#pragma unroll 1
      for (int s = 0; s < n; ++s) {
        HALF(fa0, fb0, fa1, fb1, so + a1, sb + b0 + 8192);
        TR(2);
        wait_lgkm0();
        TR(2);
        bar();
        TR(2);
        sb = (bseq & 15) * SLOT; bseq = (bseq >> 4) | ((bseq & 15) << 16); so = (so + SLOT) & (4 * SLOT - 1);
        HALF(fa1, fb1, fa0, fb0, so + a0, sb + b0);
      }
      if (seg == 0) {
        wait_vm<0>();
        unsigned old = 0;
        if (lane == 0) old = __hip_atomic_fetch_add((LAS unsigned*)(lds + LDS_TAG + 8), 1u, __ATOMIC_RELAXED, __HIP_MEMORY_SCOPE_WORKGROUP);
        if (lane == 0 && old == 3) set_flag(ws, 1, c.rp * 16 + c.cp, tag);
      }
    }
    HALF(fa0, fb0, fa1, fb1, so + a1, sb + b0 + 8192);
    wait_lgkm0();
    bar();
#pragma unroll
    for (int i = 0; i < 16; ++i) {
      acc[i >> 2][i & 3] = MFMA(fb1[i & 3], fa1[i >> 2], acc[i >> 2][i & 3]); SCHED();
      PREAD(hA, wA, 0)
    }
    STAMP(6);
    if (w == c.ks) {
#pragma unroll
      for (int m = 0; m < 4; ++m)
#pragma unroll
        for (int n = 0; n < 4; ++n) *(LAS f32x4*)(lds + 8 * SLOT + (m * 4 + n) * 1024 + lane * 16) = acc[m][n];
    } else {
      const unsigned yb = WS_YP + (unsigned)((((c.tp * 4 + c.ec) * 4 + c.ks) * 4 + w) * 8192);
#pragma unroll
      for (int m = 0; m < 4; ++m)
#pragma unroll
        for (int j = 0; j < 2; ++j) {
          const f32x4 e = acc[m][2 * j], o = acc[m][2 * j + 1];
          __builtin_amdgcn_raw_buffer_store_b128((u32x4){pk_bf16(e[0], e[1]), pk_bf16(e[2], e[3]), pk_bf16(o[0], o[1]), pk_bf16(o[2], o[3])}, rs, lane * 16 + (m * 2 + j) * 1024 + yb, 0, 16);
        }
    }
  }
  STAMP(7);
  {
    f32x4 acc[2][2];
#pragma unroll
    for (int m = 0; m < 2; ++m) { acc[m][0] = (f32x4){0, 0, 0, 0}; acc[m][1] = (f32x4){0, 0, 0, 0}; }
    u32x4 yp[3][2];
    u32x2 xr[2][2], b2r[2];
    const int trow = c.tp * 128 + (c.ks >> 1) * 64 + 32 * wt + 8 * qq + (cc & 3);
    const int ecol = c.ec * 128 + (c.ks & 1) * 64 + 32 * we + 4 * q;
    unsigned so = 0;
#define PSTEP(HC, WC, HN, WN) do { wait_lgkm0(); TR(3); bar(); TR(3); so = (so + SLOT) & (4 * SLOT - 1); SCHED(); \
      _Pragma("unroll") for (int i = 0; i < 16; ++i) { \
        acc[(i >> 1) & 1][i & 1] = MFMA(WC[i >> 2][i & 1], HC[i >> 2][(i >> 1) & 1], acc[(i >> 1) & 1][i & 1]); SCHED(); \
        PREAD(HN, WN, so) } } while (0)
#pragma unroll 1
    for (int seg = 0; seg < 2; ++seg) {
#pragma unroll 1
      for (int s = 0; s < 2; ++s) { PSTEP(hA, wA, hB, wB); PSTEP(hB, wB, hA, wA); }
      if (seg == 0) {
        wait_vm<0>();
        unsigned old = 0;
        if (lane == 0) old = __hip_atomic_fetch_add((LAS unsigned*)(lds + LDS_TAG + 12), 1u, __ATOMIC_RELAXED, __HIP_MEMORY_SCOPE_WORKGROUP);
        if (lane == 0 && old == 3) set_flag(ws, 2, (c.tp * 4 + c.ec) * 4 + c.ks, tag);
      }
    }
    PSTEP(hA, wA, hB, wB);
    STAMP(8);
    {
      const unsigned yb = WS_YP + (unsigned)(((c.tp * 4 + c.ec) * 16 + c.ks) * 8192);
#pragma unroll
      for (int i = 0; i < 3; ++i) {
        const int src = (c.ks + 1 + i) & 3;
#pragma unroll
        for (int m = 0; m < 2; ++m) yp[i][m] = __builtin_amdgcn_raw_buffer_load_b128(rs, lane * 16 + ((2 * wt + m) * 2 + we) * 1024, yb + src * 32768, 16);
      }
#pragma unroll
      for (int m = 0; m < 2; ++m)
#pragma unroll
        for (int n = 0; n < 2; ++n) xr[m][n] = *(const u32x2*)(c.x + (size_t)(trow + 4 * m) * 1024 + (ecol + 16 * n) * 2);
#pragma unroll
      for (int n = 0; n < 2; ++n) b2r[n] = *(const u32x2*)(c.b2 + (ecol + 16 * n) * 2);
    }
    SCHED();
    PSTEP(hB, wB, hA, wA);
    PSTEP(hA, wA, hB, wB);
    PSTEP(hB, wB, hA, wA);
    PSTEP(hA, wA, hB, wB);
    f32x4 yo[2][2];
#pragma unroll
    for (int m = 0; m < 2; ++m)
#pragma unroll
      for (int n = 0; n < 2; ++n) yo[m][n] = *(const LAS f32x4*)(lds + 8 * SLOT + ((2 * wt + m) * 4 + 2 * we + n) * 1024 + lane * 16);
#pragma unroll
    for (int i = 0; i < 16; ++i) acc[(i >> 1) & 1][i & 1] = MFMA(wB[i >> 2][i & 1], hB[i >> 2][(i >> 1) & 1], acc[(i >> 1) & 1][i & 1]);
    STAMP(9);
#pragma unroll
    for (int m = 0; m < 2; ++m)
#pragma unroll
      for (int n = 0; n < 2; ++n) {
        f32x4 y = yo[m][n];
#pragma unroll
        for (int i = 0; i < 3; ++i) y += (f32x4){bflo(yp[i][m][2 * n]), bfhi(yp[i][m][2 * n]), bflo(yp[i][m][2 * n + 1]), bfhi(yp[i][m][2 * n + 1])};
        const float bv[4] = {bflo(b2r[n].x), bfhi(b2r[n].x), bflo(b2r[n].y), bfhi(b2r[n].y)};
        const float xv[4] = {bflo(xr[m][n].x), bfhi(xr[m][n].x), bflo(xr[m][n].y), bfhi(xr[m][n].y)};
        float o[4];
#pragma unroll
        for (int r = 0; r < 4; ++r) {
          const float z = acc[m][n][r] + bv[r];
          const float g = z * __builtin_amdgcn_rcpf(1.f + __expf(-z));
          o[r] = xv[r] + y[r] * g;
        }
        u32x2 d = {pk_bf16(o[0], o[1]), pk_bf16(o[2], o[3])};
        *(u32x2*)(c.out + (size_t)(trow + 4 * m) * 1024 + (ecol + 16 * n) * 2) = d;
      }
    STAMP(10);
    TR_END();
  }
}

__global__ __launch_bounds__(512, 2) void fnet_mlp_fused(const char* x, const char* Cr, const char* Ci, const char* Wr, const char* Wi,
                                                         const char* w1, const char* b1, const char* w2, const char* b2, char* out, char* ws) {
  extern __shared__ __attribute__((aligned(16))) char shm[];
  Ctx c;
  c.x = x; c.Cr = Cr; c.Ci = Ci; c.Wr = Wr; c.Wi = Wi; c.w1 = w1; c.b1 = b1; c.w2 = w2; c.b2 = b2; c.out = out; c.ws = ws;
  c.lds = (LAS char*)shm;
  { u64 pc; asm volatile("s_getpc_b64 %0" : "=s"(pc)); c.pc = (const char*)(pc & ~(u64)255); }
  c.wid = __builtin_amdgcn_readfirstlane(threadIdx.x >> 6); c.lane = threadIdx.x & 63;
  const int b = blockIdx.x, xcd = b & 7, r = b >> 3;
  c.ks = xcd >> 1; c.tp = (xcd & 1) * 8 + (r >> 2); c.ec = r & 3;
  c.rp = c.tp; c.cp = c.ks * 4 + c.ec;
  if (c.wid >= 6) helper(c); else if (c.wid >= 4) loader(c); else consumer(c);
}

#define WSOFF(n) 0
extern "C" void kernel_launch(void* const* d_in, const int* in_sizes, int n_in, void* d_out, int out_size, void* d_ws, size_t ws_size, hipStream_t stream) {
  (void)hipFuncSetAttribute((const void*)fnet_mlp_fused, hipFuncAttributeMaxDynamicSharedMemorySize, LDS_BYTES);
  fnet_mlp_fused<<<256, 512, LDS_BYTES, stream>>>((const char*)d_in[0], (const char*)d_in[1], (const char*)d_in[2], (const char*)d_in[3], (const char*)d_in[4],
                                                  (const char*)d_in[6], (const char*)d_in[7], (const char*)d_in[8], (const char*)d_in[9], (char*)d_out, (char*)d_ws + WSOFF(ws_size));
}
```

```cpp
#define AUXC 0
#define PREFC 1
#define PREFS 5
#define PREFI 4
#define PREFW 4
#include <hip/hip_runtime.h>
typedef unsigned short u16;
typedef unsigned long long u64;
typedef short bf16x8 __attribute__((ext_vector_type(8)));
typedef short s16x4 __attribute__((ext_vector_type(4)));
typedef float f32x4 __attribute__((ext_vector_type(4)));
typedef float f32x2 __attribute__((ext_vector_type(2)));
typedef unsigned u32x4 __attribute__((ext_vector_type(4)));
typedef unsigned u32x2 __attribute__((ext_vector_type(2)));
typedef __bf16 bf2 __attribute__((ext_vector_type(2)));
#define LAS __attribute__((address_space(3)))
#define GAS __attribute__((address_space(1)))
#define DEV __device__ __forceinline__

constexpr int S_ = 2048, D_ = 512, F_ = 1792;
constexpr unsigned WS_EPOCH = 0, WS_FLAG = 4096, FLAG_STRIDE = 128, WS_ROWST = 256u << 10;
constexpr unsigned WS_ABT = 1u << 20, WS_H = 8u << 20, WS_YP = 16u << 20, WS_BYTES = 32u << 20;
constexpr int SLOT = 16384, NSLOT = 9;
#define LEADE 6
#define LOOKE 13
#define LEADL 5
#define LOOKL 10
constexpr int LDS_MEAN = NSLOT * SLOT, LDS_RSTD = LDS_MEAN + 512, LDS_CS = LDS_RSTD + 512;
constexpr int LDS_B1 = LDS_CS + 22 * 64;
constexpr int LDS_TAG = LDS_B1 + 512;
constexpr int LDS_DUMP = LDS_TAG + 16;
constexpr int LDS_BYTES = LDS_DUMP + 256;

template <int N> DEV void wait_vm() { asm volatile("s_waitcnt vmcnt(%0)" ::"n"(N) : "memory"); }
DEV void wait_lgkm0() { asm volatile("s_waitcnt lgkmcnt(0)" ::: "memory"); }
DEV void bar() { asm volatile("" ::: "memory"); __builtin_amdgcn_s_barrier(); asm volatile("" ::: "memory"); }
#define SCHED() __builtin_amdgcn_sched_barrier(0)
#define MFMA(a, b, c) __builtin_amdgcn_mfma_f32_16x16x32_bf16((a), (b), (c), 0, 0, 0)
DEV unsigned pk_bf16(float a, float b) { f32x2 f = {a, b}; return __builtin_bit_cast(unsigned, __builtin_convertvector(f, bf2)); }
DEV float rbf(float a) { return __uint_as_float(pk_bf16(a, 0.f) << 16); }
DEV float bflo(unsigned v) { return __uint_as_float(v << 16); }
DEV float bfhi(unsigned v) { return __uint_as_float(v & 0xffff0000u); }
DEV bf16x8 ldsb128(LAS char* p) { return *(const LAS bf16x8*)p; }
DEV bf16x8 ldstr(LAS char* p, int second) {
  s16x4 a = __builtin_amdgcn_ds_read_tr16_b64_v4i16((LAS s16x4*)p);
  s16x4 b = __builtin_amdgcn_ds_read_tr16_b64_v4i16((LAS s16x4*)(p + second));
  return __builtin_shufflevector(a, b, 0, 1, 2, 3, 4, 5, 6, 7);
}
struct Frag2 { u32x2 p[4]; };
DEV void rd2_b64x4(unsigned a0, unsigned a1, Frag2& f) {
  asm volatile("ds_read_b64 %0, %4\n\tds_read_b64 %1, %4 offset:8\n\tds_read_b64 %2, %5\n\tds_read_b64 %3, %5 offset:8"
               : "=&v"(f.p[0]), "=&v"(f.p[1]), "=&v"(f.p[2]), "=&v"(f.p[3]) : "v"(a0), "v"(a1) : "memory");
}
DEV void rd2_tr(unsigned a, Frag2& f) {
  asm volatile("ds_read_b64_tr_b16 %0, %4\n\tds_read_b64_tr_b16 %1, %4 offset:512\n\tds_read_b64_tr_b16 %2, %4 offset:4096\n\tds_read_b64_tr_b16 %3, %4 offset:4608"
               : "=&v"(f.p[0]), "=&v"(f.p[1]), "=&v"(f.p[2]), "=&v"(f.p[3]) : "v"(a) : "memory");
}
DEV void pin(Frag2& f) { asm volatile("" : "+v"(f.p[0]), "+v"(f.p[1]), "+v"(f.p[2]), "+v"(f.p[3])); }
DEV bf16x8 fr_of(const Frag2& f, int i) { return __builtin_bit_cast(bf16x8, (u32x4){f.p[2 * i].x, f.p[2 * i].y, f.p[2 * i + 1].x, f.p[2 * i + 1].y}); }
DEV unsigned opq(unsigned v) { asm volatile("" : "+v"(v)); return v; }
DEV void glds(const char* src, LAS char* dst) { __builtin_amdgcn_global_load_lds((const unsigned*)src, (LAS unsigned*)dst, 16, 0, 0); }
#define AUXP1 0
#define AUXW2 0
template <int AUX> DEV void gldsa(const char* src, LAS char* dst) { __builtin_amdgcn_global_load_lds((const unsigned*)src, (LAS unsigned*)dst, 16, 0, AUX); }
DEV void glds_h(const char* src, LAS char* dst) { __builtin_amdgcn_global_load_lds((const unsigned*)src, (LAS unsigned*)dst, 16, 0, 16); }
DEV u64 ld8_async(const char* p) { u64 v; asm volatile("global_load_dwordx2 %0, %1, off sc1" : "=v"(v) : "v"(p) : "memory"); return v; }
DEV u32x4 ld16_async(const char* sbase, unsigned voff) { u32x4 v; asm volatile("global_load_dwordx4 %0, %1, %2" : "=v"(v) : "v"(voff), "s"(sbase) : "memory"); return v; }
template <int OFF> DEV void ds_w128(unsigned a, u32x4 v) { asm volatile("ds_write_b128 %0, %1 offset:%2" ::"v"(a), "v"(v), "n"(OFF) : "memory"); }
#define PREFPOL ""
DEV void touch(const char* p, unsigned dump) {
  unsigned keep;
  asm volatile("s_mov_b32 %0, m0\n\ts_mov_b32 m0, %2\n\ts_nop 0\n\tglobal_load_lds_dword %1, off " PREFPOL "\n\ts_mov_b32 m0, %0" : "=&s"(keep) : "v"(p), "s"(dump) : "memory");
}
template <int CTRL> DEV float dpp_add(float x) { return x + __builtin_bit_cast(float, __builtin_amdgcn_update_dpp(0, __builtin_bit_cast(int, x), CTRL, 0xf, 0xf, false)); }
DEV float sum16(float x) { x = dpp_add<0xB1>(x); x = dpp_add<0x4E>(x); x = dpp_add<0x141>(x); return dpp_add<0x140>(x); }
DEV float sum32(float x) {
  x = dpp_add<0xB1>(x); x = dpp_add<0x4E>(x); x = dpp_add<0x141>(x); x = dpp_add<0x140>(x);
  const auto r = __builtin_amdgcn_permlane16_swap(__float_as_uint(x), __float_as_uint(x), false, false);
  return __uint_as_float(r[0]) + __uint_as_float(r[1]);
}

#define STAMP(i) do {} while (0)
#define TR_DECL
#define TR(ph) do {} while (0)
#define TR_END() do {} while (0)

template <int N> struct IC { static constexpr int value = N; };
template <int B, int E, class F> DEV void static_for(F&& f) { if constexpr (B < E) { f(IC<B>{}); static_for<B + 1, E>(f); } }
constexpr int lead1(int k) { return k >= 7 ? LEADL : LEADE; }
constexpr int sched1(int t) { const int k = t / 14, e0 = t - (k >= 7 ? LOOKL : LOOKE), e1 = 14 * k - lead1(k) + (t - 14 * k) / 2; return k == 0 ? (t - 7 > 0 ? (t - 7) / 2 : 0) : (e0 > e1 ? e0 : e1); }
struct Ctx {
  const char *x, *Cr, *Ci, *Wr, *Wi, *w1, *b1, *w2, *b2; char* out; char* ws; LAS char* lds;
  const char* pc;
  int wid, lane, rp, cp, tp, ec, ks;
};

template <class P> DEV u64 poll(const char* p, P ok) {
  const u64 t0 = __builtin_amdgcn_s_memrealtime();
  u64 v;
  for (;;) {
    v = __hip_atomic_load((GAS u64*)p, __ATOMIC_RELAXED, __HIP_MEMORY_SCOPE_AGENT);
    if (__all(ok(v))) break;
    if (__builtin_amdgcn_s_memrealtime() - t0 > 20000000ull) break;
    __builtin_amdgcn_s_sleep(1);
  }
  return v;
}
DEV void set_flag(char* ws, int kind, int idx, u64 tag) {
  __hip_atomic_store((GAS u64*)(ws + WS_FLAG + (unsigned)(kind * 256 + idx) * FLAG_STRIDE), tag, __ATOMIC_RELAXED, __HIP_MEMORY_SCOPE_AGENT);
}
DEV u64 uniform64(u64 v) {
  const unsigned lo = __builtin_amdgcn_readfirstlane((unsigned)v), hi = __builtin_amdgcn_readfirstlane((unsigned)(v >> 32));
  return ((u64)hi << 32) | lo;
}

DEV void loader(const Ctx& c) {
  const int lw = c.wid - 4, lane = c.lane, wid = c.wid;
  TR_DECL;
  u64 pr0 = 0, pr1 = 0; (void)pr0; (void)pr1;
  LAS char* lds = c.lds; char* ws = c.ws;
  const bool typeB = c.ec == 3;
  const int G = c.cp >> 2, v = c.cp & 3;
  const int E0 = 16 * G + 3 * v;
  const int PA = 7 * G + 2 * v;
  const int PB = typeB ? PA : PA + 1;
  const int r8 = lane >> 3, p8 = lane & 7;
  const int trow0 = 64 * lw + 8 * (r8 >> 2) + (r8 & 3);
#define PROW(i) (32 * ((i) >> 2) + 16 * ((i) & 1) + 4 * (((i) >> 1) & 1))
  const unsigned swz = (unsigned)((p8 ^ r8) * 16);
  const unsigned swzJ0 = (unsigned)((p8 ^ (r8 & 2)) * 16), swzJ1 = (unsigned)((p8 ^ (r8 & 2) ^ 4) * 16);
  LAS char* ld = lds + lw * 8192;
  const char* Xl = c.x + (size_t)(c.rp * 128 + trow0) * 1024 + swz;
  auto issueX = [&](int s) {
#pragma unroll
    for (int i = 0; i < 8; ++i) gldsa<AUXP1>(Xl + PROW(i) * 1024 + s * 128, ld + (s & 3) * SLOT + i * 1024);
  };
  issueX(0);
  const char* pB[8]; unsigned strB[8];
  {
    const char* Wm = (E0 >= 32 ? c.Wi : c.Wr) + (size_t)(E0 & 31) * 16384;
#pragma unroll
    for (int i = 0; i < 8; ++i) {
      const int p = lw * 8 + i;
      const bool isJ = p >= 14, isW = !isJ && (typeB || p < 6);
      const int pr = isJ ? p - 14 : p - 6;
      const char* b1_ = c.w1 + (size_t)(pr * 8) * (F_ * 2) + (isJ ? PB : PA) * 128;
      const char* bw_ = Wm + (size_t)p * 8192;
      pB[i] = (isW ? bw_ : b1_) + (isW ? r8 * 1024 + swz : r8 * (F_ * 2) + ((i & 1) ? swzJ1 : swzJ0));
      strB[i] = isW ? 128 : (isJ ? 16 * F_ * 2 : 64 * F_ * 2);
    }
  }
  auto issueB1 = [&](int s) {
#pragma unroll
    for (int i = 0; i < 8; ++i) {
      LAS char* d = (i >= 6 && lw == 1) ? lds + 8 * SLOT + (i - 6) * 1024 + s * 2048 : ld + (4 + (s & 3)) * SLOT + i * 1024;
      gldsa<AUXP1>(pB[i], d);
      pB[i] += strB[i];
    }
  };
  issueB1(0); issueX(1); issueB1(1);
  const char* Jl = c.w1 + PB * 128 + (size_t)(64 * lw + r8) * (F_ * 2);
  const char* Cl = (c.ks >> 1 ? c.Ci : c.Cr) + (size_t)(c.tp * 128 + trow0) * 4096 + (c.ks & 1) * 2048 + swz;
  const char* ABb = ws + WS_ABT + (size_t)(((c.ks >> 1) * 4 + c.ec) * 64 + (c.ks & 1) * 32) * 8192 + lw * 8192 + lane * 16;
  const char* Hb = ws + WS_H + (size_t)(2 * c.tp + (c.ks >> 1)) * 56 * 4096 + lw * 8192 + lane * 16;
  const char* W2l = c.w2 + (c.ec * 128 + (c.ks & 1) * 64) * 2 + (64 * lw + r8) * 1024 + (p8 ^ (r8 & 6)) * 16;
  auto issueJ = [&](int j) {
#pragma unroll
    for (int i = 0; i < 8; ++i) gldsa<AUXP1>(Jl + (j * 128 + i * 8) * (F_ * 2) + ((i & 1) ? swzJ1 : swzJ0), ld + (3 + j) * SLOT + i * 1024);
  };
  auto issueC = [&](int s) {
#pragma unroll
    for (int i = 0; i < 8; ++i) gldsa<AUXC>(Cl + PROW(i) * 4096 + s * 128, ld + (s & 3) * SLOT + i * 1024);
  };
  auto issueAB = [&](int s, int slot) {
#pragma unroll
    for (int i = 0; i < 8; ++i) glds_h(ABb + s * 16384 + i * 1024, ld + slot * SLOT + i * 1024);
  };
  auto issueH = [&](int s) {
#pragma unroll
    for (int i = 0; i < 8; ++i) glds_h(Hb + s * 16384 + i * 1024, ld + (s & 3) * SLOT + i * 1024);
#pragma unroll
    for (int i = 0; i < 8; ++i) gldsa<AUXW2>(W2l + i * 8192 + s * 131072, ld + (4 + (s & 3)) * SLOT + i * 1024);
  };
  wait_vm<16>();
  bar();
  issueX(2); issueB1(2);
#pragma unroll 1
  for (int s = 0; s < 5; ++s) { issueX(s + 3); issueB1(s + 3); TR(1); wait_vm<32>(); TR(1); bar(); TR(1); }
#pragma unroll 1
  for (int s = 5; s < 7; ++s) { issueJ(s - 4); issueC(s - 5); TR(1); wait_vm<32>(); TR(1); bar(); TR(1); }
  issueJ(3); issueC(2);
  wait_vm<40>(); bar();
  issueC(3);
  const u64 tag = uniform64(*(const LAS u64*)(lds + LDS_TAG));
  wait_vm<16>(); bar();
  bar();
  STAMP(1);
  {
    const int G2 = 2 * (c.ks >> 1) + (c.ec >> 1), m = lane & 3;
    const bool act = lane < 32 && ((c.ec & 1) ? (m >= 2) : (m <= 2));
    poll(ws + WS_FLAG + (unsigned)(0 * 256 + (8 * (c.ks & 1) + ((lane >> 2) & 7)) * 16 + 4 * G2 + m) * FLAG_STRIDE, [&](u64 x) { return !act || x == tag; });
  }
  STAMP(2);
  issueAB(0, 7); issueAB(1, 8); issueAB(2, 4);
  wait_vm<16>();
  bar();
  STAMP(3);
  issueAB(3, 5); issueAB(4, 6);
  wait_vm<24>(); bar(); issueC(4); issueAB(5, 7);
  wait_vm<32>(); bar(); issueC(5); issueAB(6, 8);
  const char* hfp = ws + WS_FLAG + (unsigned)(1 * 256 + c.tp * 16 + (lane & 15)) * FLAG_STRIDE;
  u64 fv = 0;
  unsigned bseq = 0x87654;
#pragma unroll 1
  for (int s = 2; s < 11; ++s) {
    TR(2); wait_vm<40>(); TR(2); bar(); TR(2);
    issueC(s + 4); issueAB(s + 5, bseq & 15); bseq = (bseq >> 4) | ((bseq & 15) << 16);
    if (s == 7) fv = ld8_async(hfp);
    TR(2);
  }
  wait_vm<40>(); bar(); issueC(15);
  STAMP(4);
  asm volatile("" : "+v"(fv));
  if (!__all(fv == tag)) poll(hfp, [&](u64 x) { return x == tag; });
  STAMP(5);
  const char* yfp = ws + WS_FLAG + (unsigned)(2 * 256 + (c.tp * 4 + c.ec) * 4 + (lane & 3)) * FLAG_STRIDE;
#pragma unroll 1
  for (int s = 12; s < 24; ++s) { TR(3); wait_vm<32>(); TR(3); bar(); TR(3); issueH(s - 12); if (s == 21) fv = ld8_async(yfp); TR(3); }
  STAMP(6);
  wait_vm<32>();
  asm volatile("" : "+v"(fv));
  if (!__all(fv == tag)) poll(yfp, [&](u64 x) { return x == tag; });
  STAMP(7);
  bar(); issueH(12);
  wait_vm<32>(); bar(); issueH(13);
  wait_vm<32>(); bar();
  wait_vm<16>(); bar();
  wait_vm<0>(); bar();
  TR_END();
}

DEV void helper(const Ctx& c) {
  const int hw = c.wid - 6, lane = c.lane, wid = c.wid;
  LAS char* lds = c.lds; char* ws = c.ws;
  const bool typeB = c.ec == 3;
  const int G = c.cp >> 2, v = c.cp & 3, PA = 7 * G + 2 * v, PB = typeB ? PA : PA + 1;
  const int q = lane >> 4, cc = lane & 15, qq = cc >> 2, pp = lane & 3;
  const unsigned dummy = (unsigned)(size_t)lds + LDS_DUMP;
  if (hw == 1) {
#pragma unroll 1
    for (int i = 0; i < PREFI; ++i) touch(c.pc + i * 8192 + lane * 128, dummy);
  }
  u64 tag;
  {
    const u64 tagv = __hip_atomic_load((GAS u64*)(ws + WS_EPOCH), __ATOMIC_RELAXED, __HIP_MEMORY_SCOPE_AGENT);
    const int row = c.rp * 128 + c.cp * 8 + 4 * hw + q;
    u32x4 xv[4];
#pragma unroll
    for (int i = 0; i < 4; ++i) xv[i] = *(const u32x4*)(c.x + (size_t)row * 1024 + cc * 16 + i * 256);
    const float bv = bflo(*(const u16*)(c.b1 + ((hw ? PB : PA) * 64 + lane) * 2));
    float s = 0.f;
#pragma unroll
    for (int i = 0; i < 16; ++i) s += bflo(xv[i >> 2][i & 3]) + bfhi(xv[i >> 2][i & 3]);
    const float m = sum16(s) * (1.f / 512.f);
    float z = 0.f;
#pragma unroll
    for (int i = 0; i < 16; ++i) { const float d0 = bflo(xv[i >> 2][i & 3]) - m, d1 = bfhi(xv[i >> 2][i & 3]) - m; z += d0 * d0; z += d1 * d1; }
    const float var = sum16(z) * (1.f / 512.f);
    const float sd = __builtin_amdgcn_sqrtf(rbf(rbf(var) + 1e-5f));
    tag = uniform64(tagv) + 1;
    if (cc == 0)
      __hip_atomic_store((GAS u64*)(ws + WS_ROWST + (size_t)row * 8), ((u64)(unsigned)tag << 32) | pk_bf16(m, sd), __ATOMIC_RELAXED, __HIP_MEMORY_SCOPE_AGENT);
    *(LAS float*)(lds + LDS_B1 + (hw * 64 + lane) * 4) = bv;
    if (hw == 0 && lane == 0) { *(LAS u64*)(lds + LDS_TAG) = tag; *(LAS u64*)(lds + LDS_TAG + 8) = 0; }
  }
  const unsigned wa0 = cc * 128 + ((q ^ (cc & 7)) * 16), wa1 = cc * 128 + (((4 + q) ^ (cc & 7)) * 16);
  const unsigned wa = hw ? wa1 : wa0;
  unsigned ja[4];
#pragma unroll
  for (int n = 0; n < 4; ++n) ja[n] = (8 * q + qq) * 128 + (((2 * n + (pp >> 1)) ^ ((qq & 2) | ((q & 1) << 2))) * 16) + 8 * (pp & 1) + hw * 4096;
  const bf16x8 ones = {0x3f80, 0x3f80, 0x3f80, 0x3f80, 0x3f80, 0x3f80, 0x3f80, 0x3f80};
  const f32x4 z4 = {0.f, 0.f, 0.f, 0.f};
  f32x4 cs[7] = {z4, z4, z4, z4, z4, z4, z4};
  const int srow = 32 * (lane >> 5) + 8 * qq + 4 * (q & 1) + (lane & 3);
  const char* stp = ws + WS_ROWST + (size_t)(c.rp * 128 + srow) * 8;
  wait_lgkm0();
  bar();
  auto csum = [&](int s) {
    LAS char* ba = lds + (4 + (s & 3)) * SLOT;
    bf16x8 f[7];
#pragma unroll
    for (int n = 0; n < 3; ++n) f[n] = ldsb128(ba + n * 2048 + wa);
    if (typeB) {
#pragma unroll
      for (int n = 3; n < 7; ++n) f[n] = ldsb128(ba + n * 2048 + wa);
    } else {
#pragma unroll
      for (int n = 3; n < 7; ++n) f[n] = ldstr(ba + 6144 + ja[n - 3], 512);
    }
#pragma unroll
    for (int n = 0; n < 7; ++n) cs[n] = MFMA(ones, f[n], cs[n]);
  };
  u64 st0 = 0, st1 = 0;
  const char* Cp = (c.ks >> 1 ? c.Ci : c.Cr) + (size_t)(c.tp * 128 + c.ec * 32 + q) * 4096 + (c.ks & 1) * 2048 + cc * 128;
#pragma unroll 1
  for (int s = 0; s < 7; ++s) {
    if (hw == 0) { if (s == 4) { st0 = ld8_async(stp); st1 = ld8_async(stp + 512); } }
    else if (s >= PREFS) { touch(Cp, dummy); Cp += 4 * 4096; }
    csum(s);
    wait_lgkm0();
    bar();
  }
  {
    csum(7);
    if (hw == 0) {
      wait_vm<0>();
      asm volatile("" : "+v"(st0), "+v"(st1));
      const unsigned t32 = (unsigned)tag;
      if (!__all((unsigned)(st0 >> 32) == t32)) st0 = poll(stp, [&](u64 x) { return (unsigned)(x >> 32) == t32; });
      if (!__all((unsigned)(st1 >> 32) == t32)) st1 = poll(stp + 512, [&](u64 x) { return (unsigned)(x >> 32) == t32; });
      *(LAS float*)(lds + LDS_MEAN + lane * 4) = bflo((unsigned)st0);
      *(LAS float*)(lds + LDS_RSTD + lane * 4) = __builtin_amdgcn_rcpf(bfhi((unsigned)st0));
      *(LAS float*)(lds + LDS_MEAN + 256 + lane * 4) = bflo((unsigned)st1);
      *(LAS float*)(lds + LDS_RSTD + 256 + lane * 4) = __builtin_amdgcn_rcpf(bfhi((unsigned)st1));
    }
    else {
#pragma unroll 1
      for (int s = 0; s < PREFS + 1; ++s) { touch(Cp, dummy); Cp += 4 * 4096; }
    }
    if (lane < 16) {
#pragma unroll
      for (int n = 0; n < 7; ++n) *(LAS float*)(lds + LDS_CS + ((hw * 11 + n) * 16 + lane) * 4) = cs[n][0];
    }
    wait_lgkm0();
    bar();
  }
#pragma unroll
  for (int n = 0; n < 4; ++n) cs[n] = z4;
#pragma unroll 1
  for (int h = 0; h < 2; ++h) {
#pragma unroll
    for (int j = 0; j < 2; ++j) {
      LAS char* ba = lds + ((h | j) == 0 ? 8 : 3 + 2 * h + j) * SLOT;
#pragma unroll
      for (int k = 0; k < 2; ++k)
#pragma unroll
        for (int n = 0; n < 4; ++n) cs[n] = MFMA(ones, ldstr(ba + ja[n] + k * 8192, 512), cs[n]);
    }
    if (h == 1 && lane < 16) {
#pragma unroll
      for (int n = 0; n < 4; ++n) *(LAS float*)(lds + LDS_CS + ((hw * 11 + 7 + n) * 16 + lane) * 4) = cs[n][0];
    }
    wait_lgkm0();
    bar();
  }
  const char* Wp = c.w2 + (c.ec * 128 + (c.ks & 1) * 64) * 2 + (size_t)((c.tp & 7) * 224 + hw * 112 + (lane < 56 ? lane : 0)) * 1024;
#pragma unroll 1
  for (int s = 0; s < 30; ++s) {
    if (s == PREFW) { touch(Wp, dummy); touch(Wp + 56 * 1024, dummy); }
    bar();
  }
  wait_vm<0>();
  if (blockIdx.x < 2) {
    if (blockIdx.x == 0 && hw == 0) {
      const char* fp = ws + WS_FLAG + (unsigned)lane * FLAG_STRIDE;
      const u64 t0 = __builtin_amdgcn_s_memrealtime();
      for (;;) {
        u64 f[4];
#pragma unroll
        for (int i = 0; i < 4; ++i) f[i] = __hip_atomic_load((GAS u64*)(fp + i * 64 * FLAG_STRIDE), __ATOMIC_RELAXED, __HIP_MEMORY_SCOPE_AGENT);
        if (__all(f[0] == tag && f[1] == tag && f[2] == tag && f[3] == tag)) break;
        if (__builtin_amdgcn_s_memrealtime() - t0 > 20000000ull) break;
        __builtin_amdgcn_s_sleep(1);
      }
      if (lane == 0) __hip_atomic_store((GAS u64*)(ws + WS_EPOCH), tag, __ATOMIC_RELAXED, __HIP_MEMORY_SCOPE_AGENT);
    }
  }
}

DEV void consumer(const Ctx& c) {
  const int w = c.wid, lane = c.lane, wid = c.wid;
  TR_DECL;
  u64 pr0 = 0, pr1 = 0; (void)pr0; (void)pr1;
  LAS char* lds = c.lds; char* ws = c.ws;
  const int q = lane >> 4, cc = lane & 15, qq = cc >> 2, pp = lane & 3;
  const bool typeB = c.ec == 3;
  const int G = c.cp >> 2, v = c.cp & 3, E0 = 16 * G + 3 * v, PA = 7 * G + 2 * v, PB = typeB ? PA : PA + 1;
  const auto rs = __builtin_amdgcn_make_buffer_rsrc(ws, 0, WS_BYTES, 0x00020000);
  STAMP(0);
  const unsigned wa0 = cc * 128 + ((q ^ (cc & 7)) * 16), wa1 = cc * 128 + (((4 + q) ^ (cc & 7)) * 16);
  unsigned ja[4];
#pragma unroll
  for (int n = 0; n < 4; ++n) ja[n] = opq(4 * SLOT + (8 * q + qq) * 128 + (((2 * n + (pp >> 1)) ^ ((qq & 2) | ((q & 1) << 2))) * 16) + 8 * (pp & 1));
  bf16x8 xf[2][16];
  auto h_epi = [&](f32x4 (&acc)[4][2], int brow, int P, int csrow) {
    float mean[2], rstd[2];
#pragma unroll
    for (int u = 0; u < 2; ++u) { mean[u] = *(const LAS float*)(lds + LDS_MEAN + ((2 * w + u) * 16 + cc) * 4); rstd[u] = *(const LAS float*)(lds + LDS_RSTD + ((2 * w + u) * 16 + cc) * 4); }
    f32x4 cs[4], bv[4];
#pragma unroll
    for (int n = 0; n < 4; ++n) {
      cs[n] = *(const LAS f32x4*)(lds + LDS_CS + ((csrow + n) * 16 + 4 * q) * 4) + *(const LAS f32x4*)(lds + LDS_CS + ((11 + csrow + n) * 16 + 4 * q) * 4);
      bv[n] = *(const LAS f32x4*)(lds + LDS_B1 + (brow * 64 + n * 16 + 4 * q) * 4);
    }
    const unsigned hb = WS_H + (unsigned)((((2 * c.rp + (w >> 1)) * 56 + 2 * P) * 4 + 2 * (w & 1)) * 1024);
#pragma unroll
    for (int b = 0; b < 2; ++b)
#pragma unroll
      for (int u = 0; u < 2; ++u) {
        unsigned d[4];
#pragma unroll
        for (int h = 0; h < 2; ++h) {
          const int n = 2 * b + h;
          f32x4 z = (acc[n][u] - mean[u] * cs[n]) * rstd[u] + bv[n];
          z = __builtin_elementwise_max(z, (f32x4){0.f, 0.f, 0.f, 0.f});
          z = z * z;
          d[2 * h] = pk_bf16(z[0], z[1]); d[2 * h + 1] = pk_bf16(z[2], z[3]);
        }
        __builtin_amdgcn_raw_buffer_store_b128((u32x4){d[0], d[1], d[2], d[3]}, rs, lane * 16 + u * 1024 + hb + b * 4096, 0, 16);
      }
  };
  constexpr int L2 = 8;
  bf16x8 g[64];
  auto rd2 = [&](auto t_) {
    constexpr int t = decltype(t_)::value, j = t / 16, k = (t / 4) & 3, n = t & 3;
    if constexpr (j == 0) g[t] = ldstr(lds + 4 * SLOT + ja[n] + k * 4096, 512); else g[t] = ldstr(lds + (j - 1) * SLOT + ja[n] + k * 4096, 512);
  };
  bar();
  STAMP(1);
  auto pass1 = [&](auto na_) {
    constexpr int NA = decltype(na_)::value;
    f32x4 acc[7][2];
#pragma unroll
    for (int n = 0; n < 7; ++n) { acc[n][0] = (f32x4){0, 0, 0, 0}; acc[n][1] = (f32x4){0, 0, 0, 0}; }
    bf16x8 fr[112];
    const unsigned bx0 = opq(wa0 + w * 4096), bx1 = opq(wa1 + w * 4096), bw0 = opq(wa0 + 4 * SLOT), bw1 = opq(wa1 + 4 * SLOT);
    auto rd = [&](auto t_) {
      constexpr int t = decltype(t_)::value, kk = t / 7, n = t % 7, so = ((kk >> 1) & 3) * SLOT;
      if constexpr (n == 0) { xf[0][kk] = ldsb128(lds + ((kk & 1) ? bx1 : bx0) + so); xf[1][kk] = ldsb128(lds + ((kk & 1) ? bx1 : bx0) + so + 2048); }
      if constexpr (n < NA) fr[t] = ldsb128(lds + ((kk & 1) ? bw1 : bw0) + so + n * 2048);
      else fr[t] = ldstr(lds + ja[(n - 3) & 3] + so + 6144 + (kk & 1) * 4096, 512);
    };
    static_for<0, 7>(rd);
    SCHED();
    static_for<0, 112>([&](auto p_) {
      constexpr int p = decltype(p_)::value, kk = p / 7, n = p % 7;
      constexpr int kn = p / 14 + 1;
      if constexpr (p == 14 * kn - lead1(kn)) {
        TR(1);
        wait_lgkm0();
        TR(1);
        bar();
        TR(1);
        SCHED();
      }
      const bf16x8 f = fr[p];
      if constexpr (n < NA) acc[n][0] = MFMA(xf[0][kk], f, acc[n][0]); else acc[n][0] = MFMA(f, xf[0][kk], acc[n][0]);
      SCHED();
      static_for<p + 1, p + 15>([&](auto t_) {
        constexpr int t = decltype(t_)::value;
        if constexpr (t < 112 && t >= 7 && sched1(t) == p) { rd(t_); SCHED(); }
      });
      if constexpr (n < NA) acc[n][1] = MFMA(xf[1][kk], f, acc[n][1]); else acc[n][1] = MFMA(f, xf[1][kk], acc[n][1]);
      SCHED();
    });
    STAMP(2);
    f32x4 mr[2], rr[2];
    const float sgn = E0 >= 32 ? -1.f : 1.f;
#pragma unroll
    for (int u = 0; u < 2; ++u) {
      mr[u] = *(const LAS f32x4*)(lds + LDS_MEAN + ((2 * w + u) * 16 + 4 * q) * 4);
      rr[u] = *(const LAS f32x4*)(lds + LDS_RSTD + ((2 * w + u) * 16 + 4 * q) * 4) * sgn;
    }
    float cs[NA];
#pragma unroll
    for (int n = 0; n < NA; ++n) cs[n] = *(const LAS float*)(lds + LDS_CS + (n * 16 + cc) * 4) + *(const LAS float*)(lds + LDS_CS + ((11 + n) * 16 + cc) * 4);
#pragma unroll
    for (int n = 0; n < NA; ++n) {
      const f32x4 o0 = (acc[n][0] - mr[0] * cs[n]) * rr[0], o1 = (acc[n][1] - mr[1] * cs[n]) * rr[1];
      u32x4 d = {pk_bf16(o0[0], o0[1]), pk_bf16(o0[2], o0[3]), pk_bf16(o1[0], o1[1]), pk_bf16(o1[2], o1[3])};
      const int E = E0 + n;
      __builtin_amdgcn_raw_buffer_store_b128(d, rs, lane * 16 + WS_ABT + (unsigned)((((E >> 3) * 64 + c.rp * 4 + w) * 8 + (E & 7)) * 1024), 0, 16);
    }
    SCHED();
    if (NA == 3) {
      f32x4 (&ah)[4][2] = *(f32x4(*)[4][2]) & acc[3];
      h_epi(ah, 0, PA, 3);
    }
  };
  if (typeB) pass1(IC<7>{}); else pass1(IC<3>{});
  SCHED();
  static_for<0, L2>(rd2);
  const u64 tag = uniform64(*(const LAS u64*)(lds + LDS_TAG));
  STAMP(3);
  {
    f32x4 acc[4][2];
#pragma unroll
    for (int n = 0; n < 4; ++n) { acc[n][0] = (f32x4){0, 0, 0, 0}; acc[n][1] = (f32x4){0, 0, 0, 0}; }
    SCHED();
    static_for<0, 64>([&](auto p_) {
      constexpr int p = decltype(p_)::value, kk = p / 4, n = p & 3;
      if constexpr (p == 16) {
        wait_vm<0>();
        bar();
        if (w == 0 && lane == 0) set_flag(ws, 0, c.rp * 16 + c.cp, tag);
        SCHED();
      }
      if constexpr (p == 32) { bar(); SCHED(); }
      acc[n][0] = MFMA(g[p], xf[0][kk], acc[n][0]);
      SCHED();
      if constexpr (p + L2 < 64) { rd2(IC<p + L2>{}); SCHED(); }
      acc[n][1] = MFMA(g[p], xf[1][kk], acc[n][1]);
      SCHED();
    });
    STAMP(11);
    h_epi(acc, 1, PB, 7);
  }
  STAMP(4);
  const int wt = w >> 1, we = w & 1;
  unsigned wb[2];
#pragma unroll
  for (int n = 0; n < 2; ++n) wb[n] = 4 * SLOT + (4 * q + qq) * 128 + (((2 * (2 * we + n) + (pp >> 1)) ^ ((4 * q + qq) & 6)) * 16) + 8 * (pp & 1);
  const unsigned hb = wt * 2048 + lane * 16;
  bf16x8 hA[4][2], wA[4][2], hB[4][2], wB[4][2];
  wait_lgkm0();
  bar();
  STAMP(5);
#define PREAD(HN, WN, SO) if (i < 12) { if (i % 3 == 2) { HN[i / 3][0] = ldsb128(lds + (SO) + hb + (i / 3) * 4096); HN[i / 3][1] = ldsb128(lds + (SO) + hb + (i / 3) * 4096 + 1024); } \
                                        else WN[i / 3][i % 3] = ldstr(lds + (SO) + wb[i % 3] + (i / 3) * 4096, 2048); SCHED(); }
  {
    f32x4 acc[4][4];
#pragma unroll
    for (int m = 0; m < 4; ++m)
#pragma unroll
      for (int n = 0; n < 4; ++n) acc[m][n] = (f32x4){0, 0, 0, 0};
    const unsigned a0 = wt * 8192 + wa0, a1 = wt * 8192 + wa1, b0 = we * 4096 + lane * 16;
    bf16x8 fa0[4], fb0[4], fa1[4], fb1[4];
#pragma unroll
    for (int m = 0; m < 4; ++m) { fa0[m] = ldsb128(lds + a0 + m * 2048); fb0[m] = ldsb128(lds + b0 + 7 * SLOT + m * 1024); }
#define HALF(FA, FB, NA_, NB_, AOFF, BOFF) do { _Pragma("unroll") for (int i = 0; i < 16; ++i) { \
      acc[i >> 2][i & 3] = MFMA(FB[i & 3], FA[i >> 2], acc[i >> 2][i & 3]); SCHED(); \
      if (i < 8) { if (i == 0) NB_[0] = ldsb128(lds + (BOFF)); else if (i == 1) NA_[0] = ldsb128(lds + (AOFF)); \
        else if (i < 5) NB_[i - 1] = ldsb128(lds + (BOFF) + (i - 1) * 1024); else NA_[i - 4] = ldsb128(lds + (AOFF) + (i - 4) * 2048); SCHED(); } } } while (0)
    unsigned so = 0, sb = 7 * SLOT, bseq = 0x76548;
#pragma unroll 1
    for (int seg = 0; seg < 2; ++seg) {
      const int n = seg ? 12 : 3;
#pragma unroll 1
      for (int s = 0; s < n; ++s) {
        HALF(fa0, fb0, fa1, fb1, so + a1, sb + b0 + 8192);
        TR(2);
        wait_lgkm0();
        TR(2);
        bar();
        TR(2);
        sb = (bseq & 15) * SLOT; bseq = (bseq >> 4) | ((bseq & 15) << 16); so = (so + SLOT) & (4 * SLOT - 1);
        HALF(fa1, fb1, fa0, fb0, so + a0, sb + b0);
      }
      if (seg == 0) {
        wait_vm<0>();
        unsigned old = 0;
        if (lane == 0) old = __hip_atomic_fetch_add((LAS unsigned*)(lds + LDS_TAG + 8), 1u, __ATOMIC_RELAXED, __HIP_MEMORY_SCOPE_WORKGROUP);
        if (lane == 0 && old == 3) set_flag(ws, 1, c.rp * 16 + c.cp, tag);
      }
    }
    HALF(fa0, fb0, fa1, fb1, so + a1, sb + b0 + 8192);
    wait_lgkm0();
    bar();
#pragma unroll
    for (int i = 0; i < 16; ++i) {
      acc[i >> 2][i & 3] = MFMA(fb1[i & 3], fa1[i >> 2], acc[i >> 2][i & 3]); SCHED();
      PREAD(hA, wA, 0)
    }
    STAMP(6);
    if (w == c.ks) {
#pragma unroll
      for (int m = 0; m < 4; ++m)
#pragma unroll
        for (int n = 0; n < 4; ++n) *(LAS f32x4*)(lds + 8 * SLOT + (m * 4 + n) * 1024 + lane * 16) = acc[m][n];
    } else {
      const unsigned yb = WS_YP + (unsigned)((((c.tp * 4 + c.ec) * 4 + c.ks) * 4 + w) * 8192);
#pragma unroll
      for (int m = 0; m < 4; ++m)
#pragma unroll
        for (int j = 0; j < 2; ++j) {
          const f32x4 e = acc[m][2 * j], o = acc[m][2 * j + 1];
          __builtin_amdgcn_raw_buffer_store_b128((u32x4){pk_bf16(e[0], e[1]), pk_bf16(e[2], e[3]), pk_bf16(o[0], o[1]), pk_bf16(o[2], o[3])}, rs, lane * 16 + (m * 2 + j) * 1024 + yb, 0, 16);
        }
    }
  }
  STAMP(7);
  {
    f32x4 acc[2][2];
#pragma unroll
    for (int m = 0; m < 2; ++m) { acc[m][0] = (f32x4){0, 0, 0, 0}; acc[m][1] = (f32x4){0, 0, 0, 0}; }
    u32x4 yp[3][2];
    u32x2 xr[2][2], b2r[2];
    const int trow = c.tp * 128 + (c.ks >> 1) * 64 + 32 * wt + 8 * qq + (cc & 3);
    const int ecol = c.ec * 128 + (c.ks & 1) * 64 + 32 * we + 4 * q;
    unsigned so = 0;
#define PSTEP(HC, WC, HN, WN) do { wait_lgkm0(); TR(3); bar(); TR(3); so = (so + SLOT) & (4 * SLOT - 1); SCHED(); \
      _Pragma("unroll") for (int i = 0; i < 16; ++i) { \
        acc[(i >> 1) & 1][i & 1] = MFMA(WC[i >> 2][i & 1], HC[i >> 2][(i >> 1) & 1], acc[(i >> 1) & 1][i & 1]); SCHED(); \
        PREAD(HN, WN, so) } } while (0)
#pragma unroll 1
    for (int seg = 0; seg < 2; ++seg) {
#pragma unroll 1
      for (int s = 0; s < 2; ++s) { PSTEP(hA, wA, hB, wB); PSTEP(hB, wB, hA, wA); }
      if (seg == 0) {
        wait_vm<0>();
        unsigned old = 0;
        if (lane == 0) old = __hip_atomic_fetch_add((LAS unsigned*)(lds + LDS_TAG + 12), 1u, __ATOMIC_RELAXED, __HIP_MEMORY_SCOPE_WORKGROUP);
        if (lane == 0 && old == 3) set_flag(ws, 2, (c.tp * 4 + c.ec) * 4 + c.ks, tag);
      }
    }
    PSTEP(hA, wA, hB, wB);
    STAMP(8);
    {
      const unsigned yb = WS_YP + (unsigned)(((c.tp * 4 + c.ec) * 16 + c.ks) * 8192);
#pragma unroll
      for (int i = 0; i < 3; ++i) {
        const int src = (c.ks + 1 + i) & 3;
#pragma unroll
        for (int m = 0; m < 2; ++m) yp[i][m] = __builtin_amdgcn_raw_buffer_load_b128(rs, lane * 16 + ((2 * wt + m) * 2 + we) * 1024, yb + src * 32768, 16);
      }
#pragma unroll
      for (int m = 0; m < 2; ++m)
#pragma unroll
        for (int n = 0; n < 2; ++n) xr[m][n] = *(const u32x2*)(c.x + (size_t)(trow + 4 * m) * 1024 + (ecol + 16 * n) * 2);
#pragma unroll
      for (int n = 0; n < 2; ++n) b2r[n] = *(const u32x2*)(c.b2 + (ecol + 16 * n) * 2);
    }
    SCHED();
    PSTEP(hB, wB, hA, wA);
    PSTEP(hA, wA, hB, wB);
    PSTEP(hB, wB, hA, wA);
    PSTEP(hA, wA, hB, wB);
    f32x4 yo[2][2];
#pragma unroll
    for (int m = 0; m < 2; ++m)
#pragma unroll
      for (int n = 0; n < 2; ++n) yo[m][n] = *(const LAS f32x4*)(lds + 8 * SLOT + ((2 * wt + m) * 4 + 2 * we + n) * 1024 + lane * 16);
#pragma unroll
    for (int i = 0; i < 16; ++i) acc[(i >> 1) & 1][i & 1] = MFMA(wB[i >> 2][i & 1], hB[i >> 2][(i >> 1) & 1], acc[(i >> 1) & 1][i & 1]);
    STAMP(9);
#pragma unroll
    for (int m = 0; m < 2; ++m)
#pragma unroll
      for (int n = 0; n < 2; ++n) {
        f32x4 y = yo[m][n];
#pragma unroll
        for (int i = 0; i < 3; ++i) y += (f32x4){bflo(yp[i][m][2 * n]), bfhi(yp[i][m][2 * n]), bflo(yp[i][m][2 * n + 1]), bfhi(yp[i][m][2 * n + 1])};
        const float bv[4] = {bflo(b2r[n].x), bfhi(b2r[n].x), bflo(b2r[n].y), bfhi(b2r[n].y)};
        const float xv[4] = {bflo(xr[m][n].x), bfhi(xr[m][n].x), bflo(xr[m][n].y), bfhi(xr[m][n].y)};
        float o[4];
#pragma unroll
        for (int r = 0; r < 4; ++r) {
          const float z = acc[m][n][r] + bv[r];
          const float g = z * __builtin_amdgcn_rcpf(1.f + __expf(-z));
          o[r] = xv[r] + y[r] * g;
        }
        u32x2 d = {pk_bf16(o[0], o[1]), pk_bf16(o[2], o[3])};
        *(u32x2*)(c.out + (size_t)(trow + 4 * m) * 1024 + (ecol + 16 * n) * 2) = d;
      }
    STAMP(10);
    TR_END();
  }
}

__global__ __launch_bounds__(512, 2) void fnet_mlp_fused(const char* x, const char* Cr, const char* Ci, const char* Wr, const char* Wi,
                                                         const char* w1, const char* b1, const char* w2, const char* b2, char* out, char* ws) {
  extern __shared__ __attribute__((aligned(16))) char shm[];
  Ctx c;
  c.x = x; c.Cr = Cr; c.Ci = Ci; c.Wr = Wr; c.Wi = Wi; c.w1 = w1; c.b1 = b1; c.w2 = w2; c.b2 = b2; c.out = out; c.ws = ws;
  c.lds = (LAS char*)shm;
  { u64 pc; asm volatile("s_getpc_b64 %0" : "=s"(pc)); c.pc = (const char*)(pc & ~(u64)255); }
  c.wid = __builtin_amdgcn_readfirstlane(threadIdx.x >> 6); c.lane = threadIdx.x & 63;
  const int b = blockIdx.x, xcd = b & 7, r = b >> 3;
  c.ks = xcd >> 1; c.tp = (xcd & 1) * 8 + (r >> 2); c.ec = r & 3;
  c.rp = c.tp; c.cp = c.ks * 4 + c.ec;
  if (c.wid >= 6) helper(c); else if (c.wid >= 4) loader(c); else consumer(c);
}

#define WSOFF(n) 0
extern "C" void kernel_launch(void* const* d_in, const int* in_sizes, int n_in, void* d_out, int out_size, void* d_ws, size_t ws_size, hipStream_t stream) {
  (void)hipFuncSetAttribute((const void*)fnet_mlp_fused, hipFuncAttributeMaxDynamicSharedMemorySize, LDS_BYTES);
  fnet_mlp_fused<<<256, 512, LDS_BYTES, stream>>>((const char*)d_in[0], (const char*)d_in[1], (const char*)d_in[2], (const char*)d_in[3], (const char*)d_in[4],
                                                  (const char*)d_in[6], (const char*)d_in[7], (const char*)d_in[8], (const char*)d_in[9], (char*)d_out, (char*)d_ws + WSOFF(ws_size));
}
```

```cpp
#define PREFC 2
#define PM1 0x14
#define PMF 2
#define PM2 0x55
#define PREFW 8
#include <hip/hip_runtime.h>
typedef unsigned short u16;
typedef unsigned long long u64;
typedef short bf16x8 __attribute__((ext_vector_type(8)));
typedef short s16x4 __attribute__((ext_vector_type(4)));
typedef float f32x4 __attribute__((ext_vector_type(4)));
typedef float f32x2 __attribute__((ext_vector_type(2)));
typedef unsigned u32x4 __attribute__((ext_vector_type(4)));
typedef unsigned u32x2 __attribute__((ext_vector_type(2)));
typedef __bf16 bf2 __attribute__((ext_vector_type(2)));
#define LAS __attribute__((address_space(3)))
#define GAS __attribute__((address_space(1)))
#define DEV __device__ __forceinline__

constexpr int S_ = 2048, D_ = 512, F_ = 1792, W1R = F_ * 2;
constexpr unsigned WS_EPOCH = 0, WS_FLAG = 4096, FLAG_STRIDE = 128, WS_ROWST = 256u << 10;
constexpr unsigned WS_ABT = 1u << 20, WS_H = 8u << 20, WS_YP = 16u << 20, WS_BYTES = 32u << 20;
constexpr int SLOT = 16384, NSLOT = 9;
constexpr int LDS_XT = NSLOT * SLOT;
constexpr int LDS_MEAN = LDS_XT + 8192, LDS_RSTD = LDS_MEAN + 512, LDS_CS = LDS_RSTD + 512;
constexpr int LDS_B1 = LDS_CS + 22 * 64, LDS_B2 = LDS_B1 + 512, LDS_TAG = LDS_B2 + 256;
constexpr int LDS_DUMP = LDS_TAG + 16, LDS_BYTES = LDS_DUMP + 256;
static_assert(LDS_BYTES <= 160 * 1024, "LDS");
#define LEADE 6
#define LOOKE 13
#define LEADL 4
#define LOOKL 9
#define ROT 1
#define PREFS 2
#define PREFI 320
#define HPRIO 2
#define LPRIO 0
#define STLD 3
#define NINIT 3
#define HEARLY 0
#define EPOS 18
#define FPOS 8
#define RPOS 16
constexpr int HFLAG = 3, YFLAG = 18, YPOLL = 22, YCHK = 25;

template <int N> DEV void wait_vm() { asm volatile("s_waitcnt vmcnt(%0)" ::"n"(N) : "memory"); }
DEV void wait_lgkm0() { asm volatile("s_waitcnt lgkmcnt(0)" ::: "memory"); }
DEV void bar() { asm volatile("" ::: "memory"); __builtin_amdgcn_s_barrier(); asm volatile("" ::: "memory"); }
#define rawbar() __builtin_amdgcn_s_barrier()
#define BT_DECL
#define BT_END() do {} while (0)
#define BT_PARAM
#define BT_ARG
#define SCHED() __builtin_amdgcn_sched_barrier(0)
#define MFMA(a, b, c) __builtin_amdgcn_mfma_f32_16x16x32_bf16((a), (b), (c), 0, 0, 0)
DEV unsigned pk_bf16(float a, float b) { f32x2 f = {a, b}; return __builtin_bit_cast(unsigned, __builtin_convertvector(f, bf2)); }
DEV float rbf(float a) { return __uint_as_float(pk_bf16(a, 0.f) << 16); }
DEV float bflo(unsigned v) { return __uint_as_float(v << 16); }
DEV float bfhi(unsigned v) { return __uint_as_float(v & 0xffff0000u); }
DEV f32x4 unpk(unsigned a, unsigned b) { return (f32x4){bflo(a), bfhi(a), bflo(b), bfhi(b)}; }
DEV bf16x8 ldsb128(LAS char* p) { return *(const LAS bf16x8*)p; }
DEV bf16x8 ldstr(LAS char* p, int second) {
  s16x4 a = __builtin_amdgcn_ds_read_tr16_b64_v4i16((LAS s16x4*)p);
  s16x4 b = __builtin_amdgcn_ds_read_tr16_b64_v4i16((LAS s16x4*)(p + second));
  return __builtin_shufflevector(a, b, 0, 1, 2, 3, 4, 5, 6, 7);
}
DEV unsigned opq(unsigned v) { asm volatile("" : "+v"(v)); return v; }
DEV int fresh_lane() { int l; asm volatile("v_mbcnt_lo_u32_b32 %0, -1, 0\n\tv_mbcnt_hi_u32_b32 %0, -1, %0" : "=v"(l)); return l; }
typedef __amdgpu_buffer_rsrc_t rsrc_t;
DEV rsrc_t mk_rsrc(const void* p, unsigned bytes) { return __builtin_amdgcn_make_buffer_rsrc((void*)p, 0, bytes, 0x00020000); }
template <int AUX> DEV void dma(rsrc_t rs, unsigned voff, unsigned soff, LAS char* dst) { __builtin_amdgcn_raw_ptr_buffer_load_lds(rs, (LAS void*)dst, 16, voff, soff, 0, AUX); }
DEV u64 ld8_async(const char* p) { u64 v; asm volatile("global_load_dwordx2 %0, %1, off sc1" : "=v"(v) : "v"(p) : "memory"); return v; }
DEV void touch(const char* p, unsigned dump) {
  unsigned keep;
  asm volatile("s_mov_b32 %0, m0\n\ts_mov_b32 m0, %2\n\ts_nop 0\n\tglobal_load_lds_dword %1, off\n\ts_mov_b32 m0, %0" : "=&s"(keep) : "v"(p), "s"(dump) : "memory");
}
template <int CTRL> DEV float dpp_add(float x) { return x + __builtin_bit_cast(float, __builtin_amdgcn_update_dpp(0, __builtin_bit_cast(int, x), CTRL, 0xf, 0xf, false)); }
DEV float sum16(float x) { x = dpp_add<0xB1>(x); x = dpp_add<0x4E>(x); x = dpp_add<0x141>(x); return dpp_add<0x140>(x); }

#define STAMP(i) do {} while (0)
#define STAMP_END() do {} while (0)
#define DBG_DECL
#define DBG_PARAM
#define DBG_ARG

template <int N> struct IC { static constexpr int value = N; };
template <int B, int E, class F> DEV void static_for(F&& f) { if constexpr (B < E) { f(IC<B>{}); static_for<B + 1, E>(f); } }
constexpr int lead1(int k) { return k >= 7 ? LEADL : LEADE; }
constexpr int sched1(int t) { const int k = t / 14, e0 = t - (k >= 7 ? LOOKL : LOOKE), e1 = 14 * k - lead1(k) + (t - 14 * k) / 2; return k == 0 ? (t - 7 > 0 ? (t - 7) / 2 : 0) : (e0 > e1 ? e0 : e1); }

struct Ctx {
  const char *x, *Cr, *Ci, *Wr, *Wi, *w1, *b1, *w2, *b2; char* out; char* ws; LAS char* lds; const char* pc;
  int wid, tp, ec, ks;
  int E0, PA, PB, rot1, rot2;
  bool typeB;
};

template <class P> DEV u64 poll(const char* p, P ok) {
  const u64 t0 = __builtin_amdgcn_s_memrealtime();
  u64 v;
  for (;;) {
    v = __hip_atomic_load((GAS u64*)p, __ATOMIC_RELAXED, __HIP_MEMORY_SCOPE_AGENT);
    if (__all(ok(v))) break;
    if (__builtin_amdgcn_s_memrealtime() - t0 > 20000000ull) break;
    __builtin_amdgcn_s_sleep(1);
  }
  return v;
}
DEV void set_flag(char* ws, int kind, int idx, u64 tag) {
  __hip_atomic_store((GAS u64*)(ws + WS_FLAG + (unsigned)(kind * 256 + idx) * FLAG_STRIDE), tag, __ATOMIC_RELAXED, __HIP_MEMORY_SCOPE_AGENT);
}
DEV u64 uniform64(u64 v) {
  const unsigned lo = __builtin_amdgcn_readfirstlane((unsigned)v), hi = __builtin_amdgcn_readfirstlane((unsigned)(v >> 32));
  return ((u64)hi << 32) | lo;
}
#define PROW(p) (32 * ((p) >> 2) + 16 * ((p) & 1) + 4 * (((p) >> 1) & 1))

DEV void loaderA(const Ctx& c) {
  DBG_DECL; BT_DECL;
  __builtin_amdgcn_s_setprio(LPRIO);
  const int lane = fresh_lane(), r8 = lane >> 3, p8 = lane & 7;
  LAS char* lds = c.lds;
  const unsigned rl = 8 * (r8 >> 2) + (r8 & 3), swz = (unsigned)((p8 ^ r8) * 16);
  const rsrc_t rX = mk_rsrc(c.x + (size_t)c.tp * 131072, 131072);
  const unsigned vX = rl * 1024 + swz;
  auto issueX = [&](int s) {
    const unsigned so = ((s + c.rot1) & 7) * 128;
#pragma unroll
    for (int p = 0; p < 16; ++p) dma<0>(rX, vX, so + PROW(p) * 1024, lds + (s & 3) * SLOT + p * 1024);
  };
  STAMP(8);
  issueX(0); STAMP(9); issueX(1);
  if (NINIT == 3) issueX(2);
  const rsrc_t rC = mk_rsrc((c.ks >> 1 ? c.Ci : c.Cr) + (size_t)c.tp * 524288 + (c.ks & 1) * 2048, 524288 - 2048);
  const rsrc_t rH = mk_rsrc(c.ws + WS_H + (size_t)(2 * c.tp + (c.ks >> 1)) * (56 * 4096), 56 * 4096);
  const unsigned vC = rl * 4096 + swz, vL = lane * 16;
  auto issueC = [&](int s) {
    const unsigned so = ((s + c.rot2) & 15) * 128;
#pragma unroll
    for (int p = 0; p < 16; ++p) dma<0>(rC, vC, so + PROW(p) * 4096, lds + (s & 3) * SLOT + p * 1024);
  };
  auto issueH = [&](int s) {
#pragma unroll
    for (int p = 0; p < 16; ++p) dma<16>(rH, vL, s * 16384 + p * 1024, lds + (s & 3) * SLOT + p * 1024);
  };
  STAMP(10);
  wait_vm<16 * (NINIT - 1)>();
  STAMP(11);
  bar();
  if (NINIT == 2) issueX(2);
#pragma unroll 1
  for (int s = 0; s < 5; ++s) { issueX(s + 3); wait_vm<32>(); bar(); }
#pragma unroll 1
  for (int s = 5; s < 7; ++s) { issueC(s - 5); wait_vm<32>(); bar(); }
  issueC(2);
  bar();
  issueC(3);
  {
    const unsigned so = (c.ks >> 1) * 65536 + (c.ec * 128 + (c.ks & 1) * 64) * 2, vo = r8 * 1024 + (p8 ^ ((r8 >> 1) & 1)) * 16;
#pragma unroll
    for (int p = 0; p < 8; ++p) dma<0>(rX, vo ^ ((p & 3) * 32), so + p * 8192, lds + LDS_XT + p * 1024);
  }
  const u64 tag = uniform64(*(const LAS u64*)(lds + LDS_TAG));
  bar();
  bar();
  wait_vm<56>();
  bar();
  {
    const rsrc_t rAB = mk_rsrc(c.ws + WS_ABT + (size_t)(((c.ks >> 1) * 4 + c.ec) * 64 + (c.ks & 1) * 32) * 8192, 262144);
    const unsigned so = ((3 + c.rot2) & 15) * 16384;
#pragma unroll
    for (int p = 0; p < 16; ++p) dma<16>(rAB, vL, so + p * 1024, lds + 5 * SLOT + p * 1024);
  }
  wait_vm<56>(); bar(); issueC(4);
  wait_vm<56>(); bar(); issueC(5);
  const char* hfp = c.ws + WS_FLAG + (unsigned)(1 * 256 + c.tp * 16 + (lane & 15)) * FLAG_STRIDE;
  u64 fv = 0;
#pragma unroll 1
  for (int s = 2; s < 12; ++s) { wait_vm<32>(); bar(); issueC(s + 4); if (s == 8) fv = ld8_async(hfp); }
  wait_vm<32>();
  asm volatile("" : "+v"(fv));
  if (!__all(fv == tag)) poll(hfp, [&](u64 x) { return x == tag; });
#pragma unroll 1
  for (int s = 12; s < 26; ++s) { wait_vm<32>(); bar(); issueH(s - 12); }
  wait_vm<32>(); bar();
  wait_vm<16>(); bar();
  __builtin_amdgcn_s_waitcnt(0x0F70);
  bar();
  STAMP_END(); BT_END();
}

DEV void loaderB(const Ctx& c) {
  DBG_DECL; BT_DECL;
  __builtin_amdgcn_s_setprio(LPRIO);
  const int lane = fresh_lane(), r8 = lane >> 3, p8 = lane & 7;
  LAS char* lds = c.lds;
  const rsrc_t rW = mk_rsrc((c.E0 >= 32 ? c.Wi : c.Wr) + (size_t)(c.E0 & 31) * 16384, 7 * 16384);
  const rsrc_t r1 = mk_rsrc(c.w1, D_ * W1R);
  const unsigned vW = r8 * 1024 + (p8 ^ r8) * 16;
  const unsigned vJ[2] = {(unsigned)(r8 * W1R + (p8 ^ (r8 & 2)) * 16), (unsigned)(r8 * W1R + (p8 ^ (r8 & 2) ^ 4) * 16)};
  auto issueB = [&](int s) {
    const int k = (s + c.rot1) & 7;
    LAS char* d = lds + (4 + (s & 3)) * SLOT;
#pragma unroll
    for (int p = 0; p < 6; ++p) dma<0>(rW, vW, k * 128 + p * 8192, d + p * 1024);
    if (c.typeB) {
#pragma unroll
      for (int p = 6; p < 14; ++p) dma<0>(rW, vW, k * 128 + p * 8192, d + p * 1024);
    } else {
      const unsigned so = k * (64 * W1R) + c.PA * 128;
#pragma unroll
      for (int p = 6; p < 14; ++p) dma<0>(r1, vJ[p & 1], so + (p - 6) * (8 * W1R), d + p * 1024);
    }
    const unsigned sj = (((c.rot1 + (s >> 2)) & 7) * 64 + 16 * (s & 3)) * W1R + c.PB * 128;
    dma<0>(r1, vJ[0], sj, lds + 8 * SLOT + s * 2048);
    dma<0>(r1, vJ[1], sj + 8 * W1R, lds + 8 * SLOT + s * 2048 + 1024);
  };
#pragma unroll 1
  for (int s = 0; s < NINIT; ++s) issueB(s);
  auto issueJ = [&](int j) {
#pragma unroll
    for (int h = 0; h < 2; ++h) {
      const unsigned so = ((2 * j + h + c.rot1) & 7) * (64 * W1R) + c.PB * 128;
#pragma unroll
      for (int p = 0; p < 8; ++p) dma<0>(r1, vJ[p & 1], so + p * (8 * W1R), lds + (3 + j) * SLOT + h * 8192 + p * 1024);
    }
  };
  const rsrc_t rAB = mk_rsrc(c.ws + WS_ABT + (size_t)(((c.ks >> 1) * 4 + c.ec) * 64 + (c.ks & 1) * 32) * 8192, 262144);
  const rsrc_t r2 = mk_rsrc(c.w2 + (c.ec * 128 + (c.ks & 1) * 64) * 2, F_ * 1024 - 1024 + 128);
  const unsigned vL = lane * 16, v2 = r8 * 1024 + (p8 ^ (r8 & 6)) * 16;
  auto issueAB = [&](int s, int slot) {
    const unsigned so = ((s + c.rot2) & 15) * 16384;
#pragma unroll
    for (int p = 0; p < 16; ++p) dma<16>(rAB, vL, so + p * 1024, lds + slot * SLOT + p * 1024);
  };
  auto issueW2 = [&](int s) {
#pragma unroll
    for (int p = 0; p < 16; ++p) dma<0>(r2, v2, s * 131072 + p * 8192, lds + (4 + (s & 3)) * SLOT + p * 1024);
  };
  wait_vm<16 * (NINIT - 1)>();
  bar();
  if (NINIT == 2) issueB(2);
#pragma unroll 1
  for (int s = 0; s < 5; ++s) { issueB(s + 3); wait_vm<32>(); bar(); }
#pragma unroll 1
  for (int s = 5; s < 8; ++s) { issueJ(s - 4); wait_vm<32>(); bar(); }
  const u64 tag = uniform64(*(const LAS u64*)(lds + LDS_TAG));
  bar();
  wait_vm<0>();
  bar();
  STAMP(1);
  {
    const int G2 = 2 * (c.ks >> 1) + (c.ec >> 1), m = lane & 3;
    const bool act = lane < 32 && ((c.ec & 1) ? (m >= 2) : (m <= 2));
    poll(c.ws + WS_FLAG + (unsigned)((8 * (c.ks & 1) + ((lane >> 2) & 7)) * 16 + 4 * G2 + m) * FLAG_STRIDE, [&](u64 x) { return !act || x == tag; });
  }
  STAMP(2);
  issueAB(0, 7); issueAB(1, 8);
  wait_vm<16>();
  bar();
  STAMP(3);
  issueAB(2, 4);
  wait_vm<16>(); bar(); issueAB(4, 6);
  wait_vm<16>(); bar(); issueAB(5, 7);
  unsigned bseq = 0x76548;
#pragma unroll 1
  for (int s = 2; s < 12; ++s) { wait_vm<32>(); bar(); issueAB(s + 4, bseq & 15); bseq = (bseq >> 4) | ((bseq & 15) << 16); }
#pragma unroll 1
  for (int s = 12; s < 26; ++s) { wait_vm<32>(); bar(); issueW2(s - 12); }
  wait_vm<32>(); bar();
  wait_vm<16>(); bar();
  __builtin_amdgcn_s_waitcnt(0x0F70);
  bar();
  STAMP_END(); BT_END();
}

DEV void helper(const Ctx& c) {
  BT_DECL;
  __builtin_amdgcn_s_setprio(HPRIO);
  const int hw = c.wid - 6, lane = fresh_lane();
  LAS char* lds = c.lds; char* ws = c.ws;
  const int q = lane >> 4, cc = lane & 15, qq = cc >> 2, pp = lane & 3, cp = c.ks * 4 + c.ec;
  const unsigned dump = (unsigned)(size_t)lds + LDS_DUMP;
  const u64 tagv = __hip_atomic_load((GAS u64*)(ws + WS_EPOCH), __ATOMIC_RELAXED, __HIP_MEMORY_SCOPE_AGENT);
  const int row = c.tp * 128 + cp * 8 + 4 * hw + q;
  u32x4 xv[4];
#pragma unroll
  for (int i = 0; i < 4; ++i) xv[i] = *(const u32x4*)(c.x + (size_t)row * 1024 + cc * 16 + i * 256);
  const u16 b1v = *(const u16*)(c.b1 + ((hw ? c.PB : c.PA) * 64 + lane) * 2);
  const u16 b2v = *(const u16*)(c.b2 + (c.ec * 128 + (c.ks & 1) * 64 + lane) * 2);
  u64 tag;
  {
    float s = 0.f;
#pragma unroll
    for (int i = 0; i < 16; ++i) s += bflo(xv[i >> 2][i & 3]) + bfhi(xv[i >> 2][i & 3]);
    const float m = sum16(s) * (1.f / 512.f);
    float z = 0.f;
#pragma unroll
    for (int i = 0; i < 16; ++i) { const float d0 = bflo(xv[i >> 2][i & 3]) - m, d1 = bfhi(xv[i >> 2][i & 3]) - m; z += d0 * d0; z += d1 * d1; }
    const float var = sum16(z) * (1.f / 512.f);
    const float sd = __builtin_amdgcn_sqrtf(rbf(rbf(var) + 1e-5f));
    tag = uniform64(tagv) + 1;
    *(LAS float*)(lds + LDS_B1 + (hw * 64 + lane) * 4) = bflo(b1v);
    *(LAS float*)(lds + LDS_B2 + lane * 4) = bflo(b2v);
    SCHED();
    if (cc == 0)
      __hip_atomic_store((GAS u64*)(ws + WS_ROWST + (size_t)row * 8), ((u64)(unsigned)tag << 32) | pk_bf16(m, sd), __ATOMIC_RELAXED, __HIP_MEMORY_SCOPE_AGENT);
    if (hw == 0 && lane == 0) { *(LAS u64*)(lds + LDS_TAG) = tag; *(LAS u64*)(lds + LDS_TAG + 8) = 0; }
  }
  const unsigned wa = cc * 128 + (((4 * hw + q) ^ (cc & 7)) * 16);
  unsigned ja[4];
#pragma unroll
  for (int n = 0; n < 4; ++n) ja[n] = (8 * q + qq) * 128 + (((2 * n + (pp >> 1)) ^ ((qq & 2) | ((q & 1) << 2))) * 16) + 8 * (pp & 1) + hw * 4096;
  const bf16x8 ones = {0x3f80, 0x3f80, 0x3f80, 0x3f80, 0x3f80, 0x3f80, 0x3f80, 0x3f80};
  const f32x4 z4 = {0.f, 0.f, 0.f, 0.f};
  f32x4 cs[7] = {z4, z4, z4, z4, z4, z4, z4};
  auto csum = [&](int s) {
    LAS char* ba = lds + (4 + (s & 3)) * SLOT;
    bf16x8 f[7];
#pragma unroll
    for (int n = 0; n < 3; ++n) f[n] = ldsb128(ba + n * 2048 + wa);
    if (c.typeB) {
#pragma unroll
      for (int n = 3; n < 7; ++n) f[n] = ldsb128(ba + n * 2048 + wa);
    } else {
#pragma unroll
      for (int n = 3; n < 7; ++n) f[n] = ldstr(ba + 6144 + ja[n - 3], 512);
    }
#pragma unroll
    for (int n = 0; n < 7; ++n) cs[n] = MFMA(ones, f[n], cs[n]);
  };
  bar();
  const char* stp = ws + WS_ROWST + (size_t)(c.tp * 128 + 32 * (lane >> 5) + 8 * qq + 4 * (q & 1) + (lane & 3)) * 8;
  u64 st0 = 0, st1 = 0;
  const char* Cp = (c.ks >> 1 ? c.Ci : c.Cr) + (size_t)(c.tp * 128 + c.ec * 32 + (lane & 31)) * 4096 + (c.ks & 1) * 2048 + (lane >> 5) * 128;
#pragma unroll 1
  for (int s = 0; s < 7; ++s) {
    if (hw == 0) { if (s == STLD) { st0 = ld8_async(stp); st1 = ld8_async(stp + 512); } }
    else if ((PM1 >> s) & 1) { touch(Cp, dump); Cp += 256; }
    csum(s);
    if (hw == 0 && s == 6) {
      wait_vm<0>();
      asm volatile("" : "+v"(st0), "+v"(st1));
      const unsigned t32 = (unsigned)tag;
      if (!__all((unsigned)(st0 >> 32) == t32)) st0 = poll(stp, [&](u64 x) { return (unsigned)(x >> 32) == t32; });
      if (!__all((unsigned)(st1 >> 32) == t32)) st1 = poll(stp + 512, [&](u64 x) { return (unsigned)(x >> 32) == t32; });
      *(LAS float*)(lds + LDS_MEAN + lane * 4) = bflo((unsigned)st0);
      *(LAS float*)(lds + LDS_RSTD + lane * 4) = __builtin_amdgcn_rcpf(bfhi((unsigned)st0));
      *(LAS float*)(lds + LDS_MEAN + 256 + lane * 4) = bflo((unsigned)st1);
      *(LAS float*)(lds + LDS_RSTD + 256 + lane * 4) = __builtin_amdgcn_rcpf(bfhi((unsigned)st1));
    }
    wait_lgkm0();
    bar();
  }
  {
    csum(7);
    const float v0 = q == 0 ? cs[0][0] : q == 1 ? cs[1][0] : q == 2 ? cs[2][0] : cs[3][0];
    const float v1 = q == 0 ? cs[4][0] : q == 1 ? cs[5][0] : cs[6][0];
    *(LAS float*)(lds + LDS_CS + (hw * 11 * 16 + lane) * 4) = v0;
    if (q < 3) *(LAS float*)(lds + LDS_CS + ((hw * 11 + 4) * 16 + lane) * 4) = v1;
    wait_lgkm0();
    bar();
  }
#pragma unroll
  for (int n = 0; n < 4; ++n) cs[n] = z4;
#pragma unroll 1
  for (int h = 0; h < 2; ++h) {
#pragma unroll
    for (int j = 0; j < 2; ++j) {
      LAS char* ba = lds + ((h | j) == 0 ? 8 : 3 + 2 * h + j) * SLOT;
#pragma unroll
      for (int k = 0; k < 2; ++k)
#pragma unroll
        for (int n = 0; n < 4; ++n) cs[n] = MFMA(ones, ldstr(ba + ja[n] + k * 8192, 512), cs[n]);
    }
    if (h == 0) {
      wait_lgkm0();
      bar();
      if (hw == 0 && lane == 0) set_flag(ws, 0, c.tp * 16 + cp, tag);
      bar();
      if (hw == 1) {
#pragma unroll
        for (int i = 0; i < PMF; ++i) { touch(Cp, dump); Cp += 256; }
      }
    }
  }
  *(LAS float*)(lds + LDS_CS + ((hw * 11 + 7) * 16 + lane) * 4) = q == 0 ? cs[0][0] : q == 1 ? cs[1][0] : q == 2 ? cs[2][0] : cs[3][0];
  wait_lgkm0();
  if (lane == 0) *(volatile LAS unsigned*)(lds + LDS_TAG + 8 + 4 * hw) = 1u;
  const char* Wp = c.w2 + (c.ec * 128 + (c.ks & 1) * 64) * 2 + (size_t)((c.tp & 7) * 224 + hw * 112 + (lane < 56 ? lane : 0)) * 1024;
  const bool ew = blockIdx.x == 0 && hw == 1;
  const char* fp = ws + WS_FLAG + (unsigned)lane * FLAG_STRIDE;
  u64 f0 = 0, f1 = 0, f2 = 0, f3 = 0;
  auto run = [&](int s0, int s1) {
#pragma unroll 1
    for (int s = s0; s < s1; ++s) {
      if (PREFW && s == PREFW) { touch(Wp, dump); touch(Wp + 56 * 1024, dump); }
      if (hw == 1 && ((PM2 >> (s + 1)) & 1)) { touch(Cp, dump); Cp += 256; }
      bar();
      if (hw == 0 && lane == 0) {
        if (s == HFLAG) set_flag(ws, 1, c.tp * 16 + cp, tag);
        if (s == YFLAG) set_flag(ws, 2, (c.tp * 4 + c.ec) * 4 + c.ks, tag);
      }
    }
  };
  wait_lgkm0();
  run(-1, 10);
  if (ew) { f0 = ld8_async(fp); f1 = ld8_async(fp + 64 * FLAG_STRIDE); f2 = ld8_async(fp + 128 * FLAG_STRIDE); f3 = ld8_async(fp + 192 * FLAG_STRIDE); }
  run(10, 15);
  wait_vm<0>();
  asm volatile("" : "+v"(f0), "+v"(f1), "+v"(f2), "+v"(f3));
  const bool early = ew && __all(f0 == tag && f1 == tag && f2 == tag && f3 == tag);
  if (early && lane == 0) __hip_atomic_store((GAS u64*)(ws + WS_EPOCH), tag, __ATOMIC_RELAXED, __HIP_MEMORY_SCOPE_AGENT);
  run(15, YPOLL + 1);
  const char* yfp = ws + WS_FLAG + (unsigned)(2 * 256 + (c.tp * 4 + c.ec) * 4 + (lane & 3)) * FLAG_STRIDE;
  f0 = ld8_async(yfp);
  run(YPOLL + 1, YPOLL + 2);
  f1 = ld8_async(yfp);
  run(YPOLL + 2, YCHK);
  wait_vm<1>();
  asm volatile("" : "+v"(f0));
  if (!__all(f0 == tag)) {
    wait_vm<0>();
    asm volatile("" : "+v"(f1));
    if (!__all(f1 == tag)) poll(yfp, [&](u64 x) { return x == tag; });
  }
  run(YCHK, 29);
  wait_vm<0>();
  asm volatile("" : "+v"(f1));
  if (ew && !early) {
#pragma unroll 1
    for (int i = 0; i < 4; ++i) poll(fp + i * 64 * FLAG_STRIDE, [&](u64 x) { return x == tag; });
    if (lane == 0) __hip_atomic_store((GAS u64*)(ws + WS_EPOCH), tag, __ATOMIC_RELAXED, __HIP_MEMORY_SCOPE_AGENT);
  }
  wait_vm<0>();
  BT_END();
}

DEV void consumer2(const Ctx& c DBG_PARAM BT_PARAM) {
  const int w = c.wid, lane = fresh_lane();
  LAS char* lds = c.lds;
  const int q = lane >> 4, cc = lane & 15, qq = cc >> 2, pp = lane & 3;
  const unsigned wa0 = cc * 128 + ((q ^ (cc & 7)) * 16), wa1 = cc * 128 + (((4 + q) ^ (cc & 7)) * 16);
  const int wt = w >> 1, we = w & 1;
  unsigned wb[2];
#pragma unroll
  for (int n = 0; n < 2; ++n) wb[n] = 4 * SLOT + (4 * q + qq) * 128 + (((2 * (2 * we + n) + (pp >> 1)) ^ ((4 * q + qq) & 6)) * 16) + 8 * (pp & 1);
  const unsigned hb = wt * 2048 + lane * 16;
  bf16x8 hA[4][2], wA[4][2], hB[4][2], wB[4][2];
  wait_lgkm0();
  bar();
  STAMP(5);
#define PREAD(HN, WN, SO) if (i < 12) { if (i % 3 == 2) { HN[i / 3][0] = ldsb128(lds + (SO) + hb + (i / 3) * 4096); HN[i / 3][1] = ldsb128(lds + (SO) + hb + (i / 3) * 4096 + 1024); } \
                                        else WN[i / 3][i % 3] = ldstr(lds + (SO) + wb[i % 3] + (i / 3) * 4096, 2048); SCHED(); }
  {
    f32x4 acc[4][4];
#pragma unroll
    for (int m = 0; m < 4; ++m)
#pragma unroll
      for (int n = 0; n < 4; ++n) acc[m][n] = (f32x4){0, 0, 0, 0};
    const unsigned a0 = wt * 8192 + wa0, a1 = wt * 8192 + wa1, b0 = we * 4096 + lane * 16;
    bf16x8 fa0[4], fb0[4], fa1[4], fb1[4];
#pragma unroll
    for (int m = 0; m < 4; ++m) { fa0[m] = ldsb128(lds + a0 + m * 2048); fb0[m] = ldsb128(lds + b0 + 7 * SLOT + m * 1024); }
#define HALF(FA, FB, NA_, NB_, AOFF, BOFF) do { _Pragma("unroll") for (int i = 0; i < 16; ++i) { \
      acc[i >> 2][i & 3] = MFMA(FB[i & 3], FA[i >> 2], acc[i >> 2][i & 3]); SCHED(); \
      if (i < 8) { if (i == 0) NB_[0] = ldsb128(lds + (BOFF)); else if (i == 1) NA_[0] = ldsb128(lds + (AOFF)); \
        else if (i < 5) NB_[i - 1] = ldsb128(lds + (BOFF) + (i - 1) * 1024); else NA_[i - 4] = ldsb128(lds + (AOFF) + (i - 4) * 2048); SCHED(); } } } while (0)
    unsigned so = 0, sb = 7 * SLOT, bseq = 0x76548;
#pragma unroll 1
    for (int seg = 0; seg < 2; ++seg) {
      const int n = seg ? 15 - HFLAG : HFLAG;
#pragma unroll 1
      for (int s = 0; s < n; ++s) {
        HALF(fa0, fb0, fa1, fb1, so + a1, sb + b0 + 8192);
        wait_lgkm0();
        bar();
        sb = (bseq & 15) * SLOT; bseq = (bseq >> 4) | ((bseq & 15) << 16); so = (so + SLOT) & (4 * SLOT - 1);
        HALF(fa1, fb1, fa0, fb0, so + a0, sb + b0);
      }
      wait_vm<0>();
    }
    HALF(fa0, fb0, fa1, fb1, so + a1, sb + b0 + 8192);
    wait_lgkm0();
    bar();
#pragma unroll
    for (int i = 0; i < 16; ++i) {
      acc[i >> 2][i & 3] = MFMA(fb1[i & 3], fa1[i >> 2], acc[i >> 2][i & 3]); SCHED();
      PREAD(hA, wA, 0)
    }
    STAMP(6);
    if (w == c.ks) {
#pragma unroll
      for (int m = 0; m < 4; ++m)
#pragma unroll
        for (int n = 0; n < 4; ++n) *(LAS f32x4*)(lds + 8 * SLOT + (m * 4 + n) * 1024 + lane * 16) = acc[m][n];
    } else {
      const rsrc_t rY = mk_rsrc(c.ws + WS_YP + (size_t)(((c.tp * 4 + c.ec) * 4 + c.ks) * 4 + w) * 8192, 8192);
#pragma unroll
      for (int m = 0; m < 4; ++m)
#pragma unroll
        for (int j = 0; j < 2; ++j) {
          const f32x4 e = acc[m][2 * j], o = acc[m][2 * j + 1];
          __builtin_amdgcn_raw_buffer_store_b128((u32x4){pk_bf16(e[0], e[1]), pk_bf16(e[2], e[3]), pk_bf16(o[0], o[1]), pk_bf16(o[2], o[3])}, rY, lane * 16 + (m * 2 + j) * 1024, 0, 16);
        }
    }
  }
  STAMP(7);
  {
    f32x4 acc[2][2];
#pragma unroll
    for (int m = 0; m < 2; ++m) { acc[m][0] = (f32x4){0, 0, 0, 0}; acc[m][1] = (f32x4){0, 0, 0, 0}; }
    unsigned so = 0;
#define PSTEP(HC, WC, HN, WN) do { wait_lgkm0(); bar(); so = (so + SLOT) & (4 * SLOT - 1); SCHED(); \
      _Pragma("unroll") for (int i = 0; i < 16; ++i) { \
        acc[(i >> 1) & 1][i & 1] = MFMA(WC[i >> 2][i & 1], HC[i >> 2][(i >> 1) & 1], acc[(i >> 1) & 1][i & 1]); SCHED(); \
        PREAD(HN, WN, so) } } while (0)
    static_assert(YFLAG == 18 && YCHK == 25, "schedule");
#pragma unroll 1
    for (int seg = 0; seg < 2; ++seg) {
      const int n = seg ? 4 : 1;
#pragma unroll 1
      for (int s = 0; s < n; ++s) { PSTEP(hA, wA, hB, wB); PSTEP(hB, wB, hA, wA); }
      wait_vm<0>();
    }
    const rsrc_t rY = mk_rsrc(c.ws + WS_YP + (size_t)((c.tp * 4 + c.ec) * 16 + c.ks) * 8192, 4 * 32768 - 3 * 8192);
    u32x4 yp[3][2];
    const unsigned yv = lane * 16 + (4 * wt + we) * 1024;
#pragma unroll
    for (int i = 0; i < 3; ++i) yp[i][0] = __builtin_amdgcn_raw_buffer_load_b128(rY, yv, ((c.ks + 1 + i) & 3) * 32768, 16);
    SCHED();
    PSTEP(hA, wA, hB, wB);
#pragma unroll
    for (int i = 0; i < 3; ++i) yp[i][1] = __builtin_amdgcn_raw_buffer_load_b128(rY, yv + 2048, ((c.ks + 1 + i) & 3) * 32768, 16);
    SCHED();
    PSTEP(hB, wB, hA, wA);
    STAMP(8);
    const int tl = 32 * wt + 8 * qq + (cc & 3), el = 32 * we + 4 * q;
    f32x4 y[2][2], bv[2];
    u32x2 xr[2][2];
#pragma unroll
    for (int n = 0; n < 2; ++n) {
      bv[n] = *(const LAS f32x4*)(lds + LDS_B2 + (el + 16 * n) * 4);
#pragma unroll
      for (int m = 0; m < 2; ++m) {
        y[m][n] = *(const LAS f32x4*)(lds + 8 * SLOT + ((2 * wt + m) * 4 + 2 * we + n) * 1024 + lane * 16);
        xr[m][n] = *(const LAS u32x2*)(lds + LDS_XT + (tl + 4 * m) * 128 + (((el + 16 * n) * 2) ^ ((2 * qq + ((cc >> 1) & 1)) * 16)));
      }
    }
    PSTEP(hA, wA, hB, wB);
#pragma unroll
    for (int m = 0; m < 2; ++m)
#pragma unroll
      for (int i = 0; i < 3; ++i) { y[m][0] += unpk(yp[i][m].x, yp[i][m].y); y[m][1] += unpk(yp[i][m].z, yp[i][m].w); }
    char* op = c.out + (size_t)(c.tp * 128 + (c.ks >> 1) * 64 + tl) * 1024 + (c.ec * 128 + (c.ks & 1) * 64 + el) * 2;
    SCHED();
#pragma unroll
    for (int i = 0; i < 16; ++i) acc[i >> 3][(i >> 2) & 1] = MFMA(wB[i & 3][(i >> 2) & 1], hB[i & 3][i >> 3], acc[i >> 3][(i >> 2) & 1]);
    STAMP(9);
#pragma unroll
    for (int m = 0; m < 2; ++m)
#pragma unroll
      for (int n = 0; n < 2; ++n) {
        f32x4 o = unpk(xr[m][n].x, xr[m][n].y);
#pragma unroll
        for (int r = 0; r < 4; ++r) { const float z = acc[m][n][r] + bv[n][r]; o[r] += y[m][n][r] * (z * __builtin_amdgcn_rcpf(1.f + __expf(-z))); }
        *(u32x2*)(op + m * 4096 + n * 32) = (u32x2){pk_bf16(o[0], o[1]), pk_bf16(o[2], o[3])};
      }
    STAMP(10);
    STAMP_END(); BT_END();
  }
}

DEV void consumer(const Ctx& c) {
  DBG_DECL; BT_DECL;
  const int w = c.wid;
  LAS char* lds = c.lds;
  STAMP(0);
  unsigned ja[4], bx0, bx1, bw0, bw1;
  {
    const int lane = fresh_lane(), q = lane >> 4, cc = lane & 15, qq = cc >> 2, pp = lane & 3;
    if (PREFI) { touch(c.pc + w * 8192 + lane * 128, (unsigned)(size_t)lds + LDS_DUMP); if (256 + w * 64 < PREFI) touch(c.pc + 32768 + w * 8192 + lane * 128, (unsigned)(size_t)lds + LDS_DUMP); }
    const unsigned wa0 = cc * 128 + ((q ^ (cc & 7)) * 16), wa1 = cc * 128 + (((4 + q) ^ (cc & 7)) * 16);
#pragma unroll
    for (int n = 0; n < 4; ++n) ja[n] = opq(4 * SLOT + (8 * q + qq) * 128 + (((2 * n + (pp >> 1)) ^ ((qq & 2) | ((q & 1) << 2))) * 16) + 8 * (pp & 1));
    bx0 = opq(wa0 + w * 4096); bx1 = opq(wa1 + w * 4096); bw0 = opq(wa0 + 4 * SLOT); bw1 = opq(wa1 + 4 * SLOT);
  }
  bf16x8 xf[2][16];
  struct HTab { float mean[2], rstd[2]; f32x4 cs[4], bv[4]; unsigned vo; };
  auto h_tab = [&](HTab& t, int brow, int csrow) {
    const int lane = fresh_lane(), q = lane >> 4, cc = lane & 15;
#pragma unroll
    for (int u = 0; u < 2; ++u) { t.mean[u] = *(const LAS float*)(lds + LDS_MEAN + ((2 * w + u) * 16 + cc) * 4); t.rstd[u] = *(const LAS float*)(lds + LDS_RSTD + ((2 * w + u) * 16 + cc) * 4); }
#pragma unroll
    for (int n = 0; n < 4; ++n) {
      t.cs[n] = *(const LAS f32x4*)(lds + LDS_CS + ((csrow + n) * 16 + 4 * q) * 4) + *(const LAS f32x4*)(lds + LDS_CS + ((11 + csrow + n) * 16 + 4 * q) * 4);
      t.bv[n] = *(const LAS f32x4*)(lds + LDS_B1 + (brow * 64 + n * 16 + 4 * q) * 4);
    }
    t.vo = lane * 16;
  };
  auto h_half = [&](const HTab& t, const f32x4& a, int n, int u) {
    f32x4 z = (a - t.mean[u] * t.cs[n]) * t.rstd[u] + t.bv[n];
    z = __builtin_elementwise_max(z, (f32x4){0.f, 0.f, 0.f, 0.f});
    z = z * z;
    return (u32x2){pk_bf16(z[0], z[1]), pk_bf16(z[2], z[3])};
  };
  auto h_store = [&](const HTab& t, u32x2 d0, u32x2 d1, int P, int b, int u) {
    const rsrc_t rH = mk_rsrc(c.ws + WS_H + (size_t)((((2 * c.tp + (w >> 1)) * 56 + 2 * P) * 4 + 2 * (w & 1)) * 1024), 8192 - 2048);
    __builtin_amdgcn_raw_buffer_store_b128((u32x4){d0.x, d0.y, d1.x, d1.y}, rH, t.vo + u * 1024 + b * 4096, 0, 16);
  };
  constexpr int L2 = 8;
  bf16x8 g[64];
  auto rd2 = [&](auto t_) {
    constexpr int t = decltype(t_)::value, j = t / 16, k = (t / 4) & 3, n = t & 3;
    if constexpr (j == 0) g[t] = ldstr(lds + 4 * SLOT + ja[n] + k * 4096, 512); else g[t] = ldstr(lds + (j - 1) * SLOT + ja[n] + k * 4096, 512);
  };
  bar();
  STAMP(1);
  auto pass1 = [&](auto na_) {
    constexpr int NA = decltype(na_)::value;
    f32x4 acc[7][2];
#pragma unroll
    for (int n = 0; n < 7; ++n) { acc[n][0] = (f32x4){0, 0, 0, 0}; acc[n][1] = (f32x4){0, 0, 0, 0}; }
    bf16x8 fr[112];
    auto rd = [&](auto t_) {
      constexpr int t = decltype(t_)::value, kk = t / 7, n = t % 7, so = ((kk >> 1) & 3) * SLOT;
      if constexpr (n == 0) { xf[0][kk] = ldsb128(lds + ((kk & 1) ? bx1 : bx0) + so); xf[1][kk] = ldsb128(lds + ((kk & 1) ? bx1 : bx0) + so + 2048); }
      if constexpr (n < NA) fr[t] = ldsb128(lds + ((kk & 1) ? bw1 : bw0) + so + n * 2048);
      else fr[t] = ldstr(lds + ja[(n - 3) & 3] + so + 6144 + (kk & 1) * 4096, 512);
    };
    static_for<0, 7>(rd);
    SCHED();
    static_for<0, 112>([&](auto p_) {
      constexpr int p = decltype(p_)::value, kk = p / 7, n = p % 7;
      constexpr int kn = p / 14 + 1;
      if constexpr (p == 14 * kn - lead1(kn)) { wait_lgkm0(); bar(); SCHED(); }
      const bf16x8 f = fr[p];
      if constexpr (n < NA) acc[n][0] = MFMA(xf[0][kk], f, acc[n][0]); else acc[n][0] = MFMA(f, xf[0][kk], acc[n][0]);
      SCHED();
      static_for<p + 1, p + 15>([&](auto t_) {
        constexpr int t = decltype(t_)::value;
        if constexpr (t < 112 && t >= 7 && sched1(t) == p) { rd(t_); SCHED(); }
      });
      if constexpr (n < NA) acc[n][1] = MFMA(xf[1][kk], f, acc[n][1]); else acc[n][1] = MFMA(f, xf[1][kk], acc[n][1]);
      SCHED();
    });
    STAMP(2);
    const int lane = fresh_lane(), q = lane >> 4, cc = lane & 15;
    f32x4 mr[2], rr[2];
    const float sgn = c.E0 >= 32 ? -1.f : 1.f;
#pragma unroll
    for (int u = 0; u < 2; ++u) {
      mr[u] = *(const LAS f32x4*)(lds + LDS_MEAN + ((2 * w + u) * 16 + 4 * q) * 4);
      rr[u] = *(const LAS f32x4*)(lds + LDS_RSTD + ((2 * w + u) * 16 + 4 * q) * 4) * sgn;
    }
    float cs[NA];
#pragma unroll
    for (int n = 0; n < NA; ++n) cs[n] = *(const LAS float*)(lds + LDS_CS + (n * 16 + cc) * 4) + *(const LAS float*)(lds + LDS_CS + ((11 + n) * 16 + cc) * 4);
    const rsrc_t rA = mk_rsrc(c.ws + WS_ABT, 4u << 20);
#pragma unroll
    for (int n = 0; n < NA; ++n) {
      const f32x4 o0 = (acc[n][0] - mr[0] * cs[n]) * rr[0], o1 = (acc[n][1] - mr[1] * cs[n]) * rr[1];
      u32x4 d = {pk_bf16(o0[0], o0[1]), pk_bf16(o0[2], o0[3]), pk_bf16(o1[0], o1[1]), pk_bf16(o1[2], o1[3])};
      const int E = c.E0 + n;
      __builtin_amdgcn_raw_buffer_store_b128(d, rA, lane * 16 + (unsigned)((((E >> 3) * 64 + c.tp * 4 + w) * 8 + (E & 7)) * 1024), 0, 16);
    }
    SCHED();
    static_for<0, L2>(rd2);
    STAMP(3);
    f32x4 ac2[4][2];
#pragma unroll
    for (int n = 0; n < 4; ++n) { ac2[n][0] = (f32x4){0, 0, 0, 0}; ac2[n][1] = (f32x4){0, 0, 0, 0}; }
    HTab ht;
    u32x2 hd[2];
    SCHED();
    static_assert(RPOS >= 16 && RPOS + L2 <= 32 && FPOS < RPOS, "R must follow the last use of J0 and precede the first read of J2");
    static_for<0, 64>([&](auto p_) {
      constexpr int p = decltype(p_)::value, kk = p / 4, n = p & 3;
      if constexpr (p == FPOS) { wait_vm<0>(); bar(); SCHED(); }
      if constexpr (p == RPOS) { bar(); SCHED(); }
      ac2[n][0] = MFMA(g[p], xf[0][kk], ac2[n][0]);
      SCHED();
      if constexpr (p + L2 < 64) { rd2(IC<p + L2>{}); SCHED(); }
      if constexpr (NA == 3) {
        constexpr int e0 = EPOS + 6;
        if constexpr (p == EPOS) h_tab(ht, 0, 3);
        if constexpr (p >= e0 && p < e0 + 32 && (p - e0) % 4 == 0) {
          constexpr int i = (p - e0) / 4, b = i >> 2, u = (i >> 1) & 1, h = i & 1;
          hd[h] = h_half(ht, acc[3 + 2 * b + h][u], 2 * b + h, u);
          if constexpr (h == 1) h_store(ht, hd[0], hd[1], c.PA, b, u);
        }
      }
      ac2[n][1] = MFMA(g[p], xf[1][kk], ac2[n][1]);
      if constexpr (!(NA == 3 && p >= EPOS + 6 && p < EPOS + 38 && (p - EPOS - 6) % 4 < 2)) SCHED();
    });
    STAMP(11);
    while (*(volatile LAS u64*)(lds + LDS_TAG + 8) != 0x100000001ull) __builtin_amdgcn_s_sleep(1);
    asm volatile("" ::: "memory");
    h_tab(ht, 1, 7);
#pragma unroll
    for (int b = 0; b < 2; ++b)
#pragma unroll
      for (int u = 0; u < 2; ++u) h_store(ht, h_half(ht, ac2[2 * b][u], 2 * b, u), h_half(ht, ac2[2 * b + 1][u], 2 * b + 1, u), c.PB, b, u);
  };
  if (c.typeB) pass1(IC<7>{}); else pass1(IC<3>{});
  STAMP(4);
  consumer2(c DBG_ARG BT_ARG);
}

__global__ __launch_bounds__(512, 2) void fnet_mlp_fused(const char* x, const char* Cr, const char* Ci, const char* Wr, const char* Wi,
                                                         const char* w1, const char* b1, const char* w2, const char* b2, char* out, char* ws) {
  extern __shared__ __attribute__((aligned(16))) char shm[];
  Ctx c;
  { u64 pc; asm volatile("s_getpc_b64 %0" : "=s"(pc)); c.pc = (const char*)(pc & ~(u64)255); }
  c.x = x; c.Cr = Cr; c.Ci = Ci; c.Wr = Wr; c.Wi = Wi; c.w1 = w1; c.b1 = b1; c.w2 = w2; c.b2 = b2; c.out = out; c.ws = ws;
  c.lds = (LAS char*)shm;
  c.wid = __builtin_amdgcn_readfirstlane(threadIdx.x >> 6);
  const int b = blockIdx.x, xcd = b & 7, r = b >> 3;
  c.ks = xcd >> 1; c.tp = (xcd & 1) * 8 + (r >> 2); c.ec = r & 3;
  c.typeB = c.ec == 3;
  c.E0 = 16 * c.ks + 3 * c.ec; c.PA = 7 * c.ks + 2 * c.ec; c.PB = c.typeB ? c.PA : c.PA + 1;
  c.rot1 = (ROT & 1) ? (2 * c.ec + (r >> 2)) & 7 : 0; c.rot2 = (ROT & 2) ? (4 * c.ec + 2 * (r >> 2)) & 15 : 0;
  if (c.wid == 4) loaderA(c); else if (c.wid == 5) loaderB(c); else if (c.wid >= 6) helper(c); else consumer(c);
}

extern "C" void kernel_launch(void* const* d_in, const int* in_sizes, int n_in, void* d_out, int out_size, void* d_ws, size_t ws_size, hipStream_t stream) {
  (void)hipFuncSetAttribute((const void*)fnet_mlp_fused, hipFuncAttributeMaxDynamicSharedMemorySize, LDS_BYTES);
  fnet_mlp_fused<<<256, 512, LDS_BYTES, stream>>>((const char*)d_in[0], (const char*)d_in[1], (const char*)d_in[2], (const char*)d_in[3], (const char*)d_in[4],
                                                  (const char*)d_in[6], (const char*)d_in[7], (const char*)d_in[8], (const char*)d_in[9], (char*)d_out, (char*)d_ws);
}
```

```cpp
#define PROBE 0
#include <hip/hip_runtime.h>
typedef unsigned short u16;
typedef unsigned long long u64;
typedef short bf16x8 __attribute__((ext_vector_type(8)));
typedef short s16x4 __attribute__((ext_vector_type(4)));
typedef float f32x4 __attribute__((ext_vector_type(4)));
typedef float f32x2 __attribute__((ext_vector_type(2)));
typedef unsigned u32x4 __attribute__((ext_vector_type(4)));
typedef unsigned u32x2 __attribute__((ext_vector_type(2)));
typedef __bf16 bf2 __attribute__((ext_vector_type(2)));
#define LAS __attribute__((address_space(3)))
#define GAS __attribute__((address_space(1)))
#define DEV __device__ __forceinline__

constexpr int S_ = 2048, D_ = 512, F_ = 1792, W1R = F_ * 2;
constexpr unsigned WS_EPOCH = 0, WS_FLAG = 4096, FLAG_STRIDE = 128, WS_ROWST = 256u << 10;
constexpr unsigned WS_ABT = 1u << 20, WS_H = 8u << 20, WS_YP = 16u << 20, WS_BYTES = 32u << 20;
constexpr int SLOT = 16384, NSLOT = 9;
constexpr int LDS_XT = NSLOT * SLOT;
constexpr int LDS_MEAN = LDS_XT + 8192, LDS_RSTD = LDS_MEAN + 512, LDS_CS = LDS_RSTD + 512;
constexpr int LDS_B1 = LDS_CS + 22 * 64, LDS_B2 = LDS_B1 + 512, LDS_TAG = LDS_B2 + 256;
constexpr int LDS_DUMP = LDS_TAG + 16, LDS_BYTES = LDS_DUMP + 256;
static_assert(LDS_BYTES <= 160 * 1024, "LDS");
#define LEADE 6
#define LOOKE 13
#define LEADL 4
#define LOOKL 9
#define ROT 1
#define PREFC 2
#define PM1 0x14
#define PMF 2
#define PM2 0x55
#define PREFS 2
#define PREFI 320
#define PREFW 8
#define HPRIO 2
#define LPRIO 0
#define STLD 3
#define NINIT 3
#define HEARLY 0
#define EPOS 18
#define FPOS 8
#define RPOS 16
constexpr int HFLAG = 3, YFLAG = 18, YPOLL = 22, YCHK = 25;

template <int N> DEV void wait_vm() { asm volatile("s_waitcnt vmcnt(%0)" ::"n"(N) : "memory"); }
DEV void wait_lgkm0() { asm volatile("s_waitcnt lgkmcnt(0)" ::: "memory"); }
DEV void bar() { asm volatile("" ::: "memory"); __builtin_amdgcn_s_barrier(); asm volatile("" ::: "memory"); }
#define rawbar() __builtin_amdgcn_s_barrier()
#define BT_DECL
#define BT_END() do {} while (0)
#define BT_PARAM
#define BT_ARG
#define SCHED() __builtin_amdgcn_sched_barrier(0)
#define MFMA(a, b, c) __builtin_amdgcn_mfma_f32_16x16x32_bf16((a), (b), (c), 0, 0, 0)
DEV unsigned pk_bf16(float a, float b) { f32x2 f = {a, b}; return __builtin_bit_cast(unsigned, __builtin_convertvector(f, bf2)); }
DEV float rbf(float a) { return __uint_as_float(pk_bf16(a, 0.f) << 16); }
DEV float bflo(unsigned v) { return __uint_as_float(v << 16); }
DEV float bfhi(unsigned v) { return __uint_as_float(v & 0xffff0000u); }
DEV f32x4 unpk(unsigned a, unsigned b) { return (f32x4){bflo(a), bfhi(a), bflo(b), bfhi(b)}; }
DEV bf16x8 ldsb128(LAS char* p) { return *(const LAS bf16x8*)p; }
DEV bf16x8 ldstr(LAS char* p, int second) {
  s16x4 a = __builtin_amdgcn_ds_read_tr16_b64_v4i16((LAS s16x4*)p);
  s16x4 b = __builtin_amdgcn_ds_read_tr16_b64_v4i16((LAS s16x4*)(p + second));
  return __builtin_shufflevector(a, b, 0, 1, 2, 3, 4, 5, 6, 7);
}
DEV unsigned opq(unsigned v) { asm volatile("" : "+v"(v)); return v; }
DEV int fresh_lane() { int l; asm volatile("v_mbcnt_lo_u32_b32 %0, -1, 0\n\tv_mbcnt_hi_u32_b32 %0, -1, %0" : "=v"(l)); return l; }
typedef __amdgpu_buffer_rsrc_t rsrc_t;
DEV rsrc_t mk_rsrc(const void* p, unsigned bytes) { return __builtin_amdgcn_make_buffer_rsrc((void*)p, 0, bytes, 0x00020000); }
template <int AUX> DEV void dma(rsrc_t rs, unsigned voff, unsigned soff, LAS char* dst) { __builtin_amdgcn_raw_ptr_buffer_load_lds(rs, (LAS void*)dst, 16, voff, soff, 0, AUX); }
DEV u64 ld8_async(const char* p) { u64 v; asm volatile("global_load_dwordx2 %0, %1, off sc1" : "=v"(v) : "v"(p) : "memory"); return v; }
DEV void touch(const char* p, unsigned dump) {
  unsigned keep;
  asm volatile("s_mov_b32 %0, m0\n\ts_mov_b32 m0, %2\n\ts_nop 0\n\tglobal_load_lds_dword %1, off\n\ts_mov_b32 m0, %0" : "=&s"(keep) : "v"(p), "s"(dump) : "memory");
}
template <int CTRL> DEV float dpp_add(float x) { return x + __builtin_bit_cast(float, __builtin_amdgcn_update_dpp(0, __builtin_bit_cast(int, x), CTRL, 0xf, 0xf, false)); }
DEV float sum16(float x) { x = dpp_add<0xB1>(x); x = dpp_add<0x4E>(x); x = dpp_add<0x141>(x); return dpp_add<0x140>(x); }

#define STAMP(i) do { if (c.wid < 4) { if ((i) == PROBE) st_v = __builtin_amdgcn_s_memrealtime(); \
    if ((i) == 10 && blockIdx.x == 77 && c.wid == 0) { const u64 e = st_v + 10000; while (__builtin_amdgcn_s_memrealtime() < e) __builtin_amdgcn_s_sleep(1); } } } while (0)
#define STAMP_END() do {} while (0)
#define DBG_DECL u64 st_v = 0
#define DBG_PARAM , u64& st_v
#define DBG_ARG , st_v

template <int N> struct IC { static constexpr int value = N; };
template <int B, int E, class F> DEV void static_for(F&& f) { if constexpr (B < E) { f(IC<B>{}); static_for<B + 1, E>(f); } }
constexpr int lead1(int k) { return k >= 7 ? LEADL : LEADE; }
constexpr int sched1(int t) { const int k = t / 14, e0 = t - (k >= 7 ? LOOKL : LOOKE), e1 = 14 * k - lead1(k) + (t - 14 * k) / 2; return k == 0 ? (t - 7 > 0 ? (t - 7) / 2 : 0) : (e0 > e1 ? e0 : e1); }

struct Ctx {
  const char *x, *Cr, *Ci, *Wr, *Wi, *w1, *b1, *w2, *b2; char* out; char* ws; LAS char* lds; const char* pc;
  int wid, tp, ec, ks;
  int E0, PA, PB, rot1, rot2;
  bool typeB;
};

template <class P> DEV u64 poll(const char* p, P ok) {
  const u64 t0 = __builtin_amdgcn_s_memrealtime();
  u64 v;
  for (;;) {
    v = __hip_atomic_load((GAS u64*)p, __ATOMIC_RELAXED, __HIP_MEMORY_SCOPE_AGENT);
    if (__all(ok(v))) break;
    if (__builtin_amdgcn_s_memrealtime() - t0 > 20000000ull) break;
    __builtin_amdgcn_s_sleep(1);
  }
  return v;
}
DEV void set_flag(char* ws, int kind, int idx, u64 tag) {
  __hip_atomic_store((GAS u64*)(ws + WS_FLAG + (unsigned)(kind * 256 + idx) * FLAG_STRIDE), tag, __ATOMIC_RELAXED, __HIP_MEMORY_SCOPE_AGENT);
}
DEV u64 uniform64(u64 v) {
  const unsigned lo = __builtin_amdgcn_readfirstlane((unsigned)v), hi = __builtin_amdgcn_readfirstlane((unsigned)(v >> 32));
  return ((u64)hi << 32) | lo;
}
#define PROW(p) (32 * ((p) >> 2) + 16 * ((p) & 1) + 4 * (((p) >> 1) & 1))

DEV void loaderA(const Ctx& c) {
  DBG_DECL; BT_DECL;
  __builtin_amdgcn_s_setprio(LPRIO);
  const int lane = fresh_lane(), r8 = lane >> 3, p8 = lane & 7;
  LAS char* lds = c.lds;
  const unsigned rl = 8 * (r8 >> 2) + (r8 & 3), swz = (unsigned)((p8 ^ r8) * 16);
  const rsrc_t rX = mk_rsrc(c.x + (size_t)c.tp * 131072, 131072);
  const unsigned vX = rl * 1024 + swz;
  auto issueX = [&](int s) {
    const unsigned so = ((s + c.rot1) & 7) * 128;
#pragma unroll
    for (int p = 0; p < 16; ++p) dma<0>(rX, vX, so + PROW(p) * 1024, lds + (s & 3) * SLOT + p * 1024);
  };
  STAMP(8);
  issueX(0); STAMP(9); issueX(1);
  if (NINIT == 3) issueX(2);
  const rsrc_t rC = mk_rsrc((c.ks >> 1 ? c.Ci : c.Cr) + (size_t)c.tp * 524288 + (c.ks & 1) * 2048, 524288 - 2048);
  const rsrc_t rH = mk_rsrc(c.ws + WS_H + (size_t)(2 * c.tp + (c.ks >> 1)) * (56 * 4096), 56 * 4096);
  const unsigned vC = rl * 4096 + swz, vL = lane * 16;
  auto issueC = [&](int s) {
    const unsigned so = ((s + c.rot2) & 15) * 128;
#pragma unroll
    for (int p = 0; p < 16; ++p) dma<0>(rC, vC, so + PROW(p) * 4096, lds + (s & 3) * SLOT + p * 1024);
  };
  auto issueH = [&](int s) {
#pragma unroll
    for (int p = 0; p < 16; ++p) dma<16>(rH, vL, s * 16384 + p * 1024, lds + (s & 3) * SLOT + p * 1024);
  };
  STAMP(10);
  wait_vm<16 * (NINIT - 1)>();
  STAMP(11);
  bar();
  if (NINIT == 2) issueX(2);
#pragma unroll 1
  for (int s = 0; s < 5; ++s) { issueX(s + 3); wait_vm<32>(); bar(); }
#pragma unroll 1
  for (int s = 5; s < 7; ++s) { issueC(s - 5); wait_vm<32>(); bar(); }
  issueC(2);
  bar();
  issueC(3);
  {
    const unsigned so = (c.ks >> 1) * 65536 + (c.ec * 128 + (c.ks & 1) * 64) * 2, vo = r8 * 1024 + (p8 ^ ((r8 >> 1) & 1)) * 16;
#pragma unroll
    for (int p = 0; p < 8; ++p) dma<0>(rX, vo ^ ((p & 3) * 32), so + p * 8192, lds + LDS_XT + p * 1024);
  }
  const u64 tag = uniform64(*(const LAS u64*)(lds + LDS_TAG));
  bar();
  bar();
  wait_vm<56>();
  bar();
  {
    const rsrc_t rAB = mk_rsrc(c.ws + WS_ABT + (size_t)(((c.ks >> 1) * 4 + c.ec) * 64 + (c.ks & 1) * 32) * 8192, 262144);
    const unsigned so = ((3 + c.rot2) & 15) * 16384;
#pragma unroll
    for (int p = 0; p < 16; ++p) dma<16>(rAB, vL, so + p * 1024, lds + 5 * SLOT + p * 1024);
  }
  wait_vm<56>(); bar(); issueC(4);
  wait_vm<56>(); bar(); issueC(5);
  const char* hfp = c.ws + WS_FLAG + (unsigned)(1 * 256 + c.tp * 16 + (lane & 15)) * FLAG_STRIDE;
  u64 fv = 0;
#pragma unroll 1
  for (int s = 2; s < 12; ++s) { wait_vm<32>(); bar(); issueC(s + 4); if (s == 8) fv = ld8_async(hfp); }
  wait_vm<32>();
  asm volatile("" : "+v"(fv));
  if (!__all(fv == tag)) poll(hfp, [&](u64 x) { return x == tag; });
#pragma unroll 1
  for (int s = 12; s < 26; ++s) { wait_vm<32>(); bar(); issueH(s - 12); }
  wait_vm<32>(); bar();
  wait_vm<16>(); bar();
  __builtin_amdgcn_s_waitcnt(0x0F70);
  bar();
  STAMP_END(); BT_END();
}

DEV void loaderB(const Ctx& c) {
  DBG_DECL; BT_DECL;
  __builtin_amdgcn_s_setprio(LPRIO);
  const int lane = fresh_lane(), r8 = lane >> 3, p8 = lane & 7;
  LAS char* lds = c.lds;
  const rsrc_t rW = mk_rsrc((c.E0 >= 32 ? c.Wi : c.Wr) + (size_t)(c.E0 & 31) * 16384, 7 * 16384);
  const rsrc_t r1 = mk_rsrc(c.w1, D_ * W1R);
  const unsigned vW = r8 * 1024 + (p8 ^ r8) * 16;
  const unsigned vJ[2] = {(unsigned)(r8 * W1R + (p8 ^ (r8 & 2)) * 16), (unsigned)(r8 * W1R + (p8 ^ (r8 & 2) ^ 4) * 16)};
  auto issueB = [&](int s) {
    const int k = (s + c.rot1) & 7;
    LAS char* d = lds + (4 + (s & 3)) * SLOT;
#pragma unroll
    for (int p = 0; p < 6; ++p) dma<0>(rW, vW, k * 128 + p * 8192, d + p * 1024);
    if (c.typeB) {
#pragma unroll
      for (int p = 6; p < 14; ++p) dma<0>(rW, vW, k * 128 + p * 8192, d + p * 1024);
    } else {
      const unsigned so = k * (64 * W1R) + c.PA * 128;
#pragma unroll
      for (int p = 6; p < 14; ++p) dma<0>(r1, vJ[p & 1], so + (p - 6) * (8 * W1R), d + p * 1024);
    }
    const unsigned sj = (((c.rot1 + (s >> 2)) & 7) * 64 + 16 * (s & 3)) * W1R + c.PB * 128;
    dma<0>(r1, vJ[0], sj, lds + 8 * SLOT + s * 2048);
    dma<0>(r1, vJ[1], sj + 8 * W1R, lds + 8 * SLOT + s * 2048 + 1024);
  };
#pragma unroll 1
  for (int s = 0; s < NINIT; ++s) issueB(s);
  auto issueJ = [&](int j) {
#pragma unroll
    for (int h = 0; h < 2; ++h) {
      const unsigned so = ((2 * j + h + c.rot1) & 7) * (64 * W1R) + c.PB * 128;
#pragma unroll
      for (int p = 0; p < 8; ++p) dma<0>(r1, vJ[p & 1], so + p * (8 * W1R), lds + (3 + j) * SLOT + h * 8192 + p * 1024);
    }
  };
  const rsrc_t rAB = mk_rsrc(c.ws + WS_ABT + (size_t)(((c.ks >> 1) * 4 + c.ec) * 64 + (c.ks & 1) * 32) * 8192, 262144);
  const rsrc_t r2 = mk_rsrc(c.w2 + (c.ec * 128 + (c.ks & 1) * 64) * 2, F_ * 1024 - 1024 + 128);
  const unsigned vL = lane * 16, v2 = r8 * 1024 + (p8 ^ (r8 & 6)) * 16;
  auto issueAB = [&](int s, int slot) {
    const unsigned so = ((s + c.rot2) & 15) * 16384;
#pragma unroll
    for (int p = 0; p < 16; ++p) dma<16>(rAB, vL, so + p * 1024, lds + slot * SLOT + p * 1024);
  };
  auto issueW2 = [&](int s) {
#pragma unroll
    for (int p = 0; p < 16; ++p) dma<0>(r2, v2, s * 131072 + p * 8192, lds + (4 + (s & 3)) * SLOT + p * 1024);
  };
  wait_vm<16 * (NINIT - 1)>();
  bar();
  if (NINIT == 2) issueB(2);
#pragma unroll 1
  for (int s = 0; s < 5; ++s) { issueB(s + 3); wait_vm<32>(); bar(); }
#pragma unroll 1
  for (int s = 5; s < 8; ++s) { issueJ(s - 4); wait_vm<32>(); bar(); }
  const u64 tag = uniform64(*(const LAS u64*)(lds + LDS_TAG));
  bar();
  wait_vm<0>();
  bar();
  STAMP(1);
  {
    const int G2 = 2 * (c.ks >> 1) + (c.ec >> 1), m = lane & 3;
    const bool act = lane < 32 && ((c.ec & 1) ? (m >= 2) : (m <= 2));
    poll(c.ws + WS_FLAG + (unsigned)((8 * (c.ks & 1) + ((lane >> 2) & 7)) * 16 + 4 * G2 + m) * FLAG_STRIDE, [&](u64 x) { return !act || x == tag; });
  }
  STAMP(2);
  issueAB(0, 7); issueAB(1, 8);
  wait_vm<16>();
  bar();
  STAMP(3);
  issueAB(2, 4);
  wait_vm<16>(); bar(); issueAB(4, 6);
  wait_vm<16>(); bar(); issueAB(5, 7);
  unsigned bseq = 0x76548;
#pragma unroll 1
  for (int s = 2; s < 12; ++s) { wait_vm<32>(); bar(); issueAB(s + 4, bseq & 15); bseq = (bseq >> 4) | ((bseq & 15) << 16); }
#pragma unroll 1
  for (int s = 12; s < 26; ++s) { wait_vm<32>(); bar(); issueW2(s - 12); }
  wait_vm<32>(); bar();
  wait_vm<16>(); bar();
  __builtin_amdgcn_s_waitcnt(0x0F70);
  bar();
  STAMP_END(); BT_END();
}

DEV void helper(const Ctx& c) {
  BT_DECL;
  __builtin_amdgcn_s_setprio(HPRIO);
  const int hw = c.wid - 6, lane = fresh_lane();
  LAS char* lds = c.lds; char* ws = c.ws;
  const int q = lane >> 4, cc = lane & 15, qq = cc >> 2, pp = lane & 3, cp = c.ks * 4 + c.ec;
  const unsigned dump = (unsigned)(size_t)lds + LDS_DUMP;
  const u64 tagv = __hip_atomic_load((GAS u64*)(ws + WS_EPOCH), __ATOMIC_RELAXED, __HIP_MEMORY_SCOPE_AGENT);
  const int row = c.tp * 128 + cp * 8 + 4 * hw + q;
  u32x4 xv[4];
#pragma unroll
  for (int i = 0; i < 4; ++i) xv[i] = *(const u32x4*)(c.x + (size_t)row * 1024 + cc * 16 + i * 256);
  const u16 b1v = *(const u16*)(c.b1 + ((hw ? c.PB : c.PA) * 64 + lane) * 2);
  const u16 b2v = *(const u16*)(c.b2 + (c.ec * 128 + (c.ks & 1) * 64 + lane) * 2);
  u64 tag;
  {
    float s = 0.f;
#pragma unroll
    for (int i = 0; i < 16; ++i) s += bflo(xv[i >> 2][i & 3]) + bfhi(xv[i >> 2][i & 3]);
    const float m = sum16(s) * (1.f / 512.f);
    float z = 0.f;
#pragma unroll
    for (int i = 0; i < 16; ++i) { const float d0 = bflo(xv[i >> 2][i & 3]) - m, d1 = bfhi(xv[i >> 2][i & 3]) - m; z += d0 * d0; z += d1 * d1; }
    const float var = sum16(z) * (1.f / 512.f);
    const float sd = __builtin_amdgcn_sqrtf(rbf(rbf(var) + 1e-5f));
    tag = uniform64(tagv) + 1;
    *(LAS float*)(lds + LDS_B1 + (hw * 64 + lane) * 4) = bflo(b1v);
    *(LAS float*)(lds + LDS_B2 + lane * 4) = bflo(b2v);
    SCHED();
    if (cc == 0)
      __hip_atomic_store((GAS u64*)(ws + WS_ROWST + (size_t)row * 8), ((u64)(unsigned)tag << 32) | pk_bf16(m, sd), __ATOMIC_RELAXED, __HIP_MEMORY_SCOPE_AGENT);
    if (hw == 0 && lane == 0) { *(LAS u64*)(lds + LDS_TAG) = tag; *(LAS u64*)(lds + LDS_TAG + 8) = 0; }
  }
  const unsigned wa = cc * 128 + (((4 * hw + q) ^ (cc & 7)) * 16);
  unsigned ja[4];
#pragma unroll
  for (int n = 0; n < 4; ++n) ja[n] = (8 * q + qq) * 128 + (((2 * n + (pp >> 1)) ^ ((qq & 2) | ((q & 1) << 2))) * 16) + 8 * (pp & 1) + hw * 4096;
  const bf16x8 ones = {0x3f80, 0x3f80, 0x3f80, 0x3f80, 0x3f80, 0x3f80, 0x3f80, 0x3f80};
  const f32x4 z4 = {0.f, 0.f, 0.f, 0.f};
  f32x4 cs[7] = {z4, z4, z4, z4, z4, z4, z4};
  auto csum = [&](int s) {
    LAS char* ba = lds + (4 + (s & 3)) * SLOT;
    bf16x8 f[7];
#pragma unroll
    for (int n = 0; n < 3; ++n) f[n] = ldsb128(ba + n * 2048 + wa);
    if (c.typeB) {
#pragma unroll
      for (int n = 3; n < 7; ++n) f[n] = ldsb128(ba + n * 2048 + wa);
    } else {
#pragma unroll
      for (int n = 3; n < 7; ++n) f[n] = ldstr(ba + 6144 + ja[n - 3], 512);
    }
#pragma unroll
    for (int n = 0; n < 7; ++n) cs[n] = MFMA(ones, f[n], cs[n]);
  };
  bar();
  const char* stp = ws + WS_ROWST + (size_t)(c.tp * 128 + 32 * (lane >> 5) + 8 * qq + 4 * (q & 1) + (lane & 3)) * 8;
  u64 st0 = 0, st1 = 0;
  const char* Cp = (c.ks >> 1 ? c.Ci : c.Cr) + (size_t)(c.tp * 128 + c.ec * 32 + (lane & 31)) * 4096 + (c.ks & 1) * 2048 + (lane >> 5) * 128;
#pragma unroll 1
  for (int s = 0; s < 7; ++s) {
    if (hw == 0) { if (s == STLD) { st0 = ld8_async(stp); st1 = ld8_async(stp + 512); } }
    else if ((PM1 >> s) & 1) { touch(Cp, dump); Cp += 256; }
    csum(s);
    if (hw == 0 && s == 6) {
      wait_vm<0>();
      asm volatile("" : "+v"(st0), "+v"(st1));
      const unsigned t32 = (unsigned)tag;
      if (!__all((unsigned)(st0 >> 32) == t32)) st0 = poll(stp, [&](u64 x) { return (unsigned)(x >> 32) == t32; });
      if (!__all((unsigned)(st1 >> 32) == t32)) st1 = poll(stp + 512, [&](u64 x) { return (unsigned)(x >> 32) == t32; });
      *(LAS float*)(lds + LDS_MEAN + lane * 4) = bflo((unsigned)st0);
      *(LAS float*)(lds + LDS_RSTD + lane * 4) = __builtin_amdgcn_rcpf(bfhi((unsigned)st0));
      *(LAS float*)(lds + LDS_MEAN + 256 + lane * 4) = bflo((unsigned)st1);
      *(LAS float*)(lds + LDS_RSTD + 256 + lane * 4) = __builtin_amdgcn_rcpf(bfhi((unsigned)st1));
    }
    wait_lgkm0();
    bar();
  }
  {
    csum(7);
    const float v0 = q == 0 ? cs[0][0] : q == 1 ? cs[1][0] : q == 2 ? cs[2][0] : cs[3][0];
    const float v1 = q == 0 ? cs[4][0] : q == 1 ? cs[5][0] : cs[6][0];
    *(LAS float*)(lds + LDS_CS + (hw * 11 * 16 + lane) * 4) = v0;
    if (q < 3) *(LAS float*)(lds + LDS_CS + ((hw * 11 + 4) * 16 + lane) * 4) = v1;
    wait_lgkm0();
    bar();
  }
#pragma unroll
  for (int n = 0; n < 4; ++n) cs[n] = z4;
#pragma unroll 1
  for (int h = 0; h < 2; ++h) {
#pragma unroll
    for (int j = 0; j < 2; ++j) {
      LAS char* ba = lds + ((h | j) == 0 ? 8 : 3 + 2 * h + j) * SLOT;
#pragma unroll
      for (int k = 0; k < 2; ++k)
#pragma unroll
        for (int n = 0; n < 4; ++n) cs[n] = MFMA(ones, ldstr(ba + ja[n] + k * 8192, 512), cs[n]);
    }
    if (h == 0) {
      wait_lgkm0();
      bar();
      if (hw == 0 && lane == 0) set_flag(ws, 0, c.tp * 16 + cp, tag);
      bar();
      if (hw == 1) {
#pragma unroll
        for (int i = 0; i < PMF; ++i) { touch(Cp, dump); Cp += 256; }
      }
    }
  }
  *(LAS float*)(lds + LDS_CS + ((hw * 11 + 7) * 16 + lane) * 4) = q == 0 ? cs[0][0] : q == 1 ? cs[1][0] : q == 2 ? cs[2][0] : cs[3][0];
  wait_lgkm0();
  if (lane == 0) *(volatile LAS unsigned*)(lds + LDS_TAG + 8 + 4 * hw) = 1u;
  const char* Wp = c.w2 + (c.ec * 128 + (c.ks & 1) * 64) * 2 + (size_t)((c.tp & 7) * 224 + hw * 112 + (lane < 56 ? lane : 0)) * 1024;
  const bool ew = blockIdx.x == 0 && hw == 1;
  const char* fp = ws + WS_FLAG + (unsigned)lane * FLAG_STRIDE;
  u64 f0 = 0, f1 = 0, f2 = 0, f3 = 0;
  auto run = [&](int s0, int s1) {
#pragma unroll 1
    for (int s = s0; s < s1; ++s) {
      if (PREFW && s == PREFW) { touch(Wp, dump); touch(Wp + 56 * 1024, dump); }
      if (hw == 1 && ((PM2 >> (s + 1)) & 1)) { touch(Cp, dump); Cp += 256; }
      bar();
      if (hw == 0 && lane == 0) {
        if (s == HFLAG) set_flag(ws, 1, c.tp * 16 + cp, tag);
        if (s == YFLAG) set_flag(ws, 2, (c.tp * 4 + c.ec) * 4 + c.ks, tag);
      }
    }
  };
  wait_lgkm0();
  run(-1, 10);
  if (ew) { f0 = ld8_async(fp); f1 = ld8_async(fp + 64 * FLAG_STRIDE); f2 = ld8_async(fp + 128 * FLAG_STRIDE); f3 = ld8_async(fp + 192 * FLAG_STRIDE); }
  run(10, 15);
  wait_vm<0>();
  asm volatile("" : "+v"(f0), "+v"(f1), "+v"(f2), "+v"(f3));
  const bool early = ew && __all(f0 == tag && f1 == tag && f2 == tag && f3 == tag);
  if (early && lane == 0) __hip_atomic_store((GAS u64*)(ws + WS_EPOCH), tag, __ATOMIC_RELAXED, __HIP_MEMORY_SCOPE_AGENT);
  run(15, YPOLL + 1);
  const char* yfp = ws + WS_FLAG + (unsigned)(2 * 256 + (c.tp * 4 + c.ec) * 4 + (lane & 3)) * FLAG_STRIDE;
  f0 = ld8_async(yfp);
  run(YPOLL + 1, YPOLL + 2);
  f1 = ld8_async(yfp);
  run(YPOLL + 2, YCHK);
  wait_vm<1>();
  asm volatile("" : "+v"(f0));
  if (!__all(f0 == tag)) {
    wait_vm<0>();
    asm volatile("" : "+v"(f1));
    if (!__all(f1 == tag)) poll(yfp, [&](u64 x) { return x == tag; });
  }
  run(YCHK, 29);
  wait_vm<0>();
  asm volatile("" : "+v"(f1));
  if (ew && !early) {
#pragma unroll 1
    for (int i = 0; i < 4; ++i) poll(fp + i * 64 * FLAG_STRIDE, [&](u64 x) { return x == tag; });
    if (lane == 0) __hip_atomic_store((GAS u64*)(ws + WS_EPOCH), tag, __ATOMIC_RELAXED, __HIP_MEMORY_SCOPE_AGENT);
  }
  wait_vm<0>();
  BT_END();
}

DEV void consumer2(const Ctx& c DBG_PARAM BT_PARAM) {
  const int w = c.wid, lane = fresh_lane();
  LAS char* lds = c.lds;
  const int q = lane >> 4, cc = lane & 15, qq = cc >> 2, pp = lane & 3;
  const unsigned wa0 = cc * 128 + ((q ^ (cc & 7)) * 16), wa1 = cc * 128 + (((4 + q) ^ (cc & 7)) * 16);
  const int wt = w >> 1, we = w & 1;
  unsigned wb[2];
#pragma unroll
  for (int n = 0; n < 2; ++n) wb[n] = 4 * SLOT + (4 * q + qq) * 128 + (((2 * (2 * we + n) + (pp >> 1)) ^ ((4 * q + qq) & 6)) * 16) + 8 * (pp & 1);
  const unsigned hb = wt * 2048 + lane * 16;
  bf16x8 hA[4][2], wA[4][2], hB[4][2], wB[4][2];
  wait_lgkm0();
  bar();
  STAMP(5);
#define PREAD(HN, WN, SO) if (i < 12) { if (i % 3 == 2) { HN[i / 3][0] = ldsb128(lds + (SO) + hb + (i / 3) * 4096); HN[i / 3][1] = ldsb128(lds + (SO) + hb + (i / 3) * 4096 + 1024); } \
                                        else WN[i / 3][i % 3] = ldstr(lds + (SO) + wb[i % 3] + (i / 3) * 4096, 2048); SCHED(); }
  {
    f32x4 acc[4][4];
#pragma unroll
    for (int m = 0; m < 4; ++m)
#pragma unroll
      for (int n = 0; n < 4; ++n) acc[m][n] = (f32x4){0, 0, 0, 0};
    const unsigned a0 = wt * 8192 + wa0, a1 = wt * 8192 + wa1, b0 = we * 4096 + lane * 16;
    bf16x8 fa0[4], fb0[4], fa1[4], fb1[4];
#pragma unroll
    for (int m = 0; m < 4; ++m) { fa0[m] = ldsb128(lds + a0 + m * 2048); fb0[m] = ldsb128(lds + b0 + 7 * SLOT + m * 1024); }
#define HALF(FA, FB, NA_, NB_, AOFF, BOFF) do { _Pragma("unroll") for (int i = 0; i < 16; ++i) { \
      acc[i >> 2][i & 3] = MFMA(FB[i & 3], FA[i >> 2], acc[i >> 2][i & 3]); SCHED(); \
      if (i < 8) { if (i == 0) NB_[0] = ldsb128(lds + (BOFF)); else if (i == 1) NA_[0] = ldsb128(lds + (AOFF)); \
        else if (i < 5) NB_[i - 1] = ldsb128(lds + (BOFF) + (i - 1) * 1024); else NA_[i - 4] = ldsb128(lds + (AOFF) + (i - 4) * 2048); SCHED(); } } } while (0)
    unsigned so = 0, sb = 7 * SLOT, bseq = 0x76548;
#pragma unroll 1
    for (int seg = 0; seg < 2; ++seg) {
      const int n = seg ? 15 - HFLAG : HFLAG;
#pragma unroll 1
      for (int s = 0; s < n; ++s) {
        HALF(fa0, fb0, fa1, fb1, so + a1, sb + b0 + 8192);
        wait_lgkm0();
        bar();
        sb = (bseq & 15) * SLOT; bseq = (bseq >> 4) | ((bseq & 15) << 16); so = (so + SLOT) & (4 * SLOT - 1);
        HALF(fa1, fb1, fa0, fb0, so + a0, sb + b0);
      }
      wait_vm<0>();
    }
    HALF(fa0, fb0, fa1, fb1, so + a1, sb + b0 + 8192);
    wait_lgkm0();
    bar();
#pragma unroll
    for (int i = 0; i < 16; ++i) {
      acc[i >> 2][i & 3] = MFMA(fb1[i & 3], fa1[i >> 2], acc[i >> 2][i & 3]); SCHED();
      PREAD(hA, wA, 0)
    }
    STAMP(6);
    if (w == c.ks) {
#pragma unroll
      for (int m = 0; m < 4; ++m)
#pragma unroll
        for (int n = 0; n < 4; ++n) *(LAS f32x4*)(lds + 8 * SLOT + (m * 4 + n) * 1024 + lane * 16) = acc[m][n];
    } else {
      const rsrc_t rY = mk_rsrc(c.ws + WS_YP + (size_t)(((c.tp * 4 + c.ec) * 4 + c.ks) * 4 + w) * 8192, 8192);
#pragma unroll
      for (int m = 0; m < 4; ++m)
#pragma unroll
        for (int j = 0; j < 2; ++j) {
          const f32x4 e = acc[m][2 * j], o = acc[m][2 * j + 1];
          __builtin_amdgcn_raw_buffer_store_b128((u32x4){pk_bf16(e[0], e[1]), pk_bf16(e[2], e[3]), pk_bf16(o[0], o[1]), pk_bf16(o[2], o[3])}, rY, lane * 16 + (m * 2 + j) * 1024, 0, 16);
        }
    }
  }
  STAMP(7);
  {
    f32x4 acc[2][2];
#pragma unroll
    for (int m = 0; m < 2; ++m) { acc[m][0] = (f32x4){0, 0, 0, 0}; acc[m][1] = (f32x4){0, 0, 0, 0}; }
    unsigned so = 0;
#define PSTEP(HC, WC, HN, WN) do { wait_lgkm0(); bar(); so = (so + SLOT) & (4 * SLOT - 1); SCHED(); \
      _Pragma("unroll") for (int i = 0; i < 16; ++i) { \
        acc[(i >> 1) & 1][i & 1] = MFMA(WC[i >> 2][i & 1], HC[i >> 2][(i >> 1) & 1], acc[(i >> 1) & 1][i & 1]); SCHED(); \
        PREAD(HN, WN, so) } } while (0)
    static_assert(YFLAG == 18 && YCHK == 25, "schedule");
#pragma unroll 1
    for (int seg = 0; seg < 2; ++seg) {
      const int n = seg ? 4 : 1;
#pragma unroll 1
      for (int s = 0; s < n; ++s) { PSTEP(hA, wA, hB, wB); PSTEP(hB, wB, hA, wA); }
      wait_vm<0>();
    }
    const rsrc_t rY = mk_rsrc(c.ws + WS_YP + (size_t)((c.tp * 4 + c.ec) * 16 + c.ks) * 8192, 4 * 32768 - 3 * 8192);
    u32x4 yp[3][2];
    const unsigned yv = lane * 16 + (4 * wt + we) * 1024;
#pragma unroll
    for (int i = 0; i < 3; ++i) yp[i][0] = __builtin_amdgcn_raw_buffer_load_b128(rY, yv, ((c.ks + 1 + i) & 3) * 32768, 16);
    SCHED();
    PSTEP(hA, wA, hB, wB);
#pragma unroll
    for (int i = 0; i < 3; ++i) yp[i][1] = __builtin_amdgcn_raw_buffer_load_b128(rY, yv + 2048, ((c.ks + 1 + i) & 3) * 32768, 16);
    SCHED();
    PSTEP(hB, wB, hA, wA);
    STAMP(8);
    const int tl = 32 * wt + 8 * qq + (cc & 3), el = 32 * we + 4 * q;
    f32x4 y[2][2], bv[2];
    u32x2 xr[2][2];
#pragma unroll
    for (int n = 0; n < 2; ++n) {
      bv[n] = *(const LAS f32x4*)(lds + LDS_B2 + (el + 16 * n) * 4);
#pragma unroll
      for (int m = 0; m < 2; ++m) {
        y[m][n] = *(const LAS f32x4*)(lds + 8 * SLOT + ((2 * wt + m) * 4 + 2 * we + n) * 1024 + lane * 16);
        xr[m][n] = *(const LAS u32x2*)(lds + LDS_XT + (tl + 4 * m) * 128 + (((el + 16 * n) * 2) ^ ((2 * qq + ((cc >> 1) & 1)) * 16)));
      }
    }
    PSTEP(hA, wA, hB, wB);
#pragma unroll
    for (int m = 0; m < 2; ++m)
#pragma unroll
      for (int i = 0; i < 3; ++i) { y[m][0] += unpk(yp[i][m].x, yp[i][m].y); y[m][1] += unpk(yp[i][m].z, yp[i][m].w); }
    char* op = c.out + (size_t)(c.tp * 128 + (c.ks >> 1) * 64 + tl) * 1024 + (c.ec * 128 + (c.ks & 1) * 64 + el) * 2;
    SCHED();
#pragma unroll
    for (int i = 0; i < 16; ++i) acc[i >> 3][(i >> 2) & 1] = MFMA(wB[i & 3][(i >> 2) & 1], hB[i & 3][i >> 3], acc[i >> 3][(i >> 2) & 1]);
    STAMP(9);
#pragma unroll
    for (int m = 0; m < 2; ++m)
#pragma unroll
      for (int n = 0; n < 2; ++n) {
        f32x4 o = unpk(xr[m][n].x, xr[m][n].y);
#pragma unroll
        for (int r = 0; r < 4; ++r) { const float z = acc[m][n][r] + bv[n][r]; o[r] += y[m][n][r] * (z * __builtin_amdgcn_rcpf(1.f + __expf(-z))); }
        *(u32x2*)(op + m * 4096 + n * 32) = (u32x2){pk_bf16(o[0], o[1]), pk_bf16(o[2], o[3])};
      }
    STAMP(10);
    STAMP_END(); BT_END();
  }
}

DEV void consumer(const Ctx& c) {
  DBG_DECL; BT_DECL;
  const int w = c.wid;
  LAS char* lds = c.lds;
  STAMP(0);
  unsigned ja[4], bx0, bx1, bw0, bw1;
  {
    const int lane = fresh_lane(), q = lane >> 4, cc = lane & 15, qq = cc >> 2, pp = lane & 3;
    if (PREFI) { touch(c.pc + w * 8192 + lane * 128, (unsigned)(size_t)lds + LDS_DUMP); if (256 + w * 64 < PREFI) touch(c.pc + 32768 + w * 8192 + lane * 128, (unsigned)(size_t)lds + LDS_DUMP); }
    const unsigned wa0 = cc * 128 + ((q ^ (cc & 7)) * 16), wa1 = cc * 128 + (((4 + q) ^ (cc & 7)) * 16);
#pragma unroll
    for (int n = 0; n < 4; ++n) ja[n] = opq(4 * SLOT + (8 * q + qq) * 128 + (((2 * n + (pp >> 1)) ^ ((qq & 2) | ((q & 1) << 2))) * 16) + 8 * (pp & 1));
    bx0 = opq(wa0 + w * 4096); bx1 = opq(wa1 + w * 4096); bw0 = opq(wa0 + 4 * SLOT); bw1 = opq(wa1 + 4 * SLOT);
  }
  bf16x8 xf[2][16];
  struct HTab { float mean[2], rstd[2]; f32x4 cs[4], bv[4]; unsigned vo; };
  auto h_tab = [&](HTab& t, int brow, int csrow) {
    const int lane = fresh_lane(), q = lane >> 4, cc = lane & 15;
#pragma unroll
    for (int u = 0; u < 2; ++u) { t.mean[u] = *(const LAS float*)(lds + LDS_MEAN + ((2 * w + u) * 16 + cc) * 4); t.rstd[u] = *(const LAS float*)(lds + LDS_RSTD + ((2 * w + u) * 16 + cc) * 4); }
#pragma unroll
    for (int n = 0; n < 4; ++n) {
      t.cs[n] = *(const LAS f32x4*)(lds + LDS_CS + ((csrow + n) * 16 + 4 * q) * 4) + *(const LAS f32x4*)(lds + LDS_CS + ((11 + csrow + n) * 16 + 4 * q) * 4);
      t.bv[n] = *(const LAS f32x4*)(lds + LDS_B1 + (brow * 64 + n * 16 + 4 * q) * 4);
    }
    t.vo = lane * 16;
  };
  auto h_half = [&](const HTab& t, const f32x4& a, int n, int u) {
    f32x4 z = (a - t.mean[u] * t.cs[n]) * t.rstd[u] + t.bv[n];
    z = __builtin_elementwise_max(z, (f32x4){0.f, 0.f, 0.f, 0.f});
    z = z * z;
    return (u32x2){pk_bf16(z[0], z[1]), pk_bf16(z[2], z[3])};
  };
  auto h_store = [&](const HTab& t, u32x2 d0, u32x2 d1, int P, int b, int u) {
    const rsrc_t rH = mk_rsrc(c.ws + WS_H + (size_t)((((2 * c.tp + (w >> 1)) * 56 + 2 * P) * 4 + 2 * (w & 1)) * 1024), 8192 - 2048);
    __builtin_amdgcn_raw_buffer_store_b128((u32x4){d0.x, d0.y, d1.x, d1.y}, rH, t.vo + u * 1024 + b * 4096, 0, 16);
  };
  constexpr int L2 = 8;
  bf16x8 g[64];
  auto rd2 = [&](auto t_) {
    constexpr int t = decltype(t_)::value, j = t / 16, k = (t / 4) & 3, n = t & 3;
    if constexpr (j == 0) g[t] = ldstr(lds + 4 * SLOT + ja[n] + k * 4096, 512); else g[t] = ldstr(lds + (j - 1) * SLOT + ja[n] + k * 4096, 512);
  };
  bar();
  STAMP(1);
  auto pass1 = [&](auto na_) {
    constexpr int NA = decltype(na_)::value;
    f32x4 acc[7][2];
#pragma unroll
    for (int n = 0; n < 7; ++n) { acc[n][0] = (f32x4){0, 0, 0, 0}; acc[n][1] = (f32x4){0, 0, 0, 0}; }
    bf16x8 fr[112];
    auto rd = [&](auto t_) {
      constexpr int t = decltype(t_)::value, kk = t / 7, n = t % 7, so = ((kk >> 1) & 3) * SLOT;
      if constexpr (n == 0) { xf[0][kk] = ldsb128(lds + ((kk & 1) ? bx1 : bx0) + so); xf[1][kk] = ldsb128(lds + ((kk & 1) ? bx1 : bx0) + so + 2048); }
      if constexpr (n < NA) fr[t] = ldsb128(lds + ((kk & 1) ? bw1 : bw0) + so + n * 2048);
      else fr[t] = ldstr(lds + ja[(n - 3) & 3] + so + 6144 + (kk & 1) * 4096, 512);
    };
    static_for<0, 7>(rd);
    SCHED();
    static_for<0, 112>([&](auto p_) {
      constexpr int p = decltype(p_)::value, kk = p / 7, n = p % 7;
      constexpr int kn = p / 14 + 1;
      if constexpr (p == 14 * kn - lead1(kn)) { wait_lgkm0(); bar(); SCHED(); }
      const bf16x8 f = fr[p];
      if constexpr (n < NA) acc[n][0] = MFMA(xf[0][kk], f, acc[n][0]); else acc[n][0] = MFMA(f, xf[0][kk], acc[n][0]);
      SCHED();
      static_for<p + 1, p + 15>([&](auto t_) {
        constexpr int t = decltype(t_)::value;
        if constexpr (t < 112 && t >= 7 && sched1(t) == p) { rd(t_); SCHED(); }
      });
      if constexpr (n < NA) acc[n][1] = MFMA(xf[1][kk], f, acc[n][1]); else acc[n][1] = MFMA(f, xf[1][kk], acc[n][1]);
      SCHED();
    });
    STAMP(2);
    const int lane = fresh_lane(), q = lane >> 4, cc = lane & 15;
    f32x4 mr[2], rr[2];
    const float sgn = c.E0 >= 32 ? -1.f : 1.f;
#pragma unroll
    for (int u = 0; u < 2; ++u) {
      mr[u] = *(const LAS f32x4*)(lds + LDS_MEAN + ((2 * w + u) * 16 + 4 * q) * 4);
      rr[u] = *(const LAS f32x4*)(lds + LDS_RSTD + ((2 * w + u) * 16 + 4 * q) * 4) * sgn;
    }
    float cs[NA];
#pragma unroll
    for (int n = 0; n < NA; ++n) cs[n] = *(const LAS float*)(lds + LDS_CS + (n * 16 + cc) * 4) + *(const LAS float*)(lds + LDS_CS + ((11 + n) * 16 + cc) * 4);
    const rsrc_t rA = mk_rsrc(c.ws + WS_ABT, 4u << 20);
#pragma unroll
    for (int n = 0; n < NA; ++n) {
      const f32x4 o0 = (acc[n][0] - mr[0] * cs[n]) * rr[0], o1 = (acc[n][1] - mr[1] * cs[n]) * rr[1];
      u32x4 d = {pk_bf16(o0[0], o0[1]), pk_bf16(o0[2], o0[3]), pk_bf16(o1[0], o1[1]), pk_bf16(o1[2], o1[3])};
      const int E = c.E0 + n;
      __builtin_amdgcn_raw_buffer_store_b128(d, rA, lane * 16 + (unsigned)((((E >> 3) * 64 + c.tp * 4 + w) * 8 + (E & 7)) * 1024), 0, 16);
    }
    SCHED();
    static_for<0, L2>(rd2);
    STAMP(3);
    f32x4 ac2[4][2];
#pragma unroll
    for (int n = 0; n < 4; ++n) { ac2[n][0] = (f32x4){0, 0, 0, 0}; ac2[n][1] = (f32x4){0, 0, 0, 0}; }
    HTab ht;
    u32x2 hd[2];
    SCHED();
    static_assert(RPOS >= 16 && RPOS + L2 <= 32 && FPOS < RPOS, "R must follow the last use of J0 and precede the first read of J2");
    static_for<0, 64>([&](auto p_) {
      constexpr int p = decltype(p_)::value, kk = p / 4, n = p & 3;
      if constexpr (p == FPOS) { wait_vm<0>(); bar(); SCHED(); }
      if constexpr (p == RPOS) { bar(); SCHED(); }
      ac2[n][0] = MFMA(g[p], xf[0][kk], ac2[n][0]);
      SCHED();
      if constexpr (p + L2 < 64) { rd2(IC<p + L2>{}); SCHED(); }
      if constexpr (NA == 3) {
        constexpr int e0 = EPOS + 6;
        if constexpr (p == EPOS) h_tab(ht, 0, 3);
        if constexpr (p >= e0 && p < e0 + 32 && (p - e0) % 4 == 0) {
          constexpr int i = (p - e0) / 4, b = i >> 2, u = (i >> 1) & 1, h = i & 1;
          hd[h] = h_half(ht, acc[3 + 2 * b + h][u], 2 * b + h, u);
          if constexpr (h == 1) h_store(ht, hd[0], hd[1], c.PA, b, u);
        }
      }
      ac2[n][1] = MFMA(g[p], xf[1][kk], ac2[n][1]);
      if constexpr (!(NA == 3 && p >= EPOS + 6 && p < EPOS + 38 && (p - EPOS - 6) % 4 < 2)) SCHED();
    });
    STAMP(11);
    while (*(volatile LAS u64*)(lds + LDS_TAG + 8) != 0x100000001ull) __builtin_amdgcn_s_sleep(1);
    asm volatile("" ::: "memory");
    h_tab(ht, 1, 7);
#pragma unroll
    for (int b = 0; b < 2; ++b)
#pragma unroll
      for (int u = 0; u < 2; ++u) h_store(ht, h_half(ht, ac2[2 * b][u], 2 * b, u), h_half(ht, ac2[2 * b + 1][u], 2 * b + 1, u), c.PB, b, u);
  };
  if (c.typeB) pass1(IC<7>{}); else pass1(IC<3>{});
  STAMP(4);
  consumer2(c DBG_ARG BT_ARG);
}

__global__ __launch_bounds__(512, 2) void fnet_mlp_fused(const char* x, const char* Cr, const char* Ci, const char* Wr, const char* Wi,
                                                         const char* w1, const char* b1, const char* w2, const char* b2, char* out, char* ws) {
  extern __shared__ __attribute__((aligned(16))) char shm[];
  Ctx c;
  { u64 pc; asm volatile("s_getpc_b64 %0" : "=s"(pc)); c.pc = (const char*)(pc & ~(u64)255); }
  c.x = x; c.Cr = Cr; c.Ci = Ci; c.Wr = Wr; c.Wi = Wi; c.w1 = w1; c.b1 = b1; c.w2 = w2; c.b2 = b2; c.out = out; c.ws = ws;
  c.lds = (LAS char*)shm;
  c.wid = __builtin_amdgcn_readfirstlane(threadIdx.x >> 6);
  const int b = blockIdx.x, xcd = b & 7, r = b >> 3;
  c.ks = xcd >> 1; c.tp = (xcd & 1) * 8 + (r >> 2); c.ec = r & 3;
  c.typeB = c.ec == 3;
  c.E0 = 16 * c.ks + 3 * c.ec; c.PA = 7 * c.ks + 2 * c.ec; c.PB = c.typeB ? c.PA : c.PA + 1;
  c.rot1 = (ROT & 1) ? (2 * c.ec + (r >> 2)) & 7 : 0; c.rot2 = (ROT & 2) ? (4 * c.ec + 2 * (r >> 2)) & 15 : 0;
  if (c.wid == 4) loaderA(c); else if (c.wid == 5) loaderB(c); else if (c.wid >= 6) helper(c); else consumer(c);
}

extern "C" void kernel_launch(void* const* d_in, const int* in_sizes, int n_in, void* d_out, int out_size, void* d_ws, size_t ws_size, hipStream_t stream) {
  (void)hipFuncSetAttribute((const void*)fnet_mlp_fused, hipFuncAttributeMaxDynamicSharedMemorySize, LDS_BYTES);
  fnet_mlp_fused<<<256, 512, LDS_BYTES, stream>>>((const char*)d_in[0], (const char*)d_in[1], (const char*)d_in[2], (const char*)d_in[3], (const char*)d_in[4],
                                                  (const char*)d_in[6], (const char*)d_in[7], (const char*)d_in[8], (const char*)d_in[9], (char*)d_out, (char*)d_ws);
}
```

```cpp
#include <hip/hip_runtime.h>
typedef unsigned short u16;
typedef unsigned long long u64;
typedef short bf16x8 __attribute__((ext_vector_type(8)));
typedef short s16x4 __attribute__((ext_vector_type(4)));
typedef float f32x4 __attribute__((ext_vector_type(4)));
typedef float f32x2 __attribute__((ext_vector_type(2)));
typedef unsigned u32x4 __attribute__((ext_vector_type(4)));
typedef unsigned u32x2 __attribute__((ext_vector_type(2)));
typedef __bf16 bf2 __attribute__((ext_vector_type(2)));
#define LAS __attribute__((address_space(3)))
#define GAS __attribute__((address_space(1)))
#define DEV __device__ __forceinline__

constexpr int S_ = 2048, D_ = 512, F_ = 1792, W1R = F_ * 2;
constexpr unsigned WS_EPOCH = 0, WS_FLAG = 4096, FLAG_STRIDE = 128, WS_ROWST = 256u << 10;
constexpr unsigned WS_ABT = 1u << 20, WS_H = 8u << 20, WS_YP = 16u << 20, WS_BYTES = 32u << 20;
constexpr int SLOT = 16384, NSLOT = 9;
constexpr int LDS_XT = NSLOT * SLOT;
constexpr int LDS_MEAN = LDS_XT + 8192, LDS_RSTD = LDS_MEAN + 512, LDS_CS = LDS_RSTD + 512;
constexpr int LDS_B1 = LDS_CS + 22 * 64, LDS_B2 = LDS_B1 + 512, LDS_TAG = LDS_B2 + 256;
constexpr int LDS_DUMP = LDS_TAG + 16, LDS_BYTES = LDS_DUMP + 256;
static_assert(LDS_BYTES <= 160 * 1024, "LDS");
#define LEADE 6
#define LOOKE 13
#define LEADL 4
#define LOOKL 9
#define ROT 1
#define PREFC 2
#define PM1 0x14
#define PMF 2
#define PM2 0x55
#define PREFS 2
#define PREFI 320
#define PREFW 8
#define PTLB 0
#define HPRIO 2
#define LPRIO 0
#define STLD 3
#define NINIT 3
#define HEARLY 0
#define EPOS 18
#define FPOS 8
#define RPOS 16
constexpr int HFLAG = 3, YFLAG = 18, YPOLL = 22, YCHK = 25;

template <int N> DEV void wait_vm() { asm volatile("s_waitcnt vmcnt(%0)" ::"n"(N) : "memory"); }
DEV void wait_lgkm0() { asm volatile("s_waitcnt lgkmcnt(0)" ::: "memory"); }
DEV void bar() { asm volatile("" ::: "memory"); __builtin_amdgcn_s_barrier(); asm volatile("" ::: "memory"); }
#define rawbar() __builtin_amdgcn_s_barrier()
#define BT_DECL
#define BT_END() do {} while (0)
#define BT_PARAM
#define BT_ARG
#define SCHED() __builtin_amdgcn_sched_barrier(0)
#define MFMA(a, b, c) __builtin_amdgcn_mfma_f32_16x16x32_bf16((a), (b), (c), 0, 0, 0)
DEV unsigned pk_bf16(float a, float b) { f32x2 f = {a, b}; return __builtin_bit_cast(unsigned, __builtin_convertvector(f, bf2)); }
DEV float rbf(float a) { return __uint_as_float(pk_bf16(a, 0.f) << 16); }
DEV float bflo(unsigned v) { return __uint_as_float(v << 16); }
DEV float bfhi(unsigned v) { return __uint_as_float(v & 0xffff0000u); }
DEV f32x4 unpk(unsigned a, unsigned b) { return (f32x4){bflo(a), bfhi(a), bflo(b), bfhi(b)}; }
DEV bf16x8 ldsb128(LAS char* p) { return *(const LAS bf16x8*)p; }
DEV bf16x8 ldstr(LAS char* p, int second) {
  s16x4 a = __builtin_amdgcn_ds_read_tr16_b64_v4i16((LAS s16x4*)p);
  s16x4 b = __builtin_amdgcn_ds_read_tr16_b64_v4i16((LAS s16x4*)(p + second));
  return __builtin_shufflevector(a, b, 0, 1, 2, 3, 4, 5, 6, 7);
}
DEV unsigned opq(unsigned v) { asm volatile("" : "+v"(v)); return v; }
DEV int fresh_lane() { int l; asm volatile("v_mbcnt_lo_u32_b32 %0, -1, 0\n\tv_mbcnt_hi_u32_b32 %0, -1, %0" : "=v"(l)); return l; }
typedef __amdgpu_buffer_rsrc_t rsrc_t;
DEV rsrc_t mk_rsrc(const void* p, unsigned bytes) { return __builtin_amdgcn_make_buffer_rsrc((void*)p, 0, bytes, 0x00020000); }
template <int AUX> DEV void dma(rsrc_t rs, unsigned voff, unsigned soff, LAS char* dst) { __builtin_amdgcn_raw_ptr_buffer_load_lds(rs, (LAS void*)dst, 16, voff, soff, 0, AUX); }
DEV u64 ld8_async(const char* p) { u64 v; asm volatile("global_load_dwordx2 %0, %1, off sc1" : "=v"(v) : "v"(p) : "memory"); return v; }
DEV void touch(const char* p, unsigned dump) {
  unsigned keep;
  asm volatile("s_mov_b32 %0, m0\n\ts_mov_b32 m0, %2\n\ts_nop 0\n\tglobal_load_lds_dword %1, off\n\ts_mov_b32 m0, %0" : "=&s"(keep) : "v"(p), "s"(dump) : "memory");
}
DEV void touch_sc1(const char* p, unsigned dump) {
  unsigned keep;
  asm volatile("s_mov_b32 %0, m0\n\ts_mov_b32 m0, %2\n\ts_nop 0\n\tglobal_load_lds_dword %1, off sc1\n\ts_mov_b32 m0, %0" : "=&s"(keep) : "v"(p), "s"(dump) : "memory");
}
template <int CTRL> DEV float dpp_add(float x) { return x + __builtin_bit_cast(float, __builtin_amdgcn_update_dpp(0, __builtin_bit_cast(int, x), CTRL, 0xf, 0xf, false)); }
DEV float sum16(float x) { x = dpp_add<0xB1>(x); x = dpp_add<0x4E>(x); x = dpp_add<0x141>(x); return dpp_add<0x140>(x); }

#define STAMP(i) do {} while (0)
#define STAMP_END() do {} while (0)
#define DBG_DECL
#define DBG_PARAM
#define DBG_ARG

template <int N> struct IC { static constexpr int value = N; };
template <int B, int E, class F> DEV void static_for(F&& f) { if constexpr (B < E) { f(IC<B>{}); static_for<B + 1, E>(f); } }
constexpr int lead1(int k) { return k >= 7 ? LEADL : LEADE; }
constexpr int sched1(int t) { const int k = t / 14, e0 = t - (k >= 7 ? LOOKL : LOOKE), e1 = 14 * k - lead1(k) + (t - 14 * k) / 2; return k == 0 ? (t - 7 > 0 ? (t - 7) / 2 : 0) : (e0 > e1 ? e0 : e1); }

struct Ctx {
  const char *x, *Cr, *Ci, *Wr, *Wi, *w1, *b1, *w2, *b2; char* out; char* ws; LAS char* lds; const char* pc;
  int wid, tp, ec, ks;
  int E0, PA, PB, rot1, rot2;
  bool typeB;
};

template <class P> DEV u64 poll(const char* p, P ok) {
  const u64 t0 = __builtin_amdgcn_s_memrealtime();
  u64 v;
  for (;;) {
    v = __hip_atomic_load((GAS u64*)p, __ATOMIC_RELAXED, __HIP_MEMORY_SCOPE_AGENT);
    if (__all(ok(v))) break;
    if (__builtin_amdgcn_s_memrealtime() - t0 > 20000000ull) break;
    __builtin_amdgcn_s_sleep(1);
  }
  return v;
}
DEV void set_flag(char* ws, int kind, int idx, u64 tag) {
  __hip_atomic_store((GAS u64*)(ws + WS_FLAG + (unsigned)(kind * 256 + idx) * FLAG_STRIDE), tag, __ATOMIC_RELAXED, __HIP_MEMORY_SCOPE_AGENT);
}
DEV u64 uniform64(u64 v) {
  const unsigned lo = __builtin_amdgcn_readfirstlane((unsigned)v), hi = __builtin_amdgcn_readfirstlane((unsigned)(v >> 32));
  return ((u64)hi << 32) | lo;
}
#define PROW(p) (32 * ((p) >> 2) + 16 * ((p) & 1) + 4 * (((p) >> 1) & 1))

DEV void loaderA(const Ctx& c) {
  DBG_DECL; BT_DECL;
  __builtin_amdgcn_s_setprio(LPRIO);
  const int lane = fresh_lane(), r8 = lane >> 3, p8 = lane & 7;
  LAS char* lds = c.lds;
  const unsigned rl = 8 * (r8 >> 2) + (r8 & 3), swz = (unsigned)((p8 ^ r8) * 16);
  const rsrc_t rX = mk_rsrc(c.x + (size_t)c.tp * 131072, 131072);
  const unsigned vX = rl * 1024 + swz;
  auto issueX = [&](int s) {
    const unsigned so = ((s + c.rot1) & 7) * 128;
#pragma unroll
    for (int p = 0; p < 16; ++p) dma<0>(rX, vX, so + PROW(p) * 1024, lds + (s & 3) * SLOT + p * 1024);
  };
  STAMP(8);
  issueX(0); STAMP(9); issueX(1);
  if (NINIT == 3) issueX(2);
  const rsrc_t rC = mk_rsrc((c.ks >> 1 ? c.Ci : c.Cr) + (size_t)c.tp * 524288 + (c.ks & 1) * 2048, 524288 - 2048);
  const rsrc_t rH = mk_rsrc(c.ws + WS_H + (size_t)(2 * c.tp + (c.ks >> 1)) * (56 * 4096), 56 * 4096);
  const unsigned vC = rl * 4096 + swz, vL = lane * 16;
  auto issueC = [&](int s) {
    const unsigned so = ((s + c.rot2) & 15) * 128;
#pragma unroll
    for (int p = 0; p < 16; ++p) dma<0>(rC, vC, so + PROW(p) * 4096, lds + (s & 3) * SLOT + p * 1024);
  };
  auto issueH = [&](int s) {
#pragma unroll
    for (int p = 0; p < 16; ++p) dma<16>(rH, vL, s * 16384 + p * 1024, lds + (s & 3) * SLOT + p * 1024);
  };
  STAMP(10);
  wait_vm<16 * (NINIT - 1)>();
  STAMP(11);
  bar();
  if (NINIT == 2) issueX(2);
#pragma unroll 1
  for (int s = 0; s < 5; ++s) { issueX(s + 3); wait_vm<32>(); bar(); }
#pragma unroll 1
  for (int s = 5; s < 7; ++s) { issueC(s - 5); wait_vm<32>(); bar(); }
  issueC(2);
  bar();
  issueC(3);
  {
    const unsigned so = (c.ks >> 1) * 65536 + (c.ec * 128 + (c.ks & 1) * 64) * 2, vo = r8 * 1024 + (p8 ^ ((r8 >> 1) & 1)) * 16;
#pragma unroll
    for (int p = 0; p < 8; ++p) dma<0>(rX, vo ^ ((p & 3) * 32), so + p * 8192, lds + LDS_XT + p * 1024);
  }
  const u64 tag = uniform64(*(const LAS u64*)(lds + LDS_TAG));
  bar();
  bar();
  wait_vm<56>();
  bar();
  {
    const rsrc_t rAB = mk_rsrc(c.ws + WS_ABT + (size_t)(((c.ks >> 1) * 4 + c.ec) * 64 + (c.ks & 1) * 32) * 8192, 262144);
    const unsigned so = ((3 + c.rot2) & 15) * 16384;
#pragma unroll
    for (int p = 0; p < 16; ++p) dma<16>(rAB, vL, so + p * 1024, lds + 5 * SLOT + p * 1024);
  }
  wait_vm<56>(); bar(); issueC(4);
  wait_vm<56>(); bar(); issueC(5);
  const char* hfp = c.ws + WS_FLAG + (unsigned)(1 * 256 + c.tp * 16 + (lane & 15)) * FLAG_STRIDE;
  u64 fv = 0;
#pragma unroll 1
  for (int s = 2; s < 12; ++s) { wait_vm<32>(); bar(); issueC(s + 4); if (s == 8) fv = ld8_async(hfp); }
  wait_vm<32>();
  asm volatile("" : "+v"(fv));
  if (!__all(fv == tag)) poll(hfp, [&](u64 x) { return x == tag; });
#pragma unroll 1
  for (int s = 12; s < 26; ++s) { wait_vm<32>(); bar(); issueH(s - 12); }
  wait_vm<32>(); bar();
  wait_vm<16>(); bar();
  __builtin_amdgcn_s_waitcnt(0x0F70);
  bar();
  STAMP_END(); BT_END();
}

DEV void loaderB(const Ctx& c) {
  DBG_DECL; BT_DECL;
  __builtin_amdgcn_s_setprio(LPRIO);
  const int lane = fresh_lane(), r8 = lane >> 3, p8 = lane & 7;
  LAS char* lds = c.lds;
  const rsrc_t rW = mk_rsrc((c.E0 >= 32 ? c.Wi : c.Wr) + (size_t)(c.E0 & 31) * 16384, 7 * 16384);
  const rsrc_t r1 = mk_rsrc(c.w1, D_ * W1R);
  const unsigned vW = r8 * 1024 + (p8 ^ r8) * 16;
  const unsigned vJ[2] = {(unsigned)(r8 * W1R + (p8 ^ (r8 & 2)) * 16), (unsigned)(r8 * W1R + (p8 ^ (r8 & 2) ^ 4) * 16)};
  auto issueB = [&](int s) {
    const int k = (s + c.rot1) & 7;
    LAS char* d = lds + (4 + (s & 3)) * SLOT;
#pragma unroll
    for (int p = 0; p < 6; ++p) dma<0>(rW, vW, k * 128 + p * 8192, d + p * 1024);
    if (c.typeB) {
#pragma unroll
      for (int p = 6; p < 14; ++p) dma<0>(rW, vW, k * 128 + p * 8192, d + p * 1024);
    } else {
      const unsigned so = k * (64 * W1R) + c.PA * 128;
#pragma unroll
      for (int p = 6; p < 14; ++p) dma<0>(r1, vJ[p & 1], so + (p - 6) * (8 * W1R), d + p * 1024);
    }
    const unsigned sj = (((c.rot1 + (s >> 2)) & 7) * 64 + 16 * (s & 3)) * W1R + c.PB * 128;
    dma<0>(r1, vJ[0], sj, lds + 8 * SLOT + s * 2048);
    dma<0>(r1, vJ[1], sj + 8 * W1R, lds + 8 * SLOT + s * 2048 + 1024);
  };
#pragma unroll 1
  for (int s = 0; s < NINIT; ++s) issueB(s);
  auto issueJ = [&](int j) {
#pragma unroll
    for (int h = 0; h < 2; ++h) {
      const unsigned so = ((2 * j + h + c.rot1) & 7) * (64 * W1R) + c.PB * 128;
#pragma unroll
      for (int p = 0; p < 8; ++p) dma<0>(r1, vJ[p & 1], so + p * (8 * W1R), lds + (3 + j) * SLOT + h * 8192 + p * 1024);
    }
  };
  const rsrc_t rAB = mk_rsrc(c.ws + WS_ABT + (size_t)(((c.ks >> 1) * 4 + c.ec) * 64 + (c.ks & 1) * 32) * 8192, 262144);
  const rsrc_t r2 = mk_rsrc(c.w2 + (c.ec * 128 + (c.ks & 1) * 64) * 2, F_ * 1024 - 1024 + 128);
  const unsigned vL = lane * 16, v2 = r8 * 1024 + (p8 ^ (r8 & 6)) * 16;
  auto issueAB = [&](int s, int slot) {
    const unsigned so = ((s + c.rot2) & 15) * 16384;
#pragma unroll
    for (int p = 0; p < 16; ++p) dma<16>(rAB, vL, so + p * 1024, lds + slot * SLOT + p * 1024);
  };
  auto issueW2 = [&](int s) {
#pragma unroll
    for (int p = 0; p < 16; ++p) dma<0>(r2, v2, s * 131072 + p * 8192, lds + (4 + (s & 3)) * SLOT + p * 1024);
  };
  wait_vm<16 * (NINIT - 1)>();
  bar();
  if (NINIT == 2) issueB(2);
#pragma unroll 1
  for (int s = 0; s < 5; ++s) { issueB(s + 3); wait_vm<32>(); bar(); }
#pragma unroll 1
  for (int s = 5; s < 8; ++s) { issueJ(s - 4); wait_vm<32>(); bar(); }
  const u64 tag = uniform64(*(const LAS u64*)(lds + LDS_TAG));
  bar();
  wait_vm<0>();
  bar();
  STAMP(1);
  {
    const int G2 = 2 * (c.ks >> 1) + (c.ec >> 1), m = lane & 3;
    const bool act = lane < 32 && ((c.ec & 1) ? (m >= 2) : (m <= 2));
    poll(c.ws + WS_FLAG + (unsigned)((8 * (c.ks & 1) + ((lane >> 2) & 7)) * 16 + 4 * G2 + m) * FLAG_STRIDE, [&](u64 x) { return !act || x == tag; });
  }
  STAMP(2);
  issueAB(0, 7); issueAB(1, 8);
  wait_vm<16>();
  bar();
  STAMP(3);
  issueAB(2, 4);
  wait_vm<16>(); bar(); issueAB(4, 6);
  wait_vm<16>(); bar(); issueAB(5, 7);
  unsigned bseq = 0x76548;
#pragma unroll 1
  for (int s = 2; s < 12; ++s) { wait_vm<32>(); bar(); issueAB(s + 4, bseq & 15); bseq = (bseq >> 4) | ((bseq & 15) << 16); }
#pragma unroll 1
  for (int s = 12; s < 26; ++s) { wait_vm<32>(); bar(); issueW2(s - 12); }
  wait_vm<32>(); bar();
  wait_vm<16>(); bar();
  __builtin_amdgcn_s_waitcnt(0x0F70);
  bar();
  STAMP_END(); BT_END();
}

DEV void helper(const Ctx& c) {
  BT_DECL;
  __builtin_amdgcn_s_setprio(HPRIO);
  const int hw = c.wid - 6, lane = fresh_lane();
  LAS char* lds = c.lds; char* ws = c.ws;
  const int q = lane >> 4, cc = lane & 15, qq = cc >> 2, pp = lane & 3, cp = c.ks * 4 + c.ec;
  const unsigned dump = (unsigned)(size_t)lds + LDS_DUMP;
  const u64 tagv = __hip_atomic_load((GAS u64*)(ws + WS_EPOCH), __ATOMIC_RELAXED, __HIP_MEMORY_SCOPE_AGENT);
  const int row = c.tp * 128 + cp * 8 + 4 * hw + q;
  u32x4 xv[4];
#pragma unroll
  for (int i = 0; i < 4; ++i) xv[i] = *(const u32x4*)(c.x + (size_t)row * 1024 + cc * 16 + i * 256);
  const u16 b1v = *(const u16*)(c.b1 + ((hw ? c.PB : c.PA) * 64 + lane) * 2);
  const u16 b2v = *(const u16*)(c.b2 + (c.ec * 128 + (c.ks & 1) * 64 + lane) * 2);
  u64 tag;
  {
    float s = 0.f;
#pragma unroll
    for (int i = 0; i < 16; ++i) s += bflo(xv[i >> 2][i & 3]) + bfhi(xv[i >> 2][i & 3]);
    const float m = sum16(s) * (1.f / 512.f);
    float z = 0.f;
#pragma unroll
    for (int i = 0; i < 16; ++i) { const float d0 = bflo(xv[i >> 2][i & 3]) - m, d1 = bfhi(xv[i >> 2][i & 3]) - m; z += d0 * d0; z += d1 * d1; }
    const float var = sum16(z) * (1.f / 512.f);
    const float sd = __builtin_amdgcn_sqrtf(rbf(rbf(var) + 1e-5f));
    tag = uniform64(tagv) + 1;
    *(LAS float*)(lds + LDS_B1 + (hw * 64 + lane) * 4) = bflo(b1v);
    *(LAS float*)(lds + LDS_B2 + lane * 4) = bflo(b2v);
    SCHED();
    if (cc == 0)
      __hip_atomic_store((GAS u64*)(ws + WS_ROWST + (size_t)row * 8), ((u64)(unsigned)tag << 32) | pk_bf16(m, sd), __ATOMIC_RELAXED, __HIP_MEMORY_SCOPE_AGENT);
    if (hw == 0 && lane == 0) { *(LAS u64*)(lds + LDS_TAG) = tag; *(LAS u64*)(lds + LDS_TAG + 8) = 0; }
  }
  const unsigned wa = cc * 128 + (((4 * hw + q) ^ (cc & 7)) * 16);
  unsigned ja[4];
#pragma unroll
  for (int n = 0; n < 4; ++n) ja[n] = (8 * q + qq) * 128 + (((2 * n + (pp >> 1)) ^ ((qq & 2) | ((q & 1) << 2))) * 16) + 8 * (pp & 1) + hw * 4096;
  const bf16x8 ones = {0x3f80, 0x3f80, 0x3f80, 0x3f80, 0x3f80, 0x3f80, 0x3f80, 0x3f80};
  const f32x4 z4 = {0.f, 0.f, 0.f, 0.f};
  f32x4 cs[7] = {z4, z4, z4, z4, z4, z4, z4};
  auto csum = [&](int s) {
    LAS char* ba = lds + (4 + (s & 3)) * SLOT;
    bf16x8 f[7];
#pragma unroll
    for (int n = 0; n < 3; ++n) f[n] = ldsb128(ba + n * 2048 + wa);
    if (c.typeB) {
#pragma unroll
      for (int n = 3; n < 7; ++n) f[n] = ldsb128(ba + n * 2048 + wa);
    } else {
#pragma unroll
      for (int n = 3; n < 7; ++n) f[n] = ldstr(ba + 6144 + ja[n - 3], 512);
    }
#pragma unroll
    for (int n = 0; n < 7; ++n) cs[n] = MFMA(ones, f[n], cs[n]);
  };
  bar();
  const char* stp = ws + WS_ROWST + (size_t)(c.tp * 128 + 32 * (lane >> 5) + 8 * qq + 4 * (q & 1) + (lane & 3)) * 8;
  u64 st0 = 0, st1 = 0;
  const char* Cp = (c.ks >> 1 ? c.Ci : c.Cr) + (size_t)(c.tp * 128 + c.ec * 32 + (lane & 31)) * 4096 + (c.ks & 1) * 2048 + (lane >> 5) * 128;
#pragma unroll 1
  for (int s = 0; s < 7; ++s) {
    if (hw == 0) { if (s == STLD) { st0 = ld8_async(stp); st1 = ld8_async(stp + 512); } }
    else if ((PM1 >> s) & 1) { touch(Cp, dump); Cp += 256; }
    csum(s);
    if (hw == 0 && s == 6) {
      wait_vm<0>();
      asm volatile("" : "+v"(st0), "+v"(st1));
      const unsigned t32 = (unsigned)tag;
      if (!__all((unsigned)(st0 >> 32) == t32)) st0 = poll(stp, [&](u64 x) { return (unsigned)(x >> 32) == t32; });
      if (!__all((unsigned)(st1 >> 32) == t32)) st1 = poll(stp + 512, [&](u64 x) { return (unsigned)(x >> 32) == t32; });
      *(LAS float*)(lds + LDS_MEAN + lane * 4) = bflo((unsigned)st0);
      *(LAS float*)(lds + LDS_RSTD + lane * 4) = __builtin_amdgcn_rcpf(bfhi((unsigned)st0));
      *(LAS float*)(lds + LDS_MEAN + 256 + lane * 4) = bflo((unsigned)st1);
      *(LAS float*)(lds + LDS_RSTD + 256 + lane * 4) = __builtin_amdgcn_rcpf(bfhi((unsigned)st1));
    }
    wait_lgkm0();
    bar();
  }
  {
    csum(7);
    const float v0 = q == 0 ? cs[0][0] : q == 1 ? cs[1][0] : q == 2 ? cs[2][0] : cs[3][0];
    const float v1 = q == 0 ? cs[4][0] : q == 1 ? cs[5][0] : cs[6][0];
    *(LAS float*)(lds + LDS_CS + (hw * 11 * 16 + lane) * 4) = v0;
    if (q < 3) *(LAS float*)(lds + LDS_CS + ((hw * 11 + 4) * 16 + lane) * 4) = v1;
    wait_lgkm0();
    bar();
  }
#pragma unroll
  for (int n = 0; n < 4; ++n) cs[n] = z4;
#pragma unroll 1
  for (int h = 0; h < 2; ++h) {
#pragma unroll
    for (int j = 0; j < 2; ++j) {
      LAS char* ba = lds + ((h | j) == 0 ? 8 : 3 + 2 * h + j) * SLOT;
#pragma unroll
      for (int k = 0; k < 2; ++k)
#pragma unroll
        for (int n = 0; n < 4; ++n) cs[n] = MFMA(ones, ldstr(ba + ja[n] + k * 8192, 512), cs[n]);
    }
    if (h == 0) {
      wait_lgkm0();
      bar();
      if (hw == 0 && lane == 0) set_flag(ws, 0, c.tp * 16 + cp, tag);
      bar();
      if (hw == 1) {
#pragma unroll
        for (int i = 0; i < PMF; ++i) { touch(Cp, dump); Cp += 256; }
      }
    }
  }
  *(LAS float*)(lds + LDS_CS + ((hw * 11 + 7) * 16 + lane) * 4) = q == 0 ? cs[0][0] : q == 1 ? cs[1][0] : q == 2 ? cs[2][0] : cs[3][0];
  wait_lgkm0();
  if (lane == 0) *(volatile LAS unsigned*)(lds + LDS_TAG + 8 + 4 * hw) = 1u;
  const char* Wp = c.w2 + (c.ec * 128 + (c.ks & 1) * 64) * 2 + (size_t)((c.tp & 7) * 224 + hw * 112 + (lane < 56 ? lane : 0)) * 1024;
  const bool ew = blockIdx.x == 0 && hw == 1;
  const char* fp = ws + WS_FLAG + (unsigned)lane * FLAG_STRIDE;
  u64 f0 = 0, f1 = 0, f2 = 0, f3 = 0;
  auto run = [&](int s0, int s1) {
#pragma unroll 1
    for (int s = s0; s < s1; ++s) {
      if (PREFW && s == PREFW) { touch(Wp, dump); touch(Wp + 56 * 1024, dump); }
      if (hw == 1 && ((PM2 >> (s + 1)) & 1)) { touch(Cp, dump); Cp += 256; }
      bar();
      if (hw == 0 && lane == 0) {
        if (s == HFLAG) set_flag(ws, 1, c.tp * 16 + cp, tag);
        if (s == YFLAG) set_flag(ws, 2, (c.tp * 4 + c.ec) * 4 + c.ks, tag);
      }
    }
  };
  wait_lgkm0();
  run(-1, 10);
  if (ew) { f0 = ld8_async(fp); f1 = ld8_async(fp + 64 * FLAG_STRIDE); f2 = ld8_async(fp + 128 * FLAG_STRIDE); f3 = ld8_async(fp + 192 * FLAG_STRIDE); }
  run(10, 15);
  wait_vm<0>();
  asm volatile("" : "+v"(f0), "+v"(f1), "+v"(f2), "+v"(f3));
  const bool early = ew && __all(f0 == tag && f1 == tag && f2 == tag && f3 == tag);
  if (early && lane == 0) __hip_atomic_store((GAS u64*)(ws + WS_EPOCH), tag, __ATOMIC_RELAXED, __HIP_MEMORY_SCOPE_AGENT);
  run(15, YPOLL + 1);
  const char* yfp = ws + WS_FLAG + (unsigned)(2 * 256 + (c.tp * 4 + c.ec) * 4 + (lane & 3)) * FLAG_STRIDE;
  f0 = ld8_async(yfp);
  run(YPOLL + 1, YPOLL + 2);
  f1 = ld8_async(yfp);
  run(YPOLL + 2, YCHK);
  wait_vm<1>();
  asm volatile("" : "+v"(f0));
  if (!__all(f0 == tag)) {
    wait_vm<0>();
    asm volatile("" : "+v"(f1));
    if (!__all(f1 == tag)) poll(yfp, [&](u64 x) { return x == tag; });
  }
  run(YCHK, 29);
  wait_vm<0>();
  asm volatile("" : "+v"(f1));
  if (ew && !early) {
#pragma unroll 1
    for (int i = 0; i < 4; ++i) poll(fp + i * 64 * FLAG_STRIDE, [&](u64 x) { return x == tag; });
    if (lane == 0) __hip_atomic_store((GAS u64*)(ws + WS_EPOCH), tag, __ATOMIC_RELAXED, __HIP_MEMORY_SCOPE_AGENT);
  }
  wait_vm<0>();
  BT_END();
}

DEV void consumer2(const Ctx& c DBG_PARAM BT_PARAM) {
  const int w = c.wid, lane = fresh_lane();
  LAS char* lds = c.lds;
  const int q = lane >> 4, cc = lane & 15, qq = cc >> 2, pp = lane & 3;
  const unsigned wa0 = cc * 128 + ((q ^ (cc & 7)) * 16), wa1 = cc * 128 + (((4 + q) ^ (cc & 7)) * 16);
  const int wt = w >> 1, we = w & 1;
  unsigned wb[2];
#pragma unroll
  for (int n = 0; n < 2; ++n) wb[n] = 4 * SLOT + (4 * q + qq) * 128 + (((2 * (2 * we + n) + (pp >> 1)) ^ ((4 * q + qq) & 6)) * 16) + 8 * (pp & 1);
  const unsigned hb = wt * 2048 + lane * 16;
  bf16x8 hA[4][2], wA[4][2], hB[4][2], wB[4][2];
  wait_lgkm0();
  bar();
  STAMP(5);
#define PREAD(HN, WN, SO) if (i < 12) { if (i % 3 == 2) { HN[i / 3][0] = ldsb128(lds + (SO) + hb + (i / 3) * 4096); HN[i / 3][1] = ldsb128(lds + (SO) + hb + (i / 3) * 4096 + 1024); } \
                                        else WN[i / 3][i % 3] = ldstr(lds + (SO) + wb[i % 3] + (i / 3) * 4096, 2048); SCHED(); }
  {
    f32x4 acc[4][4];
#pragma unroll
    for (int m = 0; m < 4; ++m)
#pragma unroll
      for (int n = 0; n < 4; ++n) acc[m][n] = (f32x4){0, 0, 0, 0};
    const unsigned a0 = wt * 8192 + wa0, a1 = wt * 8192 + wa1, b0 = we * 4096 + lane * 16;
    bf16x8 fa0[4], fb0[4], fa1[4], fb1[4];
#pragma unroll
    for (int m = 0; m < 4; ++m) { fa0[m] = ldsb128(lds + a0 + m * 2048); fb0[m] = ldsb128(lds + b0 + 7 * SLOT + m * 1024); }
#define HALF(FA, FB, NA_, NB_, AOFF, BOFF) do { _Pragma("unroll") for (int i = 0; i < 16; ++i) { \
      acc[i >> 2][i & 3] = MFMA(FB[i & 3], FA[i >> 2], acc[i >> 2][i & 3]); SCHED(); \
      if (i < 8) { if (i == 0) NB_[0] = ldsb128(lds + (BOFF)); else if (i == 1) NA_[0] = ldsb128(lds + (AOFF)); \
        else if (i < 5) NB_[i - 1] = ldsb128(lds + (BOFF) + (i - 1) * 1024); else NA_[i - 4] = ldsb128(lds + (AOFF) + (i - 4) * 2048); SCHED(); } } } while (0)
    unsigned so = 0, sb = 7 * SLOT, bseq = 0x76548;
#pragma unroll 1
    for (int seg = 0; seg < 2; ++seg) {
      const int n = seg ? 15 - HFLAG : HFLAG;
#pragma unroll 1
      for (int s = 0; s < n; ++s) {
        HALF(fa0, fb0, fa1, fb1, so + a1, sb + b0 + 8192);
        wait_lgkm0();
        bar();
        sb = (bseq & 15) * SLOT; bseq = (bseq >> 4) | ((bseq & 15) << 16); so = (so + SLOT) & (4 * SLOT - 1);
        HALF(fa1, fb1, fa0, fb0, so + a0, sb + b0);
      }
      wait_vm<0>();
    }
    HALF(fa0, fb0, fa1, fb1, so + a1, sb + b0 + 8192);
    wait_lgkm0();
    bar();
#pragma unroll
    for (int i = 0; i < 16; ++i) {
      acc[i >> 2][i & 3] = MFMA(fb1[i & 3], fa1[i >> 2], acc[i >> 2][i & 3]); SCHED();
      PREAD(hA, wA, 0)
    }
    STAMP(6);
    if (w == c.ks) {
#pragma unroll
      for (int m = 0; m < 4; ++m)
#pragma unroll
        for (int n = 0; n < 4; ++n) *(LAS f32x4*)(lds + 8 * SLOT + (m * 4 + n) * 1024 + lane * 16) = acc[m][n];
    } else {
      const rsrc_t rY = mk_rsrc(c.ws + WS_YP + (size_t)(((c.tp * 4 + c.ec) * 4 + c.ks) * 4 + w) * 8192, 8192);
#pragma unroll
      for (int m = 0; m < 4; ++m)
#pragma unroll
        for (int j = 0; j < 2; ++j) {
          const f32x4 e = acc[m][2 * j], o = acc[m][2 * j + 1];
          __builtin_amdgcn_raw_buffer_store_b128((u32x4){pk_bf16(e[0], e[1]), pk_bf16(e[2], e[3]), pk_bf16(o[0], o[1]), pk_bf16(o[2], o[3])}, rY, lane * 16 + (m * 2 + j) * 1024, 0, 16);
        }
    }
  }
  STAMP(7);
  {
    f32x4 acc[2][2];
#pragma unroll
    for (int m = 0; m < 2; ++m) { acc[m][0] = (f32x4){0, 0, 0, 0}; acc[m][1] = (f32x4){0, 0, 0, 0}; }
    unsigned so = 0;
#define PSTEP(HC, WC, HN, WN) do { wait_lgkm0(); bar(); so = (so + SLOT) & (4 * SLOT - 1); SCHED(); \
      _Pragma("unroll") for (int i = 0; i < 16; ++i) { \
        acc[(i >> 1) & 1][i & 1] = MFMA(WC[i >> 2][i & 1], HC[i >> 2][(i >> 1) & 1], acc[(i >> 1) & 1][i & 1]); SCHED(); \
        PREAD(HN, WN, so) } } while (0)
    static_assert(YFLAG == 18 && YCHK == 25, "schedule");
#pragma unroll 1
    for (int seg = 0; seg < 2; ++seg) {
      const int n = seg ? 4 : 1;
#pragma unroll 1
      for (int s = 0; s < n; ++s) { PSTEP(hA, wA, hB, wB); PSTEP(hB, wB, hA, wA); }
      wait_vm<0>();
    }
    const rsrc_t rY = mk_rsrc(c.ws + WS_YP + (size_t)((c.tp * 4 + c.ec) * 16 + c.ks) * 8192, 4 * 32768 - 3 * 8192);
    u32x4 yp[3][2];
    const unsigned yv = lane * 16 + (4 * wt + we) * 1024;
#pragma unroll
    for (int i = 0; i < 3; ++i) yp[i][0] = __builtin_amdgcn_raw_buffer_load_b128(rY, yv, ((c.ks + 1 + i) & 3) * 32768, 16);
    SCHED();
    PSTEP(hA, wA, hB, wB);
#pragma unroll
    for (int i = 0; i < 3; ++i) yp[i][1] = __builtin_amdgcn_raw_buffer_load_b128(rY, yv + 2048, ((c.ks + 1 + i) & 3) * 32768, 16);
    SCHED();
    PSTEP(hB, wB, hA, wA);
    STAMP(8);
    const int tl = 32 * wt + 8 * qq + (cc & 3), el = 32 * we + 4 * q;
    f32x4 y[2][2], bv[2];
    u32x2 xr[2][2];
#pragma unroll
    for (int n = 0; n < 2; ++n) {
      bv[n] = *(const LAS f32x4*)(lds + LDS_B2 + (el + 16 * n) * 4);
#pragma unroll
      for (int m = 0; m < 2; ++m) {
        y[m][n] = *(const LAS f32x4*)(lds + 8 * SLOT + ((2 * wt + m) * 4 + 2 * we + n) * 1024 + lane * 16);
        xr[m][n] = *(const LAS u32x2*)(lds + LDS_XT + (tl + 4 * m) * 128 + (((el + 16 * n) * 2) ^ ((2 * qq + ((cc >> 1) & 1)) * 16)));
      }
    }
    PSTEP(hA, wA, hB, wB);
#pragma unroll
    for (int m = 0; m < 2; ++m)
#pragma unroll
      for (int i = 0; i < 3; ++i) { y[m][0] += unpk(yp[i][m].x, yp[i][m].y); y[m][1] += unpk(yp[i][m].z, yp[i][m].w); }
    char* op = c.out + (size_t)(c.tp * 128 + (c.ks >> 1) * 64 + tl) * 1024 + (c.ec * 128 + (c.ks & 1) * 64 + el) * 2;
    SCHED();
#pragma unroll
    for (int i = 0; i < 16; ++i) acc[i >> 3][(i >> 2) & 1] = MFMA(wB[i & 3][(i >> 2) & 1], hB[i & 3][i >> 3], acc[i >> 3][(i >> 2) & 1]);
    STAMP(9);
#pragma unroll
    for (int m = 0; m < 2; ++m)
#pragma unroll
      for (int n = 0; n < 2; ++n) {
        f32x4 o = unpk(xr[m][n].x, xr[m][n].y);
#pragma unroll
        for (int r = 0; r < 4; ++r) { const float z = acc[m][n][r] + bv[n][r]; o[r] += y[m][n][r] * (z * __builtin_amdgcn_rcpf(1.f + __expf(-z))); }
        *(u32x2*)(op + m * 4096 + n * 32) = (u32x2){pk_bf16(o[0], o[1]), pk_bf16(o[2], o[3])};
      }
    STAMP(10);
    STAMP_END(); BT_END();
  }
}

DEV void consumer(const Ctx& c) {
  DBG_DECL; BT_DECL;
  const int w = c.wid;
  LAS char* lds = c.lds;
  STAMP(0);
  unsigned ja[4], bx0, bx1, bw0, bw1;
  {
    const int lane = fresh_lane(), q = lane >> 4, cc = lane & 15, qq = cc >> 2, pp = lane & 3;
    if (PREFI) { touch(c.pc + w * 8192 + lane * 128, (unsigned)(size_t)lds + LDS_DUMP); if (256 + w * 64 < PREFI) touch(c.pc + 32768 + w * 8192 + lane * 128, (unsigned)(size_t)lds + LDS_DUMP); }
    const unsigned wa0 = cc * 128 + ((q ^ (cc & 7)) * 16), wa1 = cc * 128 + (((4 + q) ^ (cc & 7)) * 16);
#pragma unroll
    for (int n = 0; n < 4; ++n) ja[n] = opq(4 * SLOT + (8 * q + qq) * 128 + (((2 * n + (pp >> 1)) ^ ((qq & 2) | ((q & 1) << 2))) * 16) + 8 * (pp & 1));
    bx0 = opq(wa0 + w * 4096); bx1 = opq(wa1 + w * 4096); bw0 = opq(wa0 + 4 * SLOT); bw1 = opq(wa1 + 4 * SLOT);
  }
  bf16x8 xf[2][16];
  struct HTab { float mean[2], rstd[2]; f32x4 cs[4], bv[4]; unsigned vo; };
  auto h_tab = [&](HTab& t, int brow, int csrow) {
    const int lane = fresh_lane(), q = lane >> 4, cc = lane & 15;
#pragma unroll
    for (int u = 0; u < 2; ++u) { t.mean[u] = *(const LAS float*)(lds + LDS_MEAN + ((2 * w + u) * 16 + cc) * 4); t.rstd[u] = *(const LAS float*)(lds + LDS_RSTD + ((2 * w + u) * 16 + cc) * 4); }
#pragma unroll
    for (int n = 0; n < 4; ++n) {
      t.cs[n] = *(const LAS f32x4*)(lds + LDS_CS + ((csrow + n) * 16 + 4 * q) * 4) + *(const LAS f32x4*)(lds + LDS_CS + ((11 + csrow + n) * 16 + 4 * q) * 4);
      t.bv[n] = *(const LAS f32x4*)(lds + LDS_B1 + (brow * 64 + n * 16 + 4 * q) * 4);
    }
    t.vo = lane * 16;
  };
  auto h_half = [&](const HTab& t, const f32x4& a, int n, int u) {
    f32x4 z = (a - t.mean[u] * t.cs[n]) * t.rstd[u] + t.bv[n];
    z = __builtin_elementwise_max(z, (f32x4){0.f, 0.f, 0.f, 0.f});
    z = z * z;
    return (u32x2){pk_bf16(z[0], z[1]), pk_bf16(z[2], z[3])};
  };
  auto h_store = [&](const HTab& t, u32x2 d0, u32x2 d1, int P, int b, int u) {
    const rsrc_t rH = mk_rsrc(c.ws + WS_H + (size_t)((((2 * c.tp + (w >> 1)) * 56 + 2 * P) * 4 + 2 * (w & 1)) * 1024), 8192 - 2048);
    __builtin_amdgcn_raw_buffer_store_b128((u32x4){d0.x, d0.y, d1.x, d1.y}, rH, t.vo + u * 1024 + b * 4096, 0, 16);
  };
  constexpr int L2 = 8;
  bf16x8 g[64];
  auto rd2 = [&](auto t_) {
    constexpr int t = decltype(t_)::value, j = t / 16, k = (t / 4) & 3, n = t & 3;
    if constexpr (j == 0) g[t] = ldstr(lds + 4 * SLOT + ja[n] + k * 4096, 512); else g[t] = ldstr(lds + (j - 1) * SLOT + ja[n] + k * 4096, 512);
  };
  bar();
  STAMP(1);
  auto pass1 = [&](auto na_) {
    constexpr int NA = decltype(na_)::value;
    f32x4 acc[7][2];
#pragma unroll
    for (int n = 0; n < 7; ++n) { acc[n][0] = (f32x4){0, 0, 0, 0}; acc[n][1] = (f32x4){0, 0, 0, 0}; }
    bf16x8 fr[112];
    auto rd = [&](auto t_) {
      constexpr int t = decltype(t_)::value, kk = t / 7, n = t % 7, so = ((kk >> 1) & 3) * SLOT;
      if constexpr (n == 0) { xf[0][kk] = ldsb128(lds + ((kk & 1) ? bx1 : bx0) + so); xf[1][kk] = ldsb128(lds + ((kk & 1) ? bx1 : bx0) + so + 2048); }
      if constexpr (n < NA) fr[t] = ldsb128(lds + ((kk & 1) ? bw1 : bw0) + so + n * 2048);
      else fr[t] = ldstr(lds + ja[(n - 3) & 3] + so + 6144 + (kk & 1) * 4096, 512);
    };
    static_for<0, 7>(rd);
    SCHED();
    static_for<0, 112>([&](auto p_) {
      constexpr int p = decltype(p_)::value, kk = p / 7, n = p % 7;
      constexpr int kn = p / 14 + 1;
      if constexpr (p == 14 * kn - lead1(kn)) { wait_lgkm0(); bar(); SCHED(); }
      const bf16x8 f = fr[p];
      if constexpr (n < NA) acc[n][0] = MFMA(xf[0][kk], f, acc[n][0]); else acc[n][0] = MFMA(f, xf[0][kk], acc[n][0]);
      SCHED();
      static_for<p + 1, p + 15>([&](auto t_) {
        constexpr int t = decltype(t_)::value;
        if constexpr (t < 112 && t >= 7 && sched1(t) == p) { rd(t_); SCHED(); }
      });
      if constexpr (n < NA) acc[n][1] = MFMA(xf[1][kk], f, acc[n][1]); else acc[n][1] = MFMA(f, xf[1][kk], acc[n][1]);
      SCHED();
    });
    STAMP(2);
    const int lane = fresh_lane(), q = lane >> 4, cc = lane & 15;
    f32x4 mr[2], rr[2];
    const float sgn = c.E0 >= 32 ? -1.f : 1.f;
#pragma unroll
    for (int u = 0; u < 2; ++u) {
      mr[u] = *(const LAS f32x4*)(lds + LDS_MEAN + ((2 * w + u) * 16 + 4 * q) * 4);
      rr[u] = *(const LAS f32x4*)(lds + LDS_RSTD + ((2 * w + u) * 16 + 4 * q) * 4) * sgn;
    }
    float cs[NA];
#pragma unroll
    for (int n = 0; n < NA; ++n) cs[n] = *(const LAS float*)(lds + LDS_CS + (n * 16 + cc) * 4) + *(const LAS float*)(lds + LDS_CS + ((11 + n) * 16 + cc) * 4);
    const rsrc_t rA = mk_rsrc(c.ws + WS_ABT, 4u << 20);
#pragma unroll
    for (int n = 0; n < NA; ++n) {
      const f32x4 o0 = (acc[n][0] - mr[0] * cs[n]) * rr[0], o1 = (acc[n][1] - mr[1] * cs[n]) * rr[1];
      u32x4 d = {pk_bf16(o0[0], o0[1]), pk_bf16(o0[2], o0[3]), pk_bf16(o1[0], o1[1]), pk_bf16(o1[2], o1[3])};
      const int E = c.E0 + n;
      __builtin_amdgcn_raw_buffer_store_b128(d, rA, lane * 16 + (unsigned)((((E >> 3) * 64 + c.tp * 4 + w) * 8 + (E & 7)) * 1024), 0, 16);
    }
    SCHED();
    static_for<0, L2>(rd2);
    STAMP(3);
    f32x4 ac2[4][2];
#pragma unroll
    for (int n = 0; n < 4; ++n) { ac2[n][0] = (f32x4){0, 0, 0, 0}; ac2[n][1] = (f32x4){0, 0, 0, 0}; }
    HTab ht;
    u32x2 hd[2];
    SCHED();
    static_assert(RPOS >= 16 && RPOS + L2 <= 32 && FPOS < RPOS, "R must follow the last use of J0 and precede the first read of J2");
    static_for<0, 64>([&](auto p_) {
      constexpr int p = decltype(p_)::value, kk = p / 4, n = p & 3;
      if constexpr (p == FPOS) { wait_vm<0>(); bar(); SCHED(); }
      if constexpr (p == RPOS) { bar(); SCHED(); }
      ac2[n][0] = MFMA(g[p], xf[0][kk], ac2[n][0]);
      SCHED();
      if constexpr (p + L2 < 64) { rd2(IC<p + L2>{}); SCHED(); }
      if constexpr (NA == 3) {
        constexpr int e0 = EPOS + 6;
        if constexpr (p == EPOS) h_tab(ht, 0, 3);
        if constexpr (p >= e0 && p < e0 + 32 && (p - e0) % 4 == 0) {
          constexpr int i = (p - e0) / 4, b = i >> 2, u = (i >> 1) & 1, h = i & 1;
          hd[h] = h_half(ht, acc[3 + 2 * b + h][u], 2 * b + h, u);
          if constexpr (h == 1) h_store(ht, hd[0], hd[1], c.PA, b, u);
        }
      }
      ac2[n][1] = MFMA(g[p], xf[1][kk], ac2[n][1]);
      if constexpr (!(NA == 3 && p >= EPOS + 6 && p < EPOS + 38 && (p - EPOS - 6) % 4 < 2)) SCHED();
    });
    STAMP(11);
    while (*(volatile LAS u64*)(lds + LDS_TAG + 8) != 0x100000001ull) __builtin_amdgcn_s_sleep(1);
    asm volatile("" ::: "memory");
    h_tab(ht, 1, 7);
#pragma unroll
    for (int b = 0; b < 2; ++b)
#pragma unroll
      for (int u = 0; u < 2; ++u) h_store(ht, h_half(ht, ac2[2 * b][u], 2 * b, u), h_half(ht, ac2[2 * b + 1][u], 2 * b + 1, u), c.PB, b, u);
  };
  if (c.typeB) pass1(IC<7>{}); else pass1(IC<3>{});
  STAMP(4);
  consumer2(c DBG_ARG BT_ARG);
}

__global__ __launch_bounds__(512, 2) void fnet_mlp_fused(const char* x, const char* Cr, const char* Ci, const char* Wr, const char* Wi,
                                                         const char* w1, const char* b1, const char* w2, const char* b2, char* out, char* ws) {
  extern __shared__ __attribute__((aligned(16))) char shm[];
  Ctx c;
  { u64 pc; asm volatile("s_getpc_b64 %0" : "=s"(pc)); c.pc = (const char*)(pc & ~(u64)255); }
  c.x = x; c.Cr = Cr; c.Ci = Ci; c.Wr = Wr; c.Wi = Wi; c.w1 = w1; c.b1 = b1; c.w2 = w2; c.b2 = b2; c.out = out; c.ws = ws;
  c.lds = (LAS char*)shm;
  c.wid = __builtin_amdgcn_readfirstlane(threadIdx.x >> 6);
  const int b = blockIdx.x, xcd = b & 7, r = b >> 3;
  c.ks = xcd >> 1; c.tp = (xcd & 1) * 8 + (r >> 2); c.ec = r & 3;
  c.typeB = c.ec == 3;
  c.E0 = 16 * c.ks + 3 * c.ec; c.PA = 7 * c.ks + 2 * c.ec; c.PB = c.typeB ? c.PA : c.PA + 1;
  c.rot1 = (ROT & 1) ? (2 * c.ec + (r >> 2)) & 7 : 0; c.rot2 = (ROT & 2) ? (4 * c.ec + 2 * (r >> 2)) & 15 : 0;
  if (c.wid == 4) loaderA(c); else if (c.wid == 5) loaderB(c); else if (c.wid >= 6) helper(c); else consumer(c);
}

extern "C" void kernel_launch(void* const* d_in, const int* in_sizes, int n_in, void* d_out, int out_size, void* d_ws, size_t ws_size, hipStream_t stream) {
  (void)hipFuncSetAttribute((const void*)fnet_mlp_fused, hipFuncAttributeMaxDynamicSharedMemorySize, LDS_BYTES);
  fnet_mlp_fused<<<256, 512, LDS_BYTES, stream>>>((const char*)d_in[0], (const char*)d_in[1], (const char*)d_in[2], (const char*)d_in[3], (const char*)d_in[4],
                                                  (const char*)d_in[6], (const char*)d_in[7], (const char*)d_in[8], (const char*)d_in[9], (char*)d_out, (char*)d_ws);
}
```

```cpp
#define MARK 1
#define PROBE 99
#include <hip/hip_runtime.h>
typedef unsigned short u16;
typedef unsigned long long u64;
typedef short bf16x8 __attribute__((ext_vector_type(8)));
typedef short s16x4 __attribute__((ext_vector_type(4)));
typedef float f32x4 __attribute__((ext_vector_type(4)));
typedef float f32x2 __attribute__((ext_vector_type(2)));
typedef unsigned u32x4 __attribute__((ext_vector_type(4)));
typedef unsigned u32x2 __attribute__((ext_vector_type(2)));
typedef __bf16 bf2 __attribute__((ext_vector_type(2)));
#define LAS __attribute__((address_space(3)))
#define GAS __attribute__((address_space(1)))
#define DEV __device__ __forceinline__

constexpr int S_ = 2048, D_ = 512, F_ = 1792, W1R = F_ * 2;
constexpr unsigned WS_EPOCH = 0, WS_FLAG = 4096, FLAG_STRIDE = 128, WS_ROWST = 256u << 10;
constexpr unsigned WS_ABT = 1u << 20, WS_H = 8u << 20, WS_YP = 16u << 20, WS_BYTES = 32u << 20;
constexpr int SLOT = 16384, NSLOT = 9;
constexpr int LDS_XT = NSLOT * SLOT;
constexpr int LDS_MEAN = LDS_XT + 8192, LDS_RSTD = LDS_MEAN + 512, LDS_CS = LDS_RSTD + 512;
constexpr int LDS_B1 = LDS_CS + 22 * 64, LDS_B2 = LDS_B1 + 512, LDS_TAG = LDS_B2 + 256;
constexpr int LDS_DUMP = LDS_TAG + 16, LDS_BYTES = LDS_DUMP + 256;
static_assert(LDS_BYTES <= 160 * 1024, "LDS");
#define LEADE 6
#define LOOKE 13
#define LEADL 4
#define LOOKL 9
#define ROT 1
#define PREFC 2
#define PM1 0x14
#define PMF 2
#define PM2 0x55
#define PREFS 2
#define PREFI 320
#define PREFW 8
#define PTLB 0
#define HPRIO 2
#define LPRIO 0
#define STLD 3
#define NINIT 3
#define HEARLY 0
#define EPOS 18
#define FPOS 8
#define RPOS 16
constexpr int HFLAG = 3, YFLAG = 18, YPOLL = 22, YCHK = 25;

template <int N> DEV void wait_vm() { asm volatile("s_waitcnt vmcnt(%0)" ::"n"(N) : "memory"); }
DEV void wait_lgkm0() { asm volatile("s_waitcnt lgkmcnt(0)" ::: "memory"); }
DEV void bar() { asm volatile("" ::: "memory"); __builtin_amdgcn_s_barrier(); asm volatile("" ::: "memory"); }
#define rawbar() __builtin_amdgcn_s_barrier()
#define BT_DECL
#define BT_END() do {} while (0)
#define BT_PARAM
#define BT_ARG
#define SCHED() __builtin_amdgcn_sched_barrier(0)
#define MFMA(a, b, c) __builtin_amdgcn_mfma_f32_16x16x32_bf16((a), (b), (c), 0, 0, 0)
DEV unsigned pk_bf16(float a, float b) { f32x2 f = {a, b}; return __builtin_bit_cast(unsigned, __builtin_convertvector(f, bf2)); }
DEV float rbf(float a) { return __uint_as_float(pk_bf16(a, 0.f) << 16); }
DEV float bflo(unsigned v) { return __uint_as_float(v << 16); }
DEV float bfhi(unsigned v) { return __uint_as_float(v & 0xffff0000u); }
DEV f32x4 unpk(unsigned a, unsigned b) { return (f32x4){bflo(a), bfhi(a), bflo(b), bfhi(b)}; }
DEV bf16x8 ldsb128(LAS char* p) { return *(const LAS bf16x8*)p; }
DEV bf16x8 ldstr(LAS char* p, int second) {
  s16x4 a = __builtin_amdgcn_ds_read_tr16_b64_v4i16((LAS s16x4*)p);
  s16x4 b = __builtin_amdgcn_ds_read_tr16_b64_v4i16((LAS s16x4*)(p + second));
  return __builtin_shufflevector(a, b, 0, 1, 2, 3, 4, 5, 6, 7);
}
DEV unsigned opq(unsigned v) { asm volatile("" : "+v"(v)); return v; }
DEV int fresh_lane() { int l; asm volatile("v_mbcnt_lo_u32_b32 %0, -1, 0\n\tv_mbcnt_hi_u32_b32 %0, -1, %0" : "=v"(l)); return l; }
typedef __amdgpu_buffer_rsrc_t rsrc_t;
DEV rsrc_t mk_rsrc(const void* p, unsigned bytes) { return __builtin_amdgcn_make_buffer_rsrc((void*)p, 0, bytes, 0x00020000); }
template <int AUX> DEV void dma(rsrc_t rs, unsigned voff, unsigned soff, LAS char* dst) { __builtin_amdgcn_raw_ptr_buffer_load_lds(rs, (LAS void*)dst, 16, voff, soff, 0, AUX); }
DEV u64 ld8_async(const char* p) { u64 v; asm volatile("global_load_dwordx2 %0, %1, off sc1" : "=v"(v) : "v"(p) : "memory"); return v; }
DEV void touch(const char* p, unsigned dump) {
  unsigned keep;
  asm volatile("s_mov_b32 %0, m0\n\ts_mov_b32 m0, %2\n\ts_nop 0\n\tglobal_load_lds_dword %1, off\n\ts_mov_b32 m0, %0" : "=&s"(keep) : "v"(p), "s"(dump) : "memory");
}
DEV void touch_sc1(const char* p, unsigned dump) {
  unsigned keep;
  asm volatile("s_mov_b32 %0, m0\n\ts_mov_b32 m0, %2\n\ts_nop 0\n\tglobal_load_lds_dword %1, off sc1\n\ts_mov_b32 m0, %0" : "=&s"(keep) : "v"(p), "s"(dump) : "memory");
}
template <int CTRL> DEV float dpp_add(float x) { return x + __builtin_bit_cast(float, __builtin_amdgcn_update_dpp(0, __builtin_bit_cast(int, x), CTRL, 0xf, 0xf, false)); }
DEV float sum16(float x) { x = dpp_add<0xB1>(x); x = dpp_add<0x4E>(x); x = dpp_add<0x141>(x); return dpp_add<0x140>(x); }

#define STAMP(i) do { if (c.wid < 4) { if ((i) == PROBE) st_v = __builtin_amdgcn_s_memrealtime(); \
    if ((i) == 10 && blockIdx.x == 77 && c.wid == 0) { const u64 e = (PROBE == 99 ? __hip_atomic_load((GAS u64*)(c.ws + 2048), __ATOMIC_RELAXED, __HIP_MEMORY_SCOPE_AGENT) : st_v) + 10000; while (__builtin_amdgcn_s_memrealtime() < e) __builtin_amdgcn_s_sleep(1); } } } while (0)
#define STAMP_END() do {} while (0)
#define DBG_DECL u64 st_v = 0
#define DBG_PARAM , u64& st_v
#define DBG_ARG , st_v

template <int N> struct IC { static constexpr int value = N; };
template <int B, int E, class F> DEV void static_for(F&& f) { if constexpr (B < E) { f(IC<B>{}); static_for<B + 1, E>(f); } }
constexpr int lead1(int k) { return k >= 7 ? LEADL : LEADE; }
constexpr int sched1(int t) { const int k = t / 14, e0 = t - (k >= 7 ? LOOKL : LOOKE), e1 = 14 * k - lead1(k) + (t - 14 * k) / 2; return k == 0 ? (t - 7 > 0 ? (t - 7) / 2 : 0) : (e0 > e1 ? e0 : e1); }

struct Ctx {
  const char *x, *Cr, *Ci, *Wr, *Wi, *w1, *b1, *w2, *b2; char* out; char* ws; LAS char* lds; const char* pc;
  int wid, tp, ec, ks;
  int E0, PA, PB, rot1, rot2;
  bool typeB;
};

template <class P> DEV u64 poll(const char* p, P ok) {
  const u64 t0 = __builtin_amdgcn_s_memrealtime();
  u64 v;
  for (;;) {
    v = __hip_atomic_load((GAS u64*)p, __ATOMIC_RELAXED, __HIP_MEMORY_SCOPE_AGENT);
    if (__all(ok(v))) break;
    if (__builtin_amdgcn_s_memrealtime() - t0 > 20000000ull) break;
    __builtin_amdgcn_s_sleep(1);
  }
  return v;
}
DEV void set_flag(char* ws, int kind, int idx, u64 tag) {
  __hip_atomic_store((GAS u64*)(ws + WS_FLAG + (unsigned)(kind * 256 + idx) * FLAG_STRIDE), tag, __ATOMIC_RELAXED, __HIP_MEMORY_SCOPE_AGENT);
}
DEV u64 uniform64(u64 v) {
  const unsigned lo = __builtin_amdgcn_readfirstlane((unsigned)v), hi = __builtin_amdgcn_readfirstlane((unsigned)(v >> 32));
  return ((u64)hi << 32) | lo;
}
#define PROW(p) (32 * ((p) >> 2) + 16 * ((p) & 1) + 4 * (((p) >> 1) & 1))

DEV void loaderA(const Ctx& c) {
  DBG_DECL; BT_DECL;
  __builtin_amdgcn_s_setprio(LPRIO);
  const int lane = fresh_lane(), r8 = lane >> 3, p8 = lane & 7;
  LAS char* lds = c.lds;
  const unsigned rl = 8 * (r8 >> 2) + (r8 & 3), swz = (unsigned)((p8 ^ r8) * 16);
  const rsrc_t rX = mk_rsrc(c.x + (size_t)c.tp * 131072, 131072);
  const unsigned vX = rl * 1024 + swz;
  auto issueX = [&](int s) {
    const unsigned so = ((s + c.rot1) & 7) * 128;
#pragma unroll
    for (int p = 0; p < 16; ++p) dma<0>(rX, vX, so + PROW(p) * 1024, lds + (s & 3) * SLOT + p * 1024);
  };
  STAMP(8);
  issueX(0); STAMP(9); issueX(1);
  if (NINIT == 3) issueX(2);
  const rsrc_t rC = mk_rsrc((c.ks >> 1 ? c.Ci : c.Cr) + (size_t)c.tp * 524288 + (c.ks & 1) * 2048, 524288 - 2048);
  const rsrc_t rH = mk_rsrc(c.ws + WS_H + (size_t)(2 * c.tp + (c.ks >> 1)) * (56 * 4096), 56 * 4096);
  const unsigned vC = rl * 4096 + swz, vL = lane * 16;
  auto issueC = [&](int s) {
    const unsigned so = ((s + c.rot2) & 15) * 128;
#pragma unroll
    for (int p = 0; p < 16; ++p) dma<0>(rC, vC, so + PROW(p) * 4096, lds + (s & 3) * SLOT + p * 1024);
  };
  auto issueH = [&](int s) {
#pragma unroll
    for (int p = 0; p < 16; ++p) dma<16>(rH, vL, s * 16384 + p * 1024, lds + (s & 3) * SLOT + p * 1024);
  };
  STAMP(10);
  wait_vm<16 * (NINIT - 1)>();
  STAMP(11);
  bar();
  if (NINIT == 2) issueX(2);
#pragma unroll 1
  for (int s = 0; s < 5; ++s) { issueX(s + 3); wait_vm<32>(); bar(); }
#pragma unroll 1
  for (int s = 5; s < 7; ++s) { issueC(s - 5); wait_vm<32>(); bar(); }
  issueC(2);
  bar();
  issueC(3);
  {
    const unsigned so = (c.ks >> 1) * 65536 + (c.ec * 128 + (c.ks & 1) * 64) * 2, vo = r8 * 1024 + (p8 ^ ((r8 >> 1) & 1)) * 16;
#pragma unroll
    for (int p = 0; p < 8; ++p) dma<0>(rX, vo ^ ((p & 3) * 32), so + p * 8192, lds + LDS_XT + p * 1024);
  }
  const u64 tag = uniform64(*(const LAS u64*)(lds + LDS_TAG));
  bar();
  bar();
  wait_vm<56>();
  bar();
  {
    const rsrc_t rAB = mk_rsrc(c.ws + WS_ABT + (size_t)(((c.ks >> 1) * 4 + c.ec) * 64 + (c.ks & 1) * 32) * 8192, 262144);
    const unsigned so = ((3 + c.rot2) & 15) * 16384;
#pragma unroll
    for (int p = 0; p < 16; ++p) dma<16>(rAB, vL, so + p * 1024, lds + 5 * SLOT + p * 1024);
  }
  wait_vm<56>(); bar(); issueC(4);
  wait_vm<56>(); bar(); issueC(5);
  const char* hfp = c.ws + WS_FLAG + (unsigned)(1 * 256 + c.tp * 16 + (lane & 15)) * FLAG_STRIDE;
  u64 fv = 0;
#pragma unroll 1
  for (int s = 2; s < 12; ++s) { wait_vm<32>(); bar(); issueC(s + 4); if (s == 8) fv = ld8_async(hfp); }
  wait_vm<32>();
  asm volatile("" : "+v"(fv));
  if (!__all(fv == tag)) poll(hfp, [&](u64 x) { return x == tag; });
#pragma unroll 1
  for (int s = 12; s < 26; ++s) { wait_vm<32>(); bar(); issueH(s - 12); }
  wait_vm<32>(); bar();
  wait_vm<16>(); bar();
  __builtin_amdgcn_s_waitcnt(0x0F70);
  bar();
  STAMP_END(); BT_END();
}

DEV void loaderB(const Ctx& c) {
  DBG_DECL; BT_DECL;
  __builtin_amdgcn_s_setprio(LPRIO);
  const int lane = fresh_lane(), r8 = lane >> 3, p8 = lane & 7;
  LAS char* lds = c.lds;
  const rsrc_t rW = mk_rsrc((c.E0 >= 32 ? c.Wi : c.Wr) + (size_t)(c.E0 & 31) * 16384, 7 * 16384);
  const rsrc_t r1 = mk_rsrc(c.w1, D_ * W1R);
  const unsigned vW = r8 * 1024 + (p8 ^ r8) * 16;
  const unsigned vJ[2] = {(unsigned)(r8 * W1R + (p8 ^ (r8 & 2)) * 16), (unsigned)(r8 * W1R + (p8 ^ (r8 & 2) ^ 4) * 16)};
  auto issueB = [&](int s) {
    const int k = (s + c.rot1) & 7;
    LAS char* d = lds + (4 + (s & 3)) * SLOT;
#pragma unroll
    for (int p = 0; p < 6; ++p) dma<0>(rW, vW, k * 128 + p * 8192, d + p * 1024);
    if (c.typeB) {
#pragma unroll
      for (int p = 6; p < 14; ++p) dma<0>(rW, vW, k * 128 + p * 8192, d + p * 1024);
    } else {
      const unsigned so = k * (64 * W1R) + c.PA * 128;
#pragma unroll
      for (int p = 6; p < 14; ++p) dma<0>(r1, vJ[p & 1], so + (p - 6) * (8 * W1R), d + p * 1024);
    }
    const unsigned sj = (((c.rot1 + (s >> 2)) & 7) * 64 + 16 * (s & 3)) * W1R + c.PB * 128;
    dma<0>(r1, vJ[0], sj, lds + 8 * SLOT + s * 2048);
    dma<0>(r1, vJ[1], sj + 8 * W1R, lds + 8 * SLOT + s * 2048 + 1024);
  };
#pragma unroll 1
  for (int s = 0; s < NINIT; ++s) issueB(s);
  auto issueJ = [&](int j) {
#pragma unroll
    for (int h = 0; h < 2; ++h) {
      const unsigned so = ((2 * j + h + c.rot1) & 7) * (64 * W1R) + c.PB * 128;
#pragma unroll
      for (int p = 0; p < 8; ++p) dma<0>(r1, vJ[p & 1], so + p * (8 * W1R), lds + (3 + j) * SLOT + h * 8192 + p * 1024);
    }
  };
  const rsrc_t rAB = mk_rsrc(c.ws + WS_ABT + (size_t)(((c.ks >> 1) * 4 + c.ec) * 64 + (c.ks & 1) * 32) * 8192, 262144);
  const rsrc_t r2 = mk_rsrc(c.w2 + (c.ec * 128 + (c.ks & 1) * 64) * 2, F_ * 1024 - 1024 + 128);
  const unsigned vL = lane * 16, v2 = r8 * 1024 + (p8 ^ (r8 & 6)) * 16;
  auto issueAB = [&](int s, int slot) {
    const unsigned so = ((s + c.rot2) & 15) * 16384;
#pragma unroll
    for (int p = 0; p < 16; ++p) dma<16>(rAB, vL, so + p * 1024, lds + slot * SLOT + p * 1024);
  };
  auto issueW2 = [&](int s) {
#pragma unroll
    for (int p = 0; p < 16; ++p) dma<0>(r2, v2, s * 131072 + p * 8192, lds + (4 + (s & 3)) * SLOT + p * 1024);
  };
  wait_vm<16 * (NINIT - 1)>();
  bar();
  if (NINIT == 2) issueB(2);
#pragma unroll 1
  for (int s = 0; s < 5; ++s) { issueB(s + 3); wait_vm<32>(); bar(); }
#pragma unroll 1
  for (int s = 5; s < 8; ++s) { issueJ(s - 4); wait_vm<32>(); bar(); }
  const u64 tag = uniform64(*(const LAS u64*)(lds + LDS_TAG));
  bar();
  wait_vm<0>();
  bar();
  STAMP(1);
  {
    const int G2 = 2 * (c.ks >> 1) + (c.ec >> 1), m = lane & 3;
    const bool act = lane < 32 && ((c.ec & 1) ? (m >= 2) : (m <= 2));
    poll(c.ws + WS_FLAG + (unsigned)((8 * (c.ks & 1) + ((lane >> 2) & 7)) * 16 + 4 * G2 + m) * FLAG_STRIDE, [&](u64 x) { return !act || x == tag; });
  }
  STAMP(2);
  issueAB(0, 7); issueAB(1, 8);
  wait_vm<16>();
  bar();
  STAMP(3);
  issueAB(2, 4);
  wait_vm<16>(); bar(); issueAB(4, 6);
  wait_vm<16>(); bar(); issueAB(5, 7);
  unsigned bseq = 0x76548;
#pragma unroll 1
  for (int s = 2; s < 12; ++s) { wait_vm<32>(); bar(); issueAB(s + 4, bseq & 15); bseq = (bseq >> 4) | ((bseq & 15) << 16); }
#pragma unroll 1
  for (int s = 12; s < 26; ++s) { wait_vm<32>(); bar(); issueW2(s - 12); }
  wait_vm<32>(); bar();
  wait_vm<16>(); bar();
  __builtin_amdgcn_s_waitcnt(0x0F70);
  bar();
  STAMP_END(); BT_END();
}

DEV void helper(const Ctx& c) {
  BT_DECL;
  __builtin_amdgcn_s_setprio(HPRIO);
  const int hw = c.wid - 6, lane = fresh_lane();
  LAS char* lds = c.lds; char* ws = c.ws;
  const int q = lane >> 4, cc = lane & 15, qq = cc >> 2, pp = lane & 3, cp = c.ks * 4 + c.ec;
  const unsigned dump = (unsigned)(size_t)lds + LDS_DUMP;
  const u64 tagv = __hip_atomic_load((GAS u64*)(ws + WS_EPOCH), __ATOMIC_RELAXED, __HIP_MEMORY_SCOPE_AGENT);
  const int row = c.tp * 128 + cp * 8 + 4 * hw + q;
  u32x4 xv[4];
#pragma unroll
  for (int i = 0; i < 4; ++i) xv[i] = *(const u32x4*)(c.x + (size_t)row * 1024 + cc * 16 + i * 256);
  const u16 b1v = *(const u16*)(c.b1 + ((hw ? c.PB : c.PA) * 64 + lane) * 2);
  const u16 b2v = *(const u16*)(c.b2 + (c.ec * 128 + (c.ks & 1) * 64 + lane) * 2);
  u64 tag;
  {
    float s = 0.f;
#pragma unroll
    for (int i = 0; i < 16; ++i) s += bflo(xv[i >> 2][i & 3]) + bfhi(xv[i >> 2][i & 3]);
    const float m = sum16(s) * (1.f / 512.f);
    float z = 0.f;
#pragma unroll
    for (int i = 0; i < 16; ++i) { const float d0 = bflo(xv[i >> 2][i & 3]) - m, d1 = bfhi(xv[i >> 2][i & 3]) - m; z += d0 * d0; z += d1 * d1; }
    const float var = sum16(z) * (1.f / 512.f);
    const float sd = __builtin_amdgcn_sqrtf(rbf(rbf(var) + 1e-5f));
    tag = uniform64(tagv) + 1;
    *(LAS float*)(lds + LDS_B1 + (hw * 64 + lane) * 4) = bflo(b1v);
    *(LAS float*)(lds + LDS_B2 + lane * 4) = bflo(b2v);
    SCHED();
    if (cc == 0)
      __hip_atomic_store((GAS u64*)(ws + WS_ROWST + (size_t)row * 8), ((u64)(unsigned)tag << 32) | pk_bf16(m, sd), __ATOMIC_RELAXED, __HIP_MEMORY_SCOPE_AGENT);
    if (hw == 0 && lane == 0) { *(LAS u64*)(lds + LDS_TAG) = tag; *(LAS u64*)(lds + LDS_TAG + 8) = 0; }
  }
  const unsigned wa = cc * 128 + (((4 * hw + q) ^ (cc & 7)) * 16);
  unsigned ja[4];
#pragma unroll
  for (int n = 0; n < 4; ++n) ja[n] = (8 * q + qq) * 128 + (((2 * n + (pp >> 1)) ^ ((qq & 2) | ((q & 1) << 2))) * 16) + 8 * (pp & 1) + hw * 4096;
  const bf16x8 ones = {0x3f80, 0x3f80, 0x3f80, 0x3f80, 0x3f80, 0x3f80, 0x3f80, 0x3f80};
  const f32x4 z4 = {0.f, 0.f, 0.f, 0.f};
  f32x4 cs[7] = {z4, z4, z4, z4, z4, z4, z4};
  auto csum = [&](int s) {
    LAS char* ba = lds + (4 + (s & 3)) * SLOT;
    bf16x8 f[7];
#pragma unroll
    for (int n = 0; n < 3; ++n) f[n] = ldsb128(ba + n * 2048 + wa);
    if (c.typeB) {
#pragma unroll
      for (int n = 3; n < 7; ++n) f[n] = ldsb128(ba + n * 2048 + wa);
    } else {
#pragma unroll
      for (int n = 3; n < 7; ++n) f[n] = ldstr(ba + 6144 + ja[n - 3], 512);
    }
#pragma unroll
    for (int n = 0; n < 7; ++n) cs[n] = MFMA(ones, f[n], cs[n]);
  };
  bar();
  const char* stp = ws + WS_ROWST + (size_t)(c.tp * 128 + 32 * (lane >> 5) + 8 * qq + 4 * (q & 1) + (lane & 3)) * 8;
  u64 st0 = 0, st1 = 0;
  const char* Cp = (c.ks >> 1 ? c.Ci : c.Cr) + (size_t)(c.tp * 128 + c.ec * 32 + (lane & 31)) * 4096 + (c.ks & 1) * 2048 + (lane >> 5) * 128;
#pragma unroll 1
  for (int s = 0; s < 7; ++s) {
    if (hw == 0) { if (s == STLD) { st0 = ld8_async(stp); st1 = ld8_async(stp + 512); } }
    else if ((PM1 >> s) & 1) { touch(Cp, dump); Cp += 256; }
    csum(s);
    if (hw == 0 && s == 6) {
      wait_vm<0>();
      asm volatile("" : "+v"(st0), "+v"(st1));
      const unsigned t32 = (unsigned)tag;
      if (!__all((unsigned)(st0 >> 32) == t32)) st0 = poll(stp, [&](u64 x) { return (unsigned)(x >> 32) == t32; });
      if (!__all((unsigned)(st1 >> 32) == t32)) st1 = poll(stp + 512, [&](u64 x) { return (unsigned)(x >> 32) == t32; });
      *(LAS float*)(lds + LDS_MEAN + lane * 4) = bflo((unsigned)st0);
      *(LAS float*)(lds + LDS_RSTD + lane * 4) = __builtin_amdgcn_rcpf(bfhi((unsigned)st0));
      *(LAS float*)(lds + LDS_MEAN + 256 + lane * 4) = bflo((unsigned)st1);
      *(LAS float*)(lds + LDS_RSTD + 256 + lane * 4) = __builtin_amdgcn_rcpf(bfhi((unsigned)st1));
    }
    wait_lgkm0();
    bar();
  }
  {
    csum(7);
    const float v0 = q == 0 ? cs[0][0] : q == 1 ? cs[1][0] : q == 2 ? cs[2][0] : cs[3][0];
    const float v1 = q == 0 ? cs[4][0] : q == 1 ? cs[5][0] : cs[6][0];
    *(LAS float*)(lds + LDS_CS + (hw * 11 * 16 + lane) * 4) = v0;
    if (q < 3) *(LAS float*)(lds + LDS_CS + ((hw * 11 + 4) * 16 + lane) * 4) = v1;
    wait_lgkm0();
    bar();
  }
#pragma unroll
  for (int n = 0; n < 4; ++n) cs[n] = z4;
#pragma unroll 1
  for (int h = 0; h < 2; ++h) {
#pragma unroll
    for (int j = 0; j < 2; ++j) {
      LAS char* ba = lds + ((h | j) == 0 ? 8 : 3 + 2 * h + j) * SLOT;
#pragma unroll
      for (int k = 0; k < 2; ++k)
#pragma unroll
        for (int n = 0; n < 4; ++n) cs[n] = MFMA(ones, ldstr(ba + ja[n] + k * 8192, 512), cs[n]);
    }
    if (h == 0) {
      wait_lgkm0();
      bar();
      if (hw == 0 && lane == 0) set_flag(ws, 0, c.tp * 16 + cp, tag);
      bar();
      if (hw == 1) {
#pragma unroll
        for (int i = 0; i < PMF; ++i) { touch(Cp, dump); Cp += 256; }
      }
    }
  }
  *(LAS float*)(lds + LDS_CS + ((hw * 11 + 7) * 16 + lane) * 4) = q == 0 ? cs[0][0] : q == 1 ? cs[1][0] : q == 2 ? cs[2][0] : cs[3][0];
  wait_lgkm0();
  if (lane == 0) *(volatile LAS unsigned*)(lds + LDS_TAG + 8 + 4 * hw) = 1u;
  const char* Wp = c.w2 + (c.ec * 128 + (c.ks & 1) * 64) * 2 + (size_t)((c.tp & 7) * 224 + hw * 112 + (lane < 56 ? lane : 0)) * 1024;
  const bool ew = blockIdx.x == 0 && hw == 1;
  const char* fp = ws + WS_FLAG + (unsigned)lane * FLAG_STRIDE;
  u64 f0 = 0, f1 = 0, f2 = 0, f3 = 0;
  auto run = [&](int s0, int s1) {
#pragma unroll 1
    for (int s = s0; s < s1; ++s) {
      if (PREFW && s == PREFW) { touch(Wp, dump); touch(Wp + 56 * 1024, dump); }
      if (hw == 1 && ((PM2 >> (s + 1)) & 1)) { touch(Cp, dump); Cp += 256; }
      bar();
      if (hw == 0 && lane == 0) {
        if (s == HFLAG) set_flag(ws, 1, c.tp * 16 + cp, tag);
        if (s == YFLAG) set_flag(ws, 2, (c.tp * 4 + c.ec) * 4 + c.ks, tag);
      }
    }
  };
  wait_lgkm0();
  run(-1, 10);
  if (ew) { f0 = ld8_async(fp); f1 = ld8_async(fp + 64 * FLAG_STRIDE); f2 = ld8_async(fp + 128 * FLAG_STRIDE); f3 = ld8_async(fp + 192 * FLAG_STRIDE); }
  run(10, 15);
  wait_vm<0>();
  asm volatile("" : "+v"(f0), "+v"(f1), "+v"(f2), "+v"(f3));
  const bool early = ew && __all(f0 == tag && f1 == tag && f2 == tag && f3 == tag);
  if (early && lane == 0) __hip_atomic_store((GAS u64*)(ws + WS_EPOCH), tag, __ATOMIC_RELAXED, __HIP_MEMORY_SCOPE_AGENT);
  run(15, YPOLL + 1);
  const char* yfp = ws + WS_FLAG + (unsigned)(2 * 256 + (c.tp * 4 + c.ec) * 4 + (lane & 3)) * FLAG_STRIDE;
  f0 = ld8_async(yfp);
  run(YPOLL + 1, YPOLL + 2);
  f1 = ld8_async(yfp);
  run(YPOLL + 2, YCHK);
  wait_vm<1>();
  asm volatile("" : "+v"(f0));
  if (!__all(f0 == tag)) {
    wait_vm<0>();
    asm volatile("" : "+v"(f1));
    if (!__all(f1 == tag)) poll(yfp, [&](u64 x) { return x == tag; });
  }
  run(YCHK, 29);
  wait_vm<0>();
  asm volatile("" : "+v"(f1));
  if (ew && !early) {
#pragma unroll 1
    for (int i = 0; i < 4; ++i) poll(fp + i * 64 * FLAG_STRIDE, [&](u64 x) { return x == tag; });
    if (lane == 0) __hip_atomic_store((GAS u64*)(ws + WS_EPOCH), tag, __ATOMIC_RELAXED, __HIP_MEMORY_SCOPE_AGENT);
  }
  wait_vm<0>();
  BT_END();
}

DEV void consumer2(const Ctx& c DBG_PARAM BT_PARAM) {
  const int w = c.wid, lane = fresh_lane();
  LAS char* lds = c.lds;
  const int q = lane >> 4, cc = lane & 15, qq = cc >> 2, pp = lane & 3;
  const unsigned wa0 = cc * 128 + ((q ^ (cc & 7)) * 16), wa1 = cc * 128 + (((4 + q) ^ (cc & 7)) * 16);
  const int wt = w >> 1, we = w & 1;
  unsigned wb[2];
#pragma unroll
  for (int n = 0; n < 2; ++n) wb[n] = 4 * SLOT + (4 * q + qq) * 128 + (((2 * (2 * we + n) + (pp >> 1)) ^ ((4 * q + qq) & 6)) * 16) + 8 * (pp & 1);
  const unsigned hb = wt * 2048 + lane * 16;
  bf16x8 hA[4][2], wA[4][2], hB[4][2], wB[4][2];
  wait_lgkm0();
  bar();
  STAMP(5);
#define PREAD(HN, WN, SO) if (i < 12) { if (i % 3 == 2) { HN[i / 3][0] = ldsb128(lds + (SO) + hb + (i / 3) * 4096); HN[i / 3][1] = ldsb128(lds + (SO) + hb + (i / 3) * 4096 + 1024); } \
                                        else WN[i / 3][i % 3] = ldstr(lds + (SO) + wb[i % 3] + (i / 3) * 4096, 2048); SCHED(); }
  {
    f32x4 acc[4][4];
#pragma unroll
    for (int m = 0; m < 4; ++m)
#pragma unroll
      for (int n = 0; n < 4; ++n) acc[m][n] = (f32x4){0, 0, 0, 0};
    const unsigned a0 = wt * 8192 + wa0, a1 = wt * 8192 + wa1, b0 = we * 4096 + lane * 16;
    bf16x8 fa0[4], fb0[4], fa1[4], fb1[4];
#pragma unroll
    for (int m = 0; m < 4; ++m) { fa0[m] = ldsb128(lds + a0 + m * 2048); fb0[m] = ldsb128(lds + b0 + 7 * SLOT + m * 1024); }
#define HALF(FA, FB, NA_, NB_, AOFF, BOFF) do { _Pragma("unroll") for (int i = 0; i < 16; ++i) { \
      acc[i >> 2][i & 3] = MFMA(FB[i & 3], FA[i >> 2], acc[i >> 2][i & 3]); SCHED(); \
      if (i < 8) { if (i == 0) NB_[0] = ldsb128(lds + (BOFF)); else if (i == 1) NA_[0] = ldsb128(lds + (AOFF)); \
        else if (i < 5) NB_[i - 1] = ldsb128(lds + (BOFF) + (i - 1) * 1024); else NA_[i - 4] = ldsb128(lds + (AOFF) + (i - 4) * 2048); SCHED(); } } } while (0)
    unsigned so = 0, sb = 7 * SLOT, bseq = 0x76548;
#pragma unroll 1
    for (int seg = 0; seg < 2; ++seg) {
      const int n = seg ? 15 - HFLAG : HFLAG;
#pragma unroll 1
      for (int s = 0; s < n; ++s) {
        HALF(fa0, fb0, fa1, fb1, so + a1, sb + b0 + 8192);
        wait_lgkm0();
        bar();
        sb = (bseq & 15) * SLOT; bseq = (bseq >> 4) | ((bseq & 15) << 16); so = (so + SLOT) & (4 * SLOT - 1);
        HALF(fa1, fb1, fa0, fb0, so + a0, sb + b0);
      }
      wait_vm<0>();
    }
    HALF(fa0, fb0, fa1, fb1, so + a1, sb + b0 + 8192);
    wait_lgkm0();
    bar();
#pragma unroll
    for (int i = 0; i < 16; ++i) {
      acc[i >> 2][i & 3] = MFMA(fb1[i & 3], fa1[i >> 2], acc[i >> 2][i & 3]); SCHED();
      PREAD(hA, wA, 0)
    }
    STAMP(6);
    if (w == c.ks) {
#pragma unroll
      for (int m = 0; m < 4; ++m)
#pragma unroll
        for (int n = 0; n < 4; ++n) *(LAS f32x4*)(lds + 8 * SLOT + (m * 4 + n) * 1024 + lane * 16) = acc[m][n];
    } else {
      const rsrc_t rY = mk_rsrc(c.ws + WS_YP + (size_t)(((c.tp * 4 + c.ec) * 4 + c.ks) * 4 + w) * 8192, 8192);
#pragma unroll
      for (int m = 0; m < 4; ++m)
#pragma unroll
        for (int j = 0; j < 2; ++j) {
          const f32x4 e = acc[m][2 * j], o = acc[m][2 * j + 1];
          __builtin_amdgcn_raw_buffer_store_b128((u32x4){pk_bf16(e[0], e[1]), pk_bf16(e[2], e[3]), pk_bf16(o[0], o[1]), pk_bf16(o[2], o[3])}, rY, lane * 16 + (m * 2 + j) * 1024, 0, 16);
        }
    }
  }
  STAMP(7);
  {
    f32x4 acc[2][2];
#pragma unroll
    for (int m = 0; m < 2; ++m) { acc[m][0] = (f32x4){0, 0, 0, 0}; acc[m][1] = (f32x4){0, 0, 0, 0}; }
    unsigned so = 0;
#define PSTEP(HC, WC, HN, WN) do { wait_lgkm0(); bar(); so = (so + SLOT) & (4 * SLOT - 1); SCHED(); \
      _Pragma("unroll") for (int i = 0; i < 16; ++i) { \
        acc[(i >> 1) & 1][i & 1] = MFMA(WC[i >> 2][i & 1], HC[i >> 2][(i >> 1) & 1], acc[(i >> 1) & 1][i & 1]); SCHED(); \
        PREAD(HN, WN, so) } } while (0)
    static_assert(YFLAG == 18 && YCHK == 25, "schedule");
#pragma unroll 1
    for (int seg = 0; seg < 2; ++seg) {
      const int n = seg ? 4 : 1;
#pragma unroll 1
      for (int s = 0; s < n; ++s) { PSTEP(hA, wA, hB, wB); PSTEP(hB, wB, hA, wA); }
      wait_vm<0>();
    }
    const rsrc_t rY = mk_rsrc(c.ws + WS_YP + (size_t)((c.tp * 4 + c.ec) * 16 + c.ks) * 8192, 4 * 32768 - 3 * 8192);
    u32x4 yp[3][2];
    const unsigned yv = lane * 16 + (4 * wt + we) * 1024;
#pragma unroll
    for (int i = 0; i < 3; ++i) yp[i][0] = __builtin_amdgcn_raw_buffer_load_b128(rY, yv, ((c.ks + 1 + i) & 3) * 32768, 16);
    SCHED();
    PSTEP(hA, wA, hB, wB);
#pragma unroll
    for (int i = 0; i < 3; ++i) yp[i][1] = __builtin_amdgcn_raw_buffer_load_b128(rY, yv + 2048, ((c.ks + 1 + i) & 3) * 32768, 16);
    SCHED();
    PSTEP(hB, wB, hA, wA);
    STAMP(8);
    const int tl = 32 * wt + 8 * qq + (cc & 3), el = 32 * we + 4 * q;
    f32x4 y[2][2], bv[2];
    u32x2 xr[2][2];
#pragma unroll
    for (int n = 0; n < 2; ++n) {
      bv[n] = *(const LAS f32x4*)(lds + LDS_B2 + (el + 16 * n) * 4);
#pragma unroll
      for (int m = 0; m < 2; ++m) {
        y[m][n] = *(const LAS f32x4*)(lds + 8 * SLOT + ((2 * wt + m) * 4 + 2 * we + n) * 1024 + lane * 16);
        xr[m][n] = *(const LAS u32x2*)(lds + LDS_XT + (tl + 4 * m) * 128 + (((el + 16 * n) * 2) ^ ((2 * qq + ((cc >> 1) & 1)) * 16)));
      }
    }
    PSTEP(hA, wA, hB, wB);
#pragma unroll
    for (int m = 0; m < 2; ++m)
#pragma unroll
      for (int i = 0; i < 3; ++i) { y[m][0] += unpk(yp[i][m].x, yp[i][m].y); y[m][1] += unpk(yp[i][m].z, yp[i][m].w); }
    char* op = c.out + (size_t)(c.tp * 128 + (c.ks >> 1) * 64 + tl) * 1024 + (c.ec * 128 + (c.ks & 1) * 64 + el) * 2;
    SCHED();
#pragma unroll
    for (int i = 0; i < 16; ++i) acc[i >> 3][(i >> 2) & 1] = MFMA(wB[i & 3][(i >> 2) & 1], hB[i & 3][i >> 3], acc[i >> 3][(i >> 2) & 1]);
    STAMP(9);
#pragma unroll
    for (int m = 0; m < 2; ++m)
#pragma unroll
      for (int n = 0; n < 2; ++n) {
        f32x4 o = unpk(xr[m][n].x, xr[m][n].y);
#pragma unroll
        for (int r = 0; r < 4; ++r) { const float z = acc[m][n][r] + bv[n][r]; o[r] += y[m][n][r] * (z * __builtin_amdgcn_rcpf(1.f + __expf(-z))); }
        *(u32x2*)(op + m * 4096 + n * 32) = (u32x2){pk_bf16(o[0], o[1]), pk_bf16(o[2], o[3])};
      }
    STAMP(10);
    STAMP_END(); BT_END();
  }
}

DEV void consumer(const Ctx& c) {
  DBG_DECL; BT_DECL;
  const int w = c.wid;
  LAS char* lds = c.lds;
  STAMP(0);
  unsigned ja[4], bx0, bx1, bw0, bw1;
  {
    const int lane = fresh_lane(), q = lane >> 4, cc = lane & 15, qq = cc >> 2, pp = lane & 3;
    if (PREFI) { touch(c.pc + w * 8192 + lane * 128, (unsigned)(size_t)lds + LDS_DUMP); if (256 + w * 64 < PREFI) touch(c.pc + 32768 + w * 8192 + lane * 128, (unsigned)(size_t)lds + LDS_DUMP); }
    const unsigned wa0 = cc * 128 + ((q ^ (cc & 7)) * 16), wa1 = cc * 128 + (((4 + q) ^ (cc & 7)) * 16);
#pragma unroll
    for (int n = 0; n < 4; ++n) ja[n] = opq(4 * SLOT + (8 * q + qq) * 128 + (((2 * n + (pp >> 1)) ^ ((qq & 2) | ((q & 1) << 2))) * 16) + 8 * (pp & 1));
    bx0 = opq(wa0 + w * 4096); bx1 = opq(wa1 + w * 4096); bw0 = opq(wa0 + 4 * SLOT); bw1 = opq(wa1 + 4 * SLOT);
  }
  bf16x8 xf[2][16];
  struct HTab { float mean[2], rstd[2]; f32x4 cs[4], bv[4]; unsigned vo; };
  auto h_tab = [&](HTab& t, int brow, int csrow) {
    const int lane = fresh_lane(), q = lane >> 4, cc = lane & 15;
#pragma unroll
    for (int u = 0; u < 2; ++u) { t.mean[u] = *(const LAS float*)(lds + LDS_MEAN + ((2 * w + u) * 16 + cc) * 4); t.rstd[u] = *(const LAS float*)(lds + LDS_RSTD + ((2 * w + u) * 16 + cc) * 4); }
#pragma unroll
    for (int n = 0; n < 4; ++n) {
      t.cs[n] = *(const LAS f32x4*)(lds + LDS_CS + ((csrow + n) * 16 + 4 * q) * 4) + *(const LAS f32x4*)(lds + LDS_CS + ((11 + csrow + n) * 16 + 4 * q) * 4);
      t.bv[n] = *(const LAS f32x4*)(lds + LDS_B1 + (brow * 64 + n * 16 + 4 * q) * 4);
    }
    t.vo = lane * 16;
  };
  auto h_half = [&](const HTab& t, const f32x4& a, int n, int u) {
    f32x4 z = (a - t.mean[u] * t.cs[n]) * t.rstd[u] + t.bv[n];
    z = __builtin_elementwise_max(z, (f32x4){0.f, 0.f, 0.f, 0.f});
    z = z * z;
    return (u32x2){pk_bf16(z[0], z[1]), pk_bf16(z[2], z[3])};
  };
  auto h_store = [&](const HTab& t, u32x2 d0, u32x2 d1, int P, int b, int u) {
    const rsrc_t rH = mk_rsrc(c.ws + WS_H + (size_t)((((2 * c.tp + (w >> 1)) * 56 + 2 * P) * 4 + 2 * (w & 1)) * 1024), 8192 - 2048);
    __builtin_amdgcn_raw_buffer_store_b128((u32x4){d0.x, d0.y, d1.x, d1.y}, rH, t.vo + u * 1024 + b * 4096, 0, 16);
  };
  constexpr int L2 = 8;
  bf16x8 g[64];
  auto rd2 = [&](auto t_) {
    constexpr int t = decltype(t_)::value, j = t / 16, k = (t / 4) & 3, n = t & 3;
    if constexpr (j == 0) g[t] = ldstr(lds + 4 * SLOT + ja[n] + k * 4096, 512); else g[t] = ldstr(lds + (j - 1) * SLOT + ja[n] + k * 4096, 512);
  };
  bar();
  STAMP(1);
  auto pass1 = [&](auto na_) {
    constexpr int NA = decltype(na_)::value;
    f32x4 acc[7][2];
#pragma unroll
    for (int n = 0; n < 7; ++n) { acc[n][0] = (f32x4){0, 0, 0, 0}; acc[n][1] = (f32x4){0, 0, 0, 0}; }
    bf16x8 fr[112];
    auto rd = [&](auto t_) {
      constexpr int t = decltype(t_)::value, kk = t / 7, n = t % 7, so = ((kk >> 1) & 3) * SLOT;
      if constexpr (n == 0) { xf[0][kk] = ldsb128(lds + ((kk & 1) ? bx1 : bx0) + so); xf[1][kk] = ldsb128(lds + ((kk & 1) ? bx1 : bx0) + so + 2048); }
      if constexpr (n < NA) fr[t] = ldsb128(lds + ((kk & 1) ? bw1 : bw0) + so + n * 2048);
      else fr[t] = ldstr(lds + ja[(n - 3) & 3] + so + 6144 + (kk & 1) * 4096, 512);
    };
    static_for<0, 7>(rd);
    SCHED();
    static_for<0, 112>([&](auto p_) {
      constexpr int p = decltype(p_)::value, kk = p / 7, n = p % 7;
      constexpr int kn = p / 14 + 1;
      if constexpr (p == 14 * kn - lead1(kn)) { wait_lgkm0(); bar(); SCHED(); }
      const bf16x8 f = fr[p];
      if constexpr (n < NA) acc[n][0] = MFMA(xf[0][kk], f, acc[n][0]); else acc[n][0] = MFMA(f, xf[0][kk], acc[n][0]);
      SCHED();
      static_for<p + 1, p + 15>([&](auto t_) {
        constexpr int t = decltype(t_)::value;
        if constexpr (t < 112 && t >= 7 && sched1(t) == p) { rd(t_); SCHED(); }
      });
      if constexpr (n < NA) acc[n][1] = MFMA(xf[1][kk], f, acc[n][1]); else acc[n][1] = MFMA(f, xf[1][kk], acc[n][1]);
      SCHED();
    });
    STAMP(2);
    const int lane = fresh_lane(), q = lane >> 4, cc = lane & 15;
    f32x4 mr[2], rr[2];
    const float sgn = c.E0 >= 32 ? -1.f : 1.f;
#pragma unroll
    for (int u = 0; u < 2; ++u) {
      mr[u] = *(const LAS f32x4*)(lds + LDS_MEAN + ((2 * w + u) * 16 + 4 * q) * 4);
      rr[u] = *(const LAS f32x4*)(lds + LDS_RSTD + ((2 * w + u) * 16 + 4 * q) * 4) * sgn;
    }
    float cs[NA];
#pragma unroll
    for (int n = 0; n < NA; ++n) cs[n] = *(const LAS float*)(lds + LDS_CS + (n * 16 + cc) * 4) + *(const LAS float*)(lds + LDS_CS + ((11 + n) * 16 + cc) * 4);
    const rsrc_t rA = mk_rsrc(c.ws + WS_ABT, 4u << 20);
#pragma unroll
    for (int n = 0; n < NA; ++n) {
      const f32x4 o0 = (acc[n][0] - mr[0] * cs[n]) * rr[0], o1 = (acc[n][1] - mr[1] * cs[n]) * rr[1];
      u32x4 d = {pk_bf16(o0[0], o0[1]), pk_bf16(o0[2], o0[3]), pk_bf16(o1[0], o1[1]), pk_bf16(o1[2], o1[3])};
      const int E = c.E0 + n;
      __builtin_amdgcn_raw_buffer_store_b128(d, rA, lane * 16 + (unsigned)((((E >> 3) * 64 + c.tp * 4 + w) * 8 + (E & 7)) * 1024), 0, 16);
    }
    SCHED();
    static_for<0, L2>(rd2);
    STAMP(3);
    f32x4 ac2[4][2];
#pragma unroll
    for (int n = 0; n < 4; ++n) { ac2[n][0] = (f32x4){0, 0, 0, 0}; ac2[n][1] = (f32x4){0, 0, 0, 0}; }
    HTab ht;
    u32x2 hd[2];
    SCHED();
    static_assert(RPOS >= 16 && RPOS + L2 <= 32 && FPOS < RPOS, "R must follow the last use of J0 and precede the first read of J2");
    static_for<0, 64>([&](auto p_) {
      constexpr int p = decltype(p_)::value, kk = p / 4, n = p & 3;
      if constexpr (p == FPOS) { wait_vm<0>(); bar(); SCHED(); }
      if constexpr (p == RPOS) { bar(); SCHED(); }
      ac2[n][0] = MFMA(g[p], xf[0][kk], ac2[n][0]);
      SCHED();
      if constexpr (p + L2 < 64) { rd2(IC<p + L2>{}); SCHED(); }
      if constexpr (NA == 3) {
        constexpr int e0 = EPOS + 6;
        if constexpr (p == EPOS) h_tab(ht, 0, 3);
        if constexpr (p >= e0 && p < e0 + 32 && (p - e0) % 4 == 0) {
          constexpr int i = (p - e0) / 4, b = i >> 2, u = (i >> 1) & 1, h = i & 1;
          hd[h] = h_half(ht, acc[3 + 2 * b + h][u], 2 * b + h, u);
          if constexpr (h == 1) h_store(ht, hd[0], hd[1], c.PA, b, u);
        }
      }
      ac2[n][1] = MFMA(g[p], xf[1][kk], ac2[n][1]);
      if constexpr (!(NA == 3 && p >= EPOS + 6 && p < EPOS + 38 && (p - EPOS - 6) % 4 < 2)) SCHED();
    });
    STAMP(11);
    while (*(volatile LAS u64*)(lds + LDS_TAG + 8) != 0x100000001ull) __builtin_amdgcn_s_sleep(1);
    asm volatile("" ::: "memory");
    h_tab(ht, 1, 7);
#pragma unroll
    for (int b = 0; b < 2; ++b)
#pragma unroll
      for (int u = 0; u < 2; ++u) h_store(ht, h_half(ht, ac2[2 * b][u], 2 * b, u), h_half(ht, ac2[2 * b + 1][u], 2 * b + 1, u), c.PB, b, u);
  };
  if (c.typeB) pass1(IC<7>{}); else pass1(IC<3>{});
  STAMP(4);
  consumer2(c DBG_ARG BT_ARG);
}

__global__ __launch_bounds__(512, 2) void fnet_mlp_fused(const char* x, const char* Cr, const char* Ci, const char* Wr, const char* Wi,
                                                         const char* w1, const char* b1, const char* w2, const char* b2, char* out, char* ws) {
  extern __shared__ __attribute__((aligned(16))) char shm[];
  Ctx c;
  { u64 pc; asm volatile("s_getpc_b64 %0" : "=s"(pc)); c.pc = (const char*)(pc & ~(u64)255); }
  c.x = x; c.Cr = Cr; c.Ci = Ci; c.Wr = Wr; c.Wi = Wi; c.w1 = w1; c.b1 = b1; c.w2 = w2; c.b2 = b2; c.out = out; c.ws = ws;
  c.lds = (LAS char*)shm;
  c.wid = __builtin_amdgcn_readfirstlane(threadIdx.x >> 6);
  const int b = blockIdx.x, xcd = b & 7, r = b >> 3;
  c.ks = xcd >> 1; c.tp = (xcd & 1) * 8 + (r >> 2); c.ec = r & 3;
  c.typeB = c.ec == 3;
  c.E0 = 16 * c.ks + 3 * c.ec; c.PA = 7 * c.ks + 2 * c.ec; c.PB = c.typeB ? c.PA : c.PA + 1;
  c.rot1 = (ROT & 1) ? (2 * c.ec + (r >> 2)) & 7 : 0; c.rot2 = (ROT & 2) ? (4 * c.ec + 2 * (r >> 2)) & 15 : 0;
  if (c.wid == 4) loaderA(c); else if (c.wid == 5) loaderB(c); else if (c.wid >= 6) helper(c); else consumer(c);
}

__global__ void k_mark(char* ws) { if (threadIdx.x == 0) __hip_atomic_store((GAS u64*)(ws + 2048), __builtin_amdgcn_s_memrealtime(), __ATOMIC_RELAXED, __HIP_MEMORY_SCOPE_AGENT); }
__global__ void k_spin(char* ws) { const u64 e = __hip_atomic_load((GAS u64*)(ws + 2048), __ATOMIC_RELAXED, __HIP_MEMORY_SCOPE_AGENT) + 10000; while (__builtin_amdgcn_s_memrealtime() < e) __builtin_amdgcn_s_sleep(1); }
extern "C" void kernel_launch(void* const* d_in, const int* in_sizes, int n_in, void* d_out, int out_size, void* d_ws, size_t ws_size, hipStream_t stream) {
  (void)hipFuncSetAttribute((const void*)fnet_mlp_fused, hipFuncAttributeMaxDynamicSharedMemorySize, LDS_BYTES);
  k_mark<<<1, 64, 0, stream>>>((char*)d_ws);
  fnet_mlp_fused<<<256, 512, LDS_BYTES, stream>>>((const char*)d_in[0], (const char*)d_in[1], (const char*)d_in[2], (const char*)d_in[3], (const char*)d_in[4],
                                                  (const char*)d_in[6], (const char*)d_in[7], (const char*)d_in[8], (const char*)d_in[9], (char*)d_out, (char*)d_ws);
}
```

```cpp
#define FPOS 14
#include <hip/hip_runtime.h>
typedef unsigned short u16;
typedef unsigned long long u64;
typedef short bf16x8 __attribute__((ext_vector_type(8)));
typedef short s16x4 __attribute__((ext_vector_type(4)));
typedef float f32x4 __attribute__((ext_vector_type(4)));
typedef float f32x2 __attribute__((ext_vector_type(2)));
typedef unsigned u32x4 __attribute__((ext_vector_type(4)));
typedef unsigned u32x2 __attribute__((ext_vector_type(2)));
typedef __bf16 bf2 __attribute__((ext_vector_type(2)));
#define LAS __attribute__((address_space(3)))
#define GAS __attribute__((address_space(1)))
#define DEV __device__ __forceinline__

constexpr int S_ = 2048, D_ = 512, F_ = 1792, W1R = F_ * 2;
constexpr unsigned WS_EPOCH = 0, WS_FLAG = 4096, FLAG_STRIDE = 128, WS_ROWST = 256u << 10, WS_CSW = 288u << 10, WS_CS1 = 512u << 10;
constexpr unsigned WS_ABT = 1u << 20, WS_H = 8u << 20, WS_YP = 16u << 20, WS_BYTES = 32u << 20;
constexpr int SLOT = 16384, NSLOT = 9;
constexpr int LDS_XT = NSLOT * SLOT;
constexpr int LDS_MEAN = LDS_XT + 8192, LDS_RSTD = LDS_MEAN + 512, LDS_CSW = LDS_RSTD + 512, LDS_CS1 = LDS_CSW + 256;
constexpr int LDS_B1 = LDS_CS1 + 512, LDS_B2 = LDS_B1 + 512, LDS_TAG = LDS_B2 + 256;
constexpr int LDS_DUMP = LDS_TAG + 32, LDS_BYTES = LDS_DUMP + 256;
static_assert(LDS_BYTES <= 160 * 1024, "LDS");
#define ROT 1
#define PM1 0x04
#define PMF 2
#define PM2 0xD5
#define PREFI 240
#define PREFW 8
#define DMAU 4
#define STLD 3
#define HSPLIT 2
#define HPRIO 3
#define FINE 1
#define NINIT 3
#define LEADE 7
#define LOOKE 14
#define LEADL 4
#define LOOKL 8
#define RPOS 16
#define LOOK2 8
constexpr int HFLAG = 3, YFLAG = 18, YPOLL = 22, YCHK = 25;
constexpr unsigned JSEQ = 0x6548u, CSEQ = 0x3210u, BSEQ = 0x65487u, WSEQ = 0x7654u;
constexpr int nib(unsigned seq, int i) { return (seq >> (4 * i)) & 15; }

template <int N> DEV void wait_vm() { asm volatile("s_waitcnt vmcnt(%0)" ::"n"(N) : "memory"); }
DEV void wait_lgkm0() { asm volatile("s_waitcnt lgkmcnt(0)" ::: "memory"); }
DEV void bar() { asm volatile("" ::: "memory"); __builtin_amdgcn_s_barrier(); asm volatile("" ::: "memory"); }
#define rawbar() __builtin_amdgcn_s_barrier()
#define BT_DECL
#define BT_END() do {} while (0)
#define BT_PARAM
#define BT_ARG
#define SCHED() __builtin_amdgcn_sched_barrier(0)
#define MFMA(a, b, c) __builtin_amdgcn_mfma_f32_16x16x32_bf16((a), (b), (c), 0, 0, 0)
DEV unsigned pk_bf16(float a, float b) { f32x2 f = {a, b}; return __builtin_bit_cast(unsigned, __builtin_convertvector(f, bf2)); }
DEV float rbf(float a) { return __uint_as_float(pk_bf16(a, 0.f) << 16); }
DEV float bflo(unsigned v) { return __uint_as_float(v << 16); }
DEV float bfhi(unsigned v) { return __uint_as_float(v & 0xffff0000u); }
DEV f32x4 unpk(unsigned a, unsigned b) { return (f32x4){bflo(a), bfhi(a), bflo(b), bfhi(b)}; }
constexpr unsigned ONE2 = 0x3f803f80u;
DEV float dot2(unsigned a, unsigned b, float c) { return __builtin_amdgcn_fdot2_f32_bf16(__builtin_bit_cast(bf2, a), __builtin_bit_cast(bf2, b), c, false); }
DEV bf16x8 ldsb128(LAS char* p) { return *(const LAS bf16x8*)p; }
DEV bf16x8 ldstr(LAS char* p, int second) {
  s16x4 a = __builtin_amdgcn_ds_read_tr16_b64_v4i16((LAS s16x4*)p);
  s16x4 b = __builtin_amdgcn_ds_read_tr16_b64_v4i16((LAS s16x4*)(p + second));
  return __builtin_shufflevector(a, b, 0, 1, 2, 3, 4, 5, 6, 7);
}
DEV unsigned opq(unsigned v) { asm volatile("" : "+v"(v)); return v; }
DEV int fresh_lane() { int l; asm volatile("v_mbcnt_lo_u32_b32 %0, -1, 0\n\tv_mbcnt_hi_u32_b32 %0, -1, %0" : "=v"(l)); return l; }
typedef __amdgpu_buffer_rsrc_t rsrc_t;
DEV rsrc_t mk_rsrc(const void* p, unsigned bytes) { return __builtin_amdgcn_make_buffer_rsrc((void*)p, 0, bytes, 0x00020000); }
DEV u32x4 mk_desc(const void* p, unsigned bytes) { const u64 a = (u64)p; return (u32x4){(unsigned)a, (unsigned)(a >> 32) & 0xffffu, bytes, 0x00020000u}; }
#define DMA4(POL) asm volatile("s_mov_b32 m0, %9\n\ts_nop 0\n\tbuffer_load_dwordx4 %0, %4, %5 offen " POL "lds\n\tbuffer_load_dwordx4 %1, %4, %6 offen offset:1024 " POL "lds\n\t" \
    "buffer_load_dwordx4 %2, %4, %7 offen offset:2048 " POL "lds\n\tbuffer_load_dwordx4 %3, %4, %8 offen offset:3072 " POL "lds" \
    ::"v"(v0), "v"(v1), "v"(v2), "v"(v3), "s"(desc), "s"(s0), "s"(s1 - 1024), "s"(s2 - 2048), "s"(s3 - 3072), "s"(dst) : "memory")
template <bool DEVSCOPE> DEV void dma4(u32x4 desc, unsigned v0, unsigned v1, unsigned v2, unsigned v3, unsigned s0, unsigned s1, unsigned s2, unsigned s3, unsigned dst) {
  if constexpr (DEVSCOPE) DMA4("sc1 "); else DMA4("");
}
DEV u32x4 ld16_async(const char* p) { u32x4 v; asm volatile("global_load_dwordx4 %0, %1, off sc1" : "=v"(v) : "v"(p) : "memory"); return v; }
DEV u32x4 ld16_plain(const char* p) { u32x4 v; asm volatile("global_load_dwordx4 %0, %1, off" : "=v"(v) : "v"(p) : "memory"); return v; }
DEV unsigned ld2_plain(const char* p) { unsigned v; asm volatile("global_load_ushort %0, %1, off" : "=v"(v) : "v"(p) : "memory"); return v; }
DEV u64 ld8_async(const char* p) { u64 v; asm volatile("global_load_dwordx2 %0, %1, off sc1" : "=v"(v) : "v"(p) : "memory"); return v; }
DEV void touch(const char* p, unsigned dump) {
  unsigned keep;
  asm volatile("s_mov_b32 %0, m0\n\ts_mov_b32 m0, %2\n\ts_nop 0\n\tglobal_load_lds_dword %1, off\n\ts_mov_b32 m0, %0" : "=&s"(keep) : "v"(p), "s"(dump) : "memory");
}
template <int CTRL> DEV float dpp_add(float x) { return x + __builtin_bit_cast(float, __builtin_amdgcn_update_dpp(0, __builtin_bit_cast(int, x), CTRL, 0xf, 0xf, false)); }
DEV float sumq(float x) {
  auto r = __builtin_amdgcn_permlane16_swap(__float_as_uint(x), __float_as_uint(x), false, false);
  x = __uint_as_float(r[0]) + __uint_as_float(r[1]);
  r = __builtin_amdgcn_permlane32_swap(__float_as_uint(x), __float_as_uint(x), false, false);
  return __uint_as_float(r[0]) + __uint_as_float(r[1]);
}
DEV float sum16(float x) { x = dpp_add<0xB1>(x); x = dpp_add<0x4E>(x); x = dpp_add<0x141>(x); return dpp_add<0x140>(x); }

#define STAMP(i) do {} while (0)
#define STAMP_END() do {} while (0)
#define DBG_DECL
#define DBG_PARAM
#define DBG_ARG

template <int N> struct IC { static constexpr int value = N; };
template <int B, int E, class F> DEV void static_for(F&& f) { if constexpr (B < E) { f(IC<B>{}); static_for<B + 1, E>(f); } }
constexpr int lead1(int k) { return k >= 6 ? LEADL : LEADE; }
constexpr int sched1(int t) { const int k = t / 16, e0 = t - (k >= 6 ? LOOKL : LOOKE), e1 = 16 * k - lead1(k) + (t - 16 * k) / 2; return k == 0 ? (t - 8 > 0 ? (t - 8) / 2 : 0) : (e0 > e1 ? e0 : e1); }

struct Ctx {
  const char *x, *Cr, *Ci, *Wr, *Wi, *w1, *b1, *w2, *b2; char* out; char* ws; LAS char* lds; const char* pc; unsigned lb;
  int wid, tp, ec, ks;
  int PA, rot1, rot2;
  bool two;
};

template <class P> DEV u64 poll(const char* p, P ok) {
  const u64 t0 = __builtin_amdgcn_s_memrealtime();
  u64 v;
  for (;;) {
    v = __hip_atomic_load((GAS u64*)p, __ATOMIC_RELAXED, __HIP_MEMORY_SCOPE_AGENT);
    if (__all(ok(v))) break;
    if (__builtin_amdgcn_s_memrealtime() - t0 > 20000000ull) break;
    __builtin_amdgcn_s_sleep(1);
  }
  return v;
}
DEV void set_flag(char* ws, int kind, int idx, u64 tag) {
  __hip_atomic_store((GAS u64*)(ws + WS_FLAG + (unsigned)(kind * 256 + idx) * FLAG_STRIDE), tag, __ATOMIC_RELAXED, __HIP_MEMORY_SCOPE_AGENT);
}
DEV u64 uniform64(u64 v) {
  const unsigned lo = __builtin_amdgcn_readfirstlane((unsigned)v), hi = __builtin_amdgcn_readfirstlane((unsigned)(v >> 32));
  return ((u64)hi << 32) | lo;
}
#define PROW(p) (32 * ((p) >> 2) + 16 * ((p) & 1) + 4 * (((p) >> 1) & 1))
#define ROTN(seq, n) do { seq = (seq >> 4) | ((seq & 15) << (4 * (n) - 4)); } while (0)

template <int U = DMAU> DEV void unit_rows(u32x4 d, unsigned v, unsigned so, unsigned rowb, unsigned dst) {
#pragma unroll U
  for (int g = 0; g < 4; ++g) {
    dma4<false>(d, v, v, v, v, so, so + PROW(1) * rowb, so + PROW(2) * rowb, so + PROW(3) * rowb, dst);
    so += 32 * rowb; dst += 4096;
  }
}
template <bool DS, int U = DMAU> DEV void unit_step(u32x4 d, unsigned v0, unsigned v1, unsigned so, unsigned step, unsigned dst, int n4) {
#pragma unroll U
  for (int g = 0; g < n4; ++g) {
    dma4<DS>(d, v0, v1, v0, v1, so, so + step, so + 2 * step, so + 3 * step, dst);
    so += 4 * step; dst += 4096;
  }
}
DEV void issueAB(const Ctx& c, int s, int slot, unsigned vL) {
  const u32x4 d = mk_desc(c.ws + WS_ABT + (size_t)(((c.ks >> 1) * 4 + c.ec) * 64 + (c.ks & 1) * 32) * 8192, 262144);
  unit_step<true>(d, vL, vL, ((s + c.rot2) & 15) * 16384, 1024, c.lb + slot * SLOT, 4);
}
DEV void issueJ(const Ctx& c, int j, unsigned vJ) {
  const u32x4 d = mk_desc(c.w1, D_ * W1R);
#pragma unroll
  for (int h = 0; h < 2; ++h) unit_step<false>(d, vJ, vJ ^ 64, ((2 * j + h + c.rot1) & 7) * (64 * W1R) + (c.PA + (c.two ? 1 : 0)) * 128, 8 * W1R, c.lb + ((JSEQ >> (4 * j)) & 15) * SLOT + h * 8192, 2);
}
template <int NB> DEV void h_epilogue(const Ctx& c, const f32x4 (&acc)[2 * NB][2], int col0, int rg) {
  LAS char* lds = c.lds;
  const int lane = fresh_lane(), q = lane >> 4, cc = lane & 15;
  float mean[2], rstd[2];
#pragma unroll
  for (int u = 0; u < 2; ++u) { mean[u] = *(const LAS float*)(lds + LDS_MEAN + ((2 * rg + u) * 16 + cc) * 4); rstd[u] = *(const LAS float*)(lds + LDS_RSTD + ((2 * rg + u) * 16 + cc) * 4); }
  const rsrc_t rH = mk_rsrc(c.ws + WS_H + (size_t)((((2 * c.tp + (rg >> 1)) * 56 + 2 * c.PA + (col0 >> 5)) * 4 + 2 * (rg & 1)) * 1024), NB * 4096 - 2048);
  LAS char* tb = lds + col0 * 4 + q * 16;
  f32x4 cs[2 * NB], bv[2 * NB];
#pragma unroll
  for (int n = 0; n < 2 * NB; ++n) { cs[n] = *(const LAS f32x4*)(tb + LDS_CS1 + n * 64); bv[n] = *(const LAS f32x4*)(tb + LDS_B1 + n * 64); }
#pragma unroll
  for (int b = 0; b < NB; ++b)
#pragma unroll
    for (int u = 0; u < 2; ++u) {
      unsigned d[4];
#pragma unroll
      for (int h = 0; h < 2; ++h) {
        f32x4 z = (acc[2 * b + h][u] - mean[u] * cs[2 * b + h]) * rstd[u] + bv[2 * b + h];
        z = __builtin_elementwise_max(z, (f32x4){0.f, 0.f, 0.f, 0.f});
        z = z * z;
        d[2 * h] = pk_bf16(z[0], z[1]); d[2 * h + 1] = pk_bf16(z[2], z[3]);
      }
      __builtin_amdgcn_raw_buffer_store_b128((u32x4){d[0], d[1], d[2], d[3]}, rH, lane * 16 + u * 1024 + b * 4096, 0, 16);
    }
}

DEV void loaderA(const Ctx& c) {
  DBG_DECL; BT_DECL;
  const int lane = fresh_lane(), r8 = lane >> 3, p8 = lane & 7;
  char* ws = c.ws;
  const unsigned lb = c.lb, rl = 8 * (r8 >> 2) + (r8 & 3), swz = (unsigned)((p8 ^ r8) * 16);
  const u32x4 dX = mk_desc(c.x + (size_t)c.tp * 131072, 131072);
  const unsigned vX = rl * 1024 + swz;
  auto issueX = [&](int s) { unit_rows(dX, vX, ((s + c.rot1) & 7) * 128, 1024, lb + (s & 3) * SLOT); };
  STAMP(8);
#pragma unroll 1
  for (int s = 0; s < NINIT; ++s) issueX(s);
  const u32x4 dC = mk_desc((c.ks >> 1 ? c.Ci : c.Cr) + (size_t)c.tp * 524288 + (c.ks & 1) * 2048, 524288 - 2048);
  const u32x4 dH = mk_desc(ws + WS_H + (size_t)(2 * c.tp + (c.ks >> 1)) * (56 * 4096), 56 * 4096);
  const unsigned vC = rl * 4096 + swz, vL = lane * 16, vJ = r8 * W1R + (p8 ^ (r8 & 2)) * 16;
  auto issueC = [&](int s) { unit_rows(dC, vC, ((s + c.rot2) & 15) * 128, 4096, lb + (s & 3) * SLOT); };
  STAMP(10);
  wait_vm<16 * (NINIT - 1)>();
  STAMP(11);
  bar();
  if (NINIT == 2) issueX(2);
#pragma unroll 1
  for (int s = 0; s < 5; ++s) { issueX(s + 3); wait_vm<32>(); bar(); }
  issueJ(c, 1, vJ); wait_vm<32>(); bar();
#pragma unroll 1
  for (int s = 0; s < 2; ++s) { issueC(s); wait_vm<32>(); bar(); }
  const u64 tag = uniform64(*(const LAS u64*)(c.lds + LDS_TAG));
  bar();
#pragma unroll 1
  for (int s = 2; s < 4; ++s) { issueC(s); if (s == 2) bar(); }
  {
    const unsigned so = (c.ks >> 1) * 65536 + (c.ec * 128 + (c.ks & 1) * 64) * 2, vo = r8 * 1024 + (p8 ^ ((r8 >> 1) & 1)) * 16;
#pragma unroll
    for (int g = 0; g < 2; ++g) dma4<false>(dX, vo, vo ^ 32, vo ^ 64, vo ^ 96, so + g * 32768, so + g * 32768 + 8192, so + g * 32768 + 16384, so + g * 32768 + 24576, lb + LDS_XT + g * 4096);
  }
  wait_vm<56>();
  bar();
  issueAB(c, 3, nib(BSEQ, 3), vL);
  const char* hfp = ws + WS_FLAG + (unsigned)(1 * 256 + c.tp * 16 + (lane & 15)) * FLAG_STRIDE;
  u64 fv = 0;
#pragma unroll 1
  for (int s = 0; s < 12; ++s) { if (s < 2) wait_vm<56>(); else wait_vm<32>(); bar(); issueC(s + 4); if (s == 8) fv = ld8_async(hfp); }
  wait_vm<32>();
  asm volatile("" : "+v"(fv));
  if (!__all(fv == tag)) poll(hfp, [&](u64 x) { return x == tag; });
#pragma unroll 1
  for (int s = 12; s < 26; ++s) { wait_vm<32>(); bar(); unit_step<true>(dH, vL, vL, (s - 12) * 16384, 1024, lb + (s & 3) * SLOT, 4); }
  wait_vm<32>(); bar();
  wait_vm<16>(); bar();
  wait_vm<0>(); bar();
  STAMP_END(); BT_END();
}

DEV void loaderB(const Ctx& c) {
  DBG_DECL; BT_DECL;
  const int lane = fresh_lane(), r8 = lane >> 3, p8 = lane & 7;
  const unsigned lb = c.lb;
  const u32x4 dW = mk_desc((c.ks >> 1 ? c.Wi : c.Wr) + (size_t)((c.ks & 1) * 4 + c.ec) * 65536, 65536), d1 = mk_desc(c.w1, D_ * W1R);
  const unsigned vW = r8 * 1024 + (p8 ^ r8) * 16, vJ = r8 * W1R + (p8 ^ (r8 & 2)) * 16;
  auto issueB = [&](int s) {
    const int k = (s + c.rot1) & 7;
    unit_step<false>(dW, vW, vW, k * 128, 8192, lb + (4 + (s & 3)) * SLOT, 2);
    unit_step<false>(d1, vJ, vJ ^ 64, k * (64 * W1R) + c.PA * 128, 8 * W1R, lb + (4 + (s & 3)) * SLOT + 8192, 2);
  };
#pragma unroll 1
  for (int s = 0; s < NINIT; ++s) issueB(s);
  const u32x4 d2 = mk_desc(c.w2 + (c.ec * 128 + (c.ks & 1) * 64) * 2, F_ * 1024 - 1024 + 128);
  const unsigned vL = lane * 16, v2 = r8 * 1024 + (p8 ^ (r8 & 6)) * 16;
  wait_vm<16 * (NINIT - 1)>();
  bar();
  if (NINIT == 2) issueB(2);
#pragma unroll 1
  for (int s = 0; s < 5; ++s) { issueB(s + 3); wait_vm<32>(); bar(); }
#pragma unroll 1
  for (int s = 5; s < 8; ++s) { issueJ(c, s == 5 ? 0 : s - 4, vJ); wait_vm<32>(); bar(); }
  const u64 tag = uniform64(*(const LAS u64*)(c.lds + LDS_TAG));
  bar();
  wait_vm<0>();
  bar();
  STAMP(1);
  const int pi = (lane >> 1) & 7;
  const char* afp = c.ws + WS_FLAG + (unsigned)((8 * (c.ks & 1) + ((pi + (c.rot2 >> 1)) & 7)) * 16 + 8 * (c.ks >> 1) + 2 * c.ec + (lane & 1)) * FLAG_STRIDE;
  u64 fv = poll(afp, [&](u64 x) { return x == tag || (FINE && pi > 0); });
  STAMP(2);
  unsigned bseq = BSEQ;
#pragma unroll 1
  for (int s = 0; s < 2; ++s) { issueAB(c, s, bseq & 15, vL); ROTN(bseq, 5); }
  if (!__all(fv == tag || pi > 1)) fv = poll(afp, [&](u64 x) { return x == tag || pi > 1; });
  wait_vm<16>();
  bar();
  STAMP(3);
  issueAB(c, 2, nib(BSEQ, 2), vL);
  ROTN(bseq, 5); ROTN(bseq, 5);
  if (!__all(fv == tag)) {
    fv = ld8_async(afp);
    wait_vm<17>(); bar();
    wait_vm<0>();
    asm volatile("" : "+v"(fv));
    if (!__all(fv == tag)) poll(afp, [&](u64 x) { return x == tag; });
  } else { wait_vm<16>(); bar(); }
  issueAB(c, 4, bseq & 15, vL); ROTN(bseq, 5);
#pragma unroll 1
  for (int s = 1; s < 12; ++s) { if (s < 2) wait_vm<16>(); else wait_vm<32>(); bar(); issueAB(c, s + 4, bseq & 15, vL); ROTN(bseq, 5); }
#pragma unroll 1
  for (int s = 12; s < 26; ++s) { wait_vm<32>(); bar(); unit_step<false>(d2, v2, v2, (s - 12) * 131072, 8192, lb + (4 + (s & 3)) * SLOT, 4); }
  wait_vm<32>(); bar();
  wait_vm<16>(); bar();
  wait_vm<0>(); bar();
  STAMP_END(); BT_END();
}

DEV void helper(const Ctx& c) {
  BT_DECL;
  __builtin_amdgcn_s_setprio(HPRIO);
  const int hw = c.wid - 6, lane = fresh_lane();
  LAS char* lds = c.lds; char* ws = c.ws;
  const int q = lane >> 4, cc = lane & 15, qq = cc >> 2, cp = c.ks * 4 + c.ec;
  const unsigned dump = c.lb + LDS_DUMP;
  const char* Cp = (c.ks >> 1 ? c.Ci : c.Cr) + (size_t)(c.tp * 128 + c.ec * 32 + (lane & 31)) * 4096 + (c.ks & 1) * 2048 + (lane >> 5) * 128;
  auto prefC = [&](int n) { if (hw == 1) for (int i = 0; i < n; ++i) { touch(Cp, dump); Cp += 256; } };
  u64 tag;
  {
    const u64 tagv = __hip_atomic_load((GAS u64*)(ws + WS_EPOCH), __ATOMIC_RELAXED, __HIP_MEMORY_SCOPE_AGENT);
    const int row = c.tp * 128 + cp * 8 + 4 * hw + q, wrow = cp * 64 + c.tp * 4 + 2 * hw + (lane >> 5);
    const char* wp = (c.ks >> 1 ? c.Wi : c.Wr) + (size_t)(wrow & 511) * 1024 + (lane & 31) * 16;
    const char* p1 = c.w1 + (size_t)(c.tp * 32 + (lane >> 3)) * W1R + (c.PA + (c.two ? hw : 0)) * 128 + (lane & 7) * 16;
    char* pubp = cc == 0 ? ws + WS_ROWST + (size_t)row * 8 : ws + WS_CSW + (size_t)wrow * 8;
    const rsrc_t rP = mk_rsrc(ws + WS_CS1 + (size_t)(cp * 16 + c.tp) * 1024 + hw * 512, 512);
    u32x4 xv[4], dv[2], wv[4];
#pragma unroll
    for (int i = 0; i < 4; ++i) xv[i] = *(const u32x4*)(c.x + (size_t)row * 1024 + cc * 16 + i * 256);
    dv[0] = *(const u32x4*)wp; dv[1] = *(const u32x4*)(wp + 512);
#pragma unroll
    for (int i = 0; i < 4; ++i) wv[i] = *(const u32x4*)(p1 + i * (8 * W1R));
    const u16 b1v = *(const u16*)(c.b1 + ((c.PA + (c.two ? hw : 0)) * 64 + lane) * 2);
    const u16 b2v = *(const u16*)(c.b2 + (c.ec * 128 + (c.ks & 1) * 64 + lane) * 2);
#define CHUNK() do { SCHED(); rawbar(); SCHED(); } while (0)
    CHUNK();
    float s0 = 0.f, s1 = 0.f, sw = 0.f;
#pragma unroll
    for (int i = 0; i < 4; ++i) { s0 = dot2(xv[i].x, ONE2, s0); s1 = dot2(xv[i].y, ONE2, s1); s0 = dot2(xv[i].z, ONE2, s0); s1 = dot2(xv[i].w, ONE2, s1); }
#pragma unroll
    for (int i = 0; i < 8; ++i) sw = dot2(dv[i >> 2][i & 3], ONE2, sw);
    const float m = sum16(s0 + s1) * (1.f / 512.f);
    sw = sum16(sw);
    { const auto r = __builtin_amdgcn_permlane16_swap(__float_as_uint(sw), __float_as_uint(sw), false, false); sw = __uint_as_float(r[0]) + __uint_as_float(r[1]); }
    f32x4 z4 = {0.f, 0.f, 0.f, 0.f}, z5 = z4;
#pragma unroll
    for (int i = 0; i < 2; ++i) { const f32x4 d0 = unpk(xv[i].x, xv[i].y) - m, d1 = unpk(xv[i].z, xv[i].w) - m; z4 += d0 * d0; z5 += d1 * d1; }
    CHUNK();
#pragma unroll
    for (int i = 2; i < 4; ++i) { const f32x4 d0 = unpk(xv[i].x, xv[i].y) - m, d1 = unpk(xv[i].z, xv[i].w) - m; z4 += d0 * d0; z5 += d1 * d1; }
    z4 += z5;
    const float var = sum16((z4[0] + z4[1]) + (z4[2] + z4[3])) * (1.f / 512.f);
    const unsigned sv = pk_bf16(m, __builtin_amdgcn_sqrtf(rbf(rbf(var) + 1e-5f)));
    tag = uniform64(tagv) + 1;
    __builtin_amdgcn_s_waitcnt(0x0F70);
    if (cc < 2 && (cc == 0 || (q & 1) == 0))
      __hip_atomic_store((GAS u64*)pubp, ((u64)(unsigned)tag << 32) | (cc == 0 ? sv : __float_as_uint(sw)), __ATOMIC_RELAXED, __HIP_MEMORY_SCOPE_AGENT);
    CHUNK();
    prefC((PM1 >> 2) & 1);
    float t[8];
#pragma unroll
    for (int j = 0; j < 4; ++j) {
      t[2 * j] = (bflo(wv[0][j]) + bflo(wv[1][j])) + (bflo(wv[2][j]) + bflo(wv[3][j]));
      t[2 * j + 1] = (bfhi(wv[0][j]) + bfhi(wv[1][j])) + (bfhi(wv[2][j]) + bfhi(wv[3][j]));
    }
    *(LAS float*)(lds + LDS_B1 + (hw * 64 + lane) * 4) = bflo(b1v);
    *(LAS float*)(lds + LDS_B2 + lane * 4) = bflo(b2v);
    if (hw == 0 && lane == 0) *(LAS u64*)(lds + LDS_TAG) = tag;
    CHUNK();
    float u4[4], u2[2];
#pragma unroll
    for (int j = 0; j < 4; ++j) { const auto r = __builtin_amdgcn_permlane32_swap(__float_as_uint(t[j]), __float_as_uint(t[j + 4]), false, false); u4[j] = __uint_as_float(r[0]) + __uint_as_float(r[1]); }
#pragma unroll
    for (int j = 0; j < 2; ++j) { const auto r = __builtin_amdgcn_permlane16_swap(__float_as_uint(u4[j]), __float_as_uint(u4[j + 2]), false, false); u2[j] = dpp_add<0x128>(__uint_as_float(r[0]) + __uint_as_float(r[1])); }
    if (cc < 8) __builtin_amdgcn_raw_buffer_store_b128((u32x4){__float_as_uint(u2[0]), (unsigned)tag, __float_as_uint(u2[1]), (unsigned)tag}, rP, (cc * 4 + q) * 16, 0, 16);
    const char* stp = ws + WS_ROWST + (size_t)(c.tp * 128 + 64 * hw + 32 * (lane >> 5) + 8 * qq + 4 * (q & 1) + (lane & 3)) * 8;
    const char* cwp = ws + WS_CSW + (size_t)(cp * 64 + lane) * 8;
    wait_lgkm0();
    bar();
    u64 st = ld8_async(stp), cw = ld8_async(cwp);
    bar(); bar();
    const unsigned t32 = (unsigned)tag;
    wait_vm<0>();
    asm volatile("" : "+v"(st), "+v"(cw));
    const bool ok1 = __all((unsigned)(st >> 32) == t32 && (unsigned)(cw >> 32) == t32);
    if (!ok1) { st = ld8_async(stp); cw = ld8_async(cwp); }
    bar();
    if (!ok1) {
      wait_vm<0>();
      asm volatile("" : "+v"(st), "+v"(cw));
      if (!__all((unsigned)(st >> 32) == t32)) st = poll(stp, [&](u64 x) { return (unsigned)(x >> 32) == t32; });
      if (!__all((unsigned)(cw >> 32) == t32)) cw = poll(cwp, [&](u64 x) { return (unsigned)(x >> 32) == t32; });
    }
    *(LAS float*)(lds + LDS_MEAN + hw * 256 + lane * 4) = bflo((unsigned)st);
    *(LAS float*)(lds + LDS_RSTD + hw * 256 + lane * 4) = __builtin_amdgcn_rcpf(bfhi((unsigned)st));
    *(LAS unsigned*)(lds + LDS_CSW + lane * 4) = (unsigned)cw;
    const char* pvp = ws + WS_CS1 + (size_t)(cp * 16 + (lane >> 5)) * 1024 + hw * 512 + (lane & 31) * 16;
    u32x4 pv[8];
#pragma unroll
    for (int i = 0; i < 8; ++i) pv[i] = ld16_async(pvp + i * 2048);
    wait_lgkm0();
    bar();
    const u64 t0 = __builtin_amdgcn_s_memrealtime();
    for (;;) {
      wait_vm<0>();
      asm volatile("" : "+v"(pv[0]), "+v"(pv[1]), "+v"(pv[2]), "+v"(pv[3]), "+v"(pv[4]), "+v"(pv[5]), "+v"(pv[6]), "+v"(pv[7]));
      bool ok = true;
#pragma unroll
      for (int i = 0; i < 8; ++i) ok = ok && pv[i].y == t32 && pv[i].w == t32;
      if (__all(ok) || __builtin_amdgcn_s_memrealtime() - t0 > 20000000ull) break;
#pragma unroll
      for (int i = 0; i < 8; ++i) pv[i] = ld16_async(pvp + i * 2048);
    }
    float a0 = 0.f, a1 = 0.f;
#pragma unroll
    for (int i = 0; i < 8; ++i) { a0 += __uint_as_float(pv[i].x); a1 += __uint_as_float(pv[i].z); }
    auto r = __builtin_amdgcn_permlane32_swap(__float_as_uint(a0), __float_as_uint(a0), false, false);
    a0 = __uint_as_float(r[0]) + __uint_as_float(r[1]);
    r = __builtin_amdgcn_permlane32_swap(__float_as_uint(a1), __float_as_uint(a1), false, false);
    a1 = __uint_as_float(r[0]) + __uint_as_float(r[1]);
    *(LAS f32x2*)(lds + LDS_CS1 + hw * 256 + (lane & 31) * 8) = (f32x2){a0, a1};
    wait_lgkm0();
  }
  bar();
  if (hw == 0 && lane == 0) set_flag(ws, 0, c.tp * 16 + cp, tag);
  bar();
  prefC(PMF);
  {
    const char* Wp = c.w2 + (c.ec * 128 + (c.ks & 1) * 64) * 2 + (size_t)((c.tp & 7) * 224 + hw * 112 + (lane < 56 ? lane : 0)) * 1024;
    const bool ew = blockIdx.x == 0 && hw == 1;
    const char* fp = ws + WS_FLAG + (unsigned)lane * FLAG_STRIDE;
    u64 f0 = 0, f1 = 0, f2 = 0, f3 = 0;
    auto run = [&](int s0, int s1) {
#pragma unroll 1
      for (int s = s0; s < s1; ++s) {
        if (PREFW && s == PREFW) { touch(Wp, dump); touch(Wp + 56 * 1024, dump); }
        prefC((PM2 >> (s + 1)) & 1);
        bar();
        if (hw == 0 && lane == 0) {
          if (s == HFLAG) set_flag(ws, 1, c.tp * 16 + cp, tag);
          if (s == YFLAG) set_flag(ws, 2, (c.tp * 4 + c.ec) * 4 + c.ks, tag);
        }
      }
    };
    run(-1, 10);
    if (ew) { f0 = ld8_async(fp); f1 = ld8_async(fp + 64 * FLAG_STRIDE); f2 = ld8_async(fp + 128 * FLAG_STRIDE); f3 = ld8_async(fp + 192 * FLAG_STRIDE); }
    run(10, 15);
    wait_vm<0>();
    asm volatile("" : "+v"(f0), "+v"(f1), "+v"(f2), "+v"(f3));
    const bool early = ew && __all(f0 == tag && f1 == tag && f2 == tag && f3 == tag);
    if (early && lane == 0) __hip_atomic_store((GAS u64*)(ws + WS_EPOCH), tag, __ATOMIC_RELAXED, __HIP_MEMORY_SCOPE_AGENT);
    run(15, YPOLL + 1);
    const char* yfp = ws + WS_FLAG + (unsigned)(2 * 256 + (c.tp * 4 + c.ec) * 4 + (lane & 3)) * FLAG_STRIDE;
    f0 = ld8_async(yfp);
    run(YPOLL + 1, YPOLL + 2);
    f1 = ld8_async(yfp);
    run(YPOLL + 2, YCHK);
    wait_vm<1>();
    asm volatile("" : "+v"(f0));
    if (!__all(f0 == tag)) {
      wait_vm<0>();
      asm volatile("" : "+v"(f1));
      if (!__all(f1 == tag)) poll(yfp, [&](u64 x) { return x == tag; });
    }
    run(YCHK, 29);
    wait_vm<0>();
    asm volatile("" : "+v"(f1));
    if (ew && !early) {
#pragma unroll 1
      for (int i = 0; i < 4; ++i) poll(fp + i * 64 * FLAG_STRIDE, [&](u64 x) { return x == tag; });
      if (lane == 0) __hip_atomic_store((GAS u64*)(ws + WS_EPOCH), tag, __ATOMIC_RELAXED, __HIP_MEMORY_SCOPE_AGENT);
    }
    wait_vm<0>();
  }
  BT_END();
}

DEV void consumer2(const Ctx& c DBG_PARAM BT_PARAM) {
  const int w = c.wid, lane = fresh_lane();
  LAS char* lds = c.lds;
  const int q = lane >> 4, cc = lane & 15, qq = cc >> 2, pp = lane & 3;
  const unsigned wa0 = cc * 128 + ((q ^ (cc & 7)) * 16), wa1 = cc * 128 + (((4 + q) ^ (cc & 7)) * 16);
  const int wt = w >> 1, we = w & 1;
  unsigned wb[2];
#pragma unroll
  for (int n = 0; n < 2; ++n) wb[n] = (4 * q + qq) * 128 + (((2 * (2 * we + n) + (pp >> 1)) ^ ((4 * q + qq) & 6)) * 16) + 8 * (pp & 1);
  const unsigned hb = wt * 2048 + lane * 16;
  bf16x8 hA[4][2], wA[4][2], hB[4][2], wB[4][2];
  wait_lgkm0();
  bar();
  STAMP(5);
#define PREAD(HN, WN, SO, SW) if (i < 12) { if (i % 3 == 2) { HN[i / 3][0] = ldsb128(lds + (SO) + hb + (i / 3) * 4096); HN[i / 3][1] = ldsb128(lds + (SO) + hb + (i / 3) * 4096 + 1024); } \
                                            else WN[i / 3][i % 3] = ldstr(lds + (SW) + wb[i % 3] + (i / 3) * 4096, 2048); SCHED(); }
  {
    f32x4 acc[4][4];
#pragma unroll
    for (int m = 0; m < 4; ++m)
#pragma unroll
      for (int n = 0; n < 4; ++n) acc[m][n] = (f32x4){0, 0, 0, 0};
    const unsigned a0 = wt * 8192 + wa0, a1 = wt * 8192 + wa1, b0 = we * 4096 + lane * 16;
    bf16x8 fa0[4], fb0[4], fa1[4], fb1[4];
#pragma unroll
    for (int m = 0; m < 4; ++m) { fa0[m] = ldsb128(lds + nib(CSEQ, 0) * SLOT + a0 + m * 2048); fb0[m] = ldsb128(lds + nib(BSEQ, 0) * SLOT + b0 + m * 1024); }
#define HALF(FA, FB, NA_, NB_, AOFF, BOFF) do { _Pragma("unroll") for (int i = 0; i < 16; ++i) { \
      acc[i >> 2][i & 3] = MFMA(FB[i & 3], FA[i >> 2], acc[i >> 2][i & 3]); SCHED(); \
      if (i < 8) { if (i == 0) NB_[0] = ldsb128(lds + (BOFF)); else if (i == 1) NA_[0] = ldsb128(lds + (AOFF)); \
        else if (i < 5) NB_[i - 1] = ldsb128(lds + (BOFF) + (i - 1) * 1024); else NA_[i - 4] = ldsb128(lds + (AOFF) + (i - 4) * 2048); SCHED(); } } } while (0)
    unsigned so = nib(CSEQ, 0) * SLOT, sb = nib(BSEQ, 0) * SLOT, cseq = CSEQ, bseq = BSEQ;
#pragma unroll 1
    for (int seg = 0; seg < 2; ++seg) {
      const int n = seg ? 15 - HFLAG : HFLAG;
#pragma unroll 1
      for (int s = 0; s < n; ++s) {
        HALF(fa0, fb0, fa1, fb1, so + a1, sb + b0 + 8192);
        wait_lgkm0();
        bar();
        ROTN(bseq, 5); ROTN(cseq, 4); sb = (bseq & 15) * SLOT; so = (cseq & 15) * SLOT;
        HALF(fa1, fb1, fa0, fb0, so + a0, sb + b0);
      }
      wait_vm<0>();
    }
    HALF(fa0, fb0, fa1, fb1, so + a1, sb + b0 + 8192);
    wait_lgkm0();
    bar();
#pragma unroll
    for (int i = 0; i < 16; ++i) {
      acc[i >> 2][i & 3] = MFMA(fb1[i & 3], fa1[i >> 2], acc[i >> 2][i & 3]); SCHED();
      PREAD(hA, wA, nib(CSEQ, 0) * SLOT, nib(WSEQ, 0) * SLOT)
    }
    STAMP(6);
    if (w == c.ks) {
#pragma unroll
      for (int m = 0; m < 4; ++m)
#pragma unroll
        for (int n = 0; n < 4; ++n) *(LAS f32x4*)(lds + 8 * SLOT + (m * 4 + n) * 1024 + lane * 16) = acc[m][n];
    } else {
      const rsrc_t rY = mk_rsrc(c.ws + WS_YP + (size_t)(((c.tp * 4 + c.ec) * 4 + c.ks) * 4 + w) * 8192, 8192);
#pragma unroll
      for (int m = 0; m < 4; ++m)
#pragma unroll
        for (int j = 0; j < 2; ++j) {
          const f32x4 e = acc[m][2 * j], o = acc[m][2 * j + 1];
          __builtin_amdgcn_raw_buffer_store_b128((u32x4){pk_bf16(e[0], e[1]), pk_bf16(e[2], e[3]), pk_bf16(o[0], o[1]), pk_bf16(o[2], o[3])}, rY, lane * 16 + (m * 2 + j) * 1024, 0, 16);
        }
    }
  }
  STAMP(7);
  {
    f32x4 acc[2][2];
#pragma unroll
    for (int m = 0; m < 2; ++m) { acc[m][0] = (f32x4){0, 0, 0, 0}; acc[m][1] = (f32x4){0, 0, 0, 0}; }
    unsigned cseq = CSEQ, wseq = WSEQ;
#define PSTEP(HC, WC, HN, WN) do { wait_lgkm0(); bar(); ROTN(cseq, 4); ROTN(wseq, 4); const unsigned so = (cseq & 15) * SLOT, sw = (wseq & 15) * SLOT; SCHED(); \
      _Pragma("unroll") for (int i = 0; i < 16; ++i) { \
        acc[(i >> 1) & 1][i & 1] = MFMA(WC[i >> 2][i & 1], HC[i >> 2][(i >> 1) & 1], acc[(i >> 1) & 1][i & 1]); SCHED(); \
        PREAD(HN, WN, so, sw) } } while (0)
    static_assert(YFLAG == 18 && YCHK == 25, "schedule");
#pragma unroll 1
    for (int seg = 0; seg < 2; ++seg) {
      const int n = seg ? 4 : 1;
#pragma unroll 1
      for (int s = 0; s < n; ++s) { PSTEP(hA, wA, hB, wB); PSTEP(hB, wB, hA, wA); }
      wait_vm<0>();
    }
    const rsrc_t rY = mk_rsrc(c.ws + WS_YP + (size_t)((c.tp * 4 + c.ec) * 16 + c.ks) * 8192, 4 * 32768 - 3 * 8192);
    u32x4 yp[3][2];
    const unsigned yv = lane * 16 + (4 * wt + we) * 1024;
#pragma unroll
    for (int i = 0; i < 3; ++i) yp[i][0] = __builtin_amdgcn_raw_buffer_load_b128(rY, yv, ((c.ks + 1 + i) & 3) * 32768, 16);
    SCHED();
    PSTEP(hA, wA, hB, wB);
#pragma unroll
    for (int i = 0; i < 3; ++i) yp[i][1] = __builtin_amdgcn_raw_buffer_load_b128(rY, yv + 2048, ((c.ks + 1 + i) & 3) * 32768, 16);
    SCHED();
    PSTEP(hB, wB, hA, wA);
    STAMP(8);
    const int tl = 32 * wt + 8 * qq + (cc & 3), el = 32 * we + 4 * q;
    f32x4 y[2][2], bv[2];
    u32x2 xr[2][2];
#pragma unroll
    for (int n = 0; n < 2; ++n) {
      bv[n] = *(const LAS f32x4*)(lds + LDS_B2 + (el + 16 * n) * 4);
#pragma unroll
      for (int m = 0; m < 2; ++m) {
        y[m][n] = *(const LAS f32x4*)(lds + 8 * SLOT + ((2 * wt + m) * 4 + 2 * we + n) * 1024 + lane * 16);
        xr[m][n] = *(const LAS u32x2*)(lds + LDS_XT + (tl + 4 * m) * 128 + (((el + 16 * n) * 2) ^ ((2 * qq + ((cc >> 1) & 1)) * 16)));
      }
    }
    PSTEP(hA, wA, hB, wB);
#pragma unroll
    for (int m = 0; m < 2; ++m)
#pragma unroll
      for (int i = 0; i < 3; ++i) { y[m][0] += unpk(yp[i][m].x, yp[i][m].y); y[m][1] += unpk(yp[i][m].z, yp[i][m].w); }
    char* op = c.out + (size_t)(c.tp * 128 + (c.ks >> 1) * 64 + tl) * 1024 + (c.ec * 128 + (c.ks & 1) * 64 + el) * 2;
    SCHED();
#pragma unroll
    for (int i = 0; i < 16; ++i) acc[i >> 3][(i >> 2) & 1] = MFMA(wB[i & 3][(i >> 2) & 1], hB[i & 3][i >> 3], acc[i >> 3][(i >> 2) & 1]);
    STAMP(9);
#pragma unroll
    for (int m = 0; m < 2; ++m)
#pragma unroll
      for (int n = 0; n < 2; ++n) {
        f32x4 o = unpk(xr[m][n].x, xr[m][n].y);
#pragma unroll
        for (int r = 0; r < 4; ++r) { const float z = acc[m][n][r] + bv[n][r]; o[r] += y[m][n][r] * (z * __builtin_amdgcn_rcpf(1.f + __expf(-z))); }
        *(u32x2*)(op + m * 4096 + n * 32) = (u32x2){pk_bf16(o[0], o[1]), pk_bf16(o[2], o[3])};
      }
    STAMP(10);
    STAMP_END(); BT_END();
  }
}

DEV void consumer(const Ctx& c) {
  DBG_DECL; BT_DECL;
  const int w = c.wid;
  LAS char* lds = c.lds;
  STAMP(0);
  unsigned ja[4], bx0, bx1, bw0, bw1;
  {
    const int lane = fresh_lane(), q = lane >> 4, cc = lane & 15, qq = cc >> 2, pp = lane & 3;
    if (PREFI) touch(c.pc + (w * 64 + lane < PREFI ? w * 64 + lane : 0) * 128, c.lb + LDS_DUMP);
    const unsigned wa0 = cc * 128 + ((q ^ (cc & 7)) * 16), wa1 = cc * 128 + (((4 + q) ^ (cc & 7)) * 16);
#pragma unroll
    for (int n = 0; n < 4; ++n) ja[n] = opq(4 * SLOT + (8 * q + qq) * 128 + (((2 * n + (pp >> 1)) ^ ((qq & 2) | ((q & 1) << 2))) * 16) + 8 * (pp & 1));
    bx0 = opq(wa0 + w * 4096); bx1 = opq(wa1 + w * 4096); bw0 = opq(wa0 + 4 * SLOT); bw1 = opq(wa1 + 4 * SLOT);
  }
  bf16x8 xf[2][16];
  bar();
  STAMP(1);
  f32x4 ach[4][2];
  {
    f32x4 acc[4][2];
#pragma unroll
    for (int n = 0; n < 4; ++n) { acc[n][0] = acc[n][1] = ach[n][0] = ach[n][1] = (f32x4){0, 0, 0, 0}; }
    bf16x8 fr[128];
    auto rd = [&](auto t_) {
      constexpr int t = decltype(t_)::value, kk = t / 8, n = t % 8, so = ((kk >> 1) & 3) * SLOT;
      if constexpr (n == 0) { xf[0][kk] = ldsb128(lds + ((kk & 1) ? bx1 : bx0) + so); xf[1][kk] = ldsb128(lds + ((kk & 1) ? bx1 : bx0) + so + 2048); }
      if constexpr ((n & 1) == 0) fr[t] = ldsb128(lds + ((kk & 1) ? bw1 : bw0) + so + (n >> 1) * 2048);
      else fr[t] = ldstr(lds + ja[n >> 1] + so + 8192 + (kk & 1) * 4096, 512);
    };
    static_for<0, 8>(rd);
    SCHED();
    static_for<0, 128>([&](auto p_) {
      constexpr int p = decltype(p_)::value, kk = p / 8, n = p % 8;
      constexpr int kn = p / 16 + 1;
      if constexpr (p == 16 * kn - lead1(kn)) { wait_lgkm0(); bar(); SCHED(); }
      if constexpr ((n & 1) == 0) acc[n >> 1][0] = MFMA(xf[0][kk], fr[p], acc[n >> 1][0]); else ach[n >> 1][0] = MFMA(fr[p], xf[0][kk], ach[n >> 1][0]);
      SCHED();
      static_for<p + 1, p + 16>([&](auto t_) {
        constexpr int t = decltype(t_)::value;
        if constexpr (t < 128 && t >= 8 && sched1(t) == p) { rd(t_); SCHED(); }
      });
      if constexpr ((n & 1) == 0) acc[n >> 1][1] = MFMA(xf[1][kk], fr[p], acc[n >> 1][1]); else ach[n >> 1][1] = MFMA(fr[p], xf[1][kk], ach[n >> 1][1]);
      SCHED();
    });
    STAMP(2);
    const int lane = fresh_lane(), q = lane >> 4, cc = lane & 15;
    f32x4 mr[2], rr[2];
    const float sgn = (c.ks >> 1) ? -1.f : 1.f;
#pragma unroll
    for (int u = 0; u < 2; ++u) {
      mr[u] = *(const LAS f32x4*)(lds + LDS_MEAN + ((2 * w + u) * 16 + 4 * q) * 4);
      rr[u] = *(const LAS f32x4*)(lds + LDS_RSTD + ((2 * w + u) * 16 + 4 * q) * 4) * sgn;
    }
    float cs[4];
#pragma unroll
    for (int n = 0; n < 4; ++n) cs[n] = *(const LAS float*)(lds + LDS_CSW + (n * 16 + cc) * 4);
    const rsrc_t rA = mk_rsrc(c.ws + WS_ABT + (size_t)((((c.ks * 2 + (c.ec >> 1)) * 64 + c.tp * 4 + w) * 8 + (c.ec & 1) * 4) * 1024), 4096);
#pragma unroll
    for (int n = 0; n < 4; ++n) {
      const f32x4 o0 = (acc[n][0] - mr[0] * cs[n]) * rr[0], o1 = (acc[n][1] - mr[1] * cs[n]) * rr[1];
      u32x4 d = {pk_bf16(o0[0], o0[1]), pk_bf16(o0[2], o0[3]), pk_bf16(o1[0], o1[1]), pk_bf16(o1[2], o1[3])};
      __builtin_amdgcn_raw_buffer_store_b128(d, rA, lane * 16 + n * 1024, 0, 16);
    }
  }
  SCHED();
  STAMP(3);
  if (c.two) {
    static_assert(RPOS >= 16 && RPOS + LOOK2 <= 32 && FPOS < RPOS, "R follows the last use of J0 and precedes the first read of J2");
    bf16x8 g[64];
    f32x4 ac2[4][2];
    auto rd2 = [&](auto t_) {
      constexpr int t = decltype(t_)::value;
      g[t] = ldstr(lds + (nib(JSEQ, t / 16) - 4) * SLOT + ja[t & 3] + ((t >> 2) & 3) * 4096, 512);
    };
    static_for<0, LOOK2>(rd2);
#pragma unroll
    for (int n = 0; n < 4; ++n) { ac2[n][0] = (f32x4){0, 0, 0, 0}; ac2[n][1] = (f32x4){0, 0, 0, 0}; }
    SCHED();
    static_for<0, 64>([&](auto p_) {
      constexpr int p = decltype(p_)::value, kk = p / 4, n = p & 3;
      if constexpr (p == FPOS) { wait_vm<0>(); bar(); SCHED(); }
      if constexpr (p == RPOS) { bar(); SCHED(); }
      ac2[n][0] = MFMA(g[p], xf[0][kk], ac2[n][0]);
      SCHED();
      if constexpr (p + LOOK2 < 64) { rd2(IC<p + LOOK2>{}); SCHED(); }
      ac2[n][1] = MFMA(g[p], xf[1][kk], ac2[n][1]);
      SCHED();
    });
    STAMP(11);
    h_epilogue<2>(c, ac2, 64, w);
  } else {
    wait_vm<0>(); bar(); bar();
    STAMP(11);
  }
  h_epilogue<2>(c, ach, 0, w);
  STAMP(4);
  consumer2(c DBG_ARG BT_ARG);
}

__global__ __launch_bounds__(512, 2) void fnet_mlp_fused(const char* x, const char* Cr, const char* Ci, const char* Wr, const char* Wi,
                                                         const char* w1, const char* b1, const char* w2, const char* b2, char* out, char* ws) {
  extern __shared__ __attribute__((aligned(16))) char shm[];
  Ctx c;
  { u64 pc; asm volatile("s_getpc_b64 %0" : "=s"(pc)); c.pc = (const char*)(pc & ~(u64)255); }
  c.x = x; c.Cr = Cr; c.Ci = Ci; c.Wr = Wr; c.Wi = Wi; c.w1 = w1; c.b1 = b1; c.w2 = w2; c.b2 = b2; c.out = out; c.ws = ws;
  c.lds = (LAS char*)shm; c.lb = (unsigned)(size_t)c.lds;
  c.wid = __builtin_amdgcn_readfirstlane(threadIdx.x >> 6);
  const int b = blockIdx.x, xcd = b & 7, r = b >> 3;
  c.ks = xcd >> 1; c.tp = (xcd & 1) * 8 + (r >> 2); c.ec = r & 3;
  c.two = c.ec != 3;
  c.PA = 7 * c.ks + 2 * c.ec;
  c.rot1 = (ROT & 1) ? (2 * c.ec + (r >> 2)) & 7 : 0; c.rot2 = (ROT & 2) ? (4 * c.ec + 2 * (r >> 2)) & 15 : 0;
  if (c.wid == 4) loaderA(c); else if (c.wid == 5) loaderB(c); else if (c.wid >= 6) helper(c); else consumer(c);
}

extern "C" void kernel_launch(void* const* d_in, const int* in_sizes, int n_in, void* d_out, int out_size, void* d_ws, size_t ws_size, hipStream_t stream) {
  (void)hipFuncSetAttribute((const void*)fnet_mlp_fused, hipFuncAttributeMaxDynamicSharedMemorySize, LDS_BYTES);
  fnet_mlp_fused<<<256, 512, LDS_BYTES, stream>>>((const char*)d_in[0], (const char*)d_in[1], (const char*)d_in[2], (const char*)d_in[3], (const char*)d_in[4],
                                                  (const char*)d_in[6], (const char*)d_in[7], (const char*)d_in[8], (const char*)d_in[9], (char*)d_out, (char*)d_ws);
}
```

```cpp
#define ROT2M 2
#define PSEQ 0x73620u
#define PM1 4
#define PMF 1
#define PM2 0xD
#include <hip/hip_runtime.h>
typedef unsigned short u16;
typedef unsigned long long u64;
typedef short bf16x8 __attribute__((ext_vector_type(8)));
typedef short s16x4 __attribute__((ext_vector_type(4)));
typedef float f32x4 __attribute__((ext_vector_type(4)));
typedef float f32x2 __attribute__((ext_vector_type(2)));
typedef unsigned u32x4 __attribute__((ext_vector_type(4)));
typedef unsigned u32x2 __attribute__((ext_vector_type(2)));
typedef __bf16 bf2 __attribute__((ext_vector_type(2)));
#define LAS __attribute__((address_space(3)))
#define GAS __attribute__((address_space(1)))
#define DEV __device__ __forceinline__

constexpr int S_ = 2048, D_ = 512, F_ = 1792, W1R = F_ * 2;
constexpr unsigned WS_EPOCH = 0, WS_FLAG = 4096, FLAG_STRIDE = 128, WS_ROWST = 256u << 10, WS_CSW = 288u << 10, WS_CS1 = 512u << 10;
constexpr unsigned WS_ABT = 1u << 20, WS_H = 8u << 20, WS_YP = 16u << 20, WS_BYTES = 32u << 20;
constexpr int SLOT = 16384, NSLOT = 9;
constexpr int LDS_XT = NSLOT * SLOT;
constexpr int LDS_MEAN = LDS_XT + 8192, LDS_RSTD = LDS_MEAN + 512, LDS_CSW = LDS_RSTD + 512, LDS_CS1 = LDS_CSW + 256;
constexpr int LDS_B1 = LDS_CS1 + 512, LDS_B2 = LDS_B1 + 512, LDS_TAG = LDS_B2 + 256;
constexpr int LDS_DUMP = LDS_TAG + 32, LDS_BYTES = LDS_DUMP + 256;
static_assert(LDS_BYTES <= 160 * 1024, "LDS");
#define ROT 1
#define PREFI 240
#define PREFW 8
#define DMAU 4
#define STLD 3
#define LT 0
#define LT0 2
#define ROTM 2
#define CSTART 0
#define WSP 0
#define EARLYP2 0
#define LW 0
#define HSPLIT 2
#define HPRIO 3
#define FINE 1
#define NINIT 3
#define LEADE 7
#define LOOKE 14
#define LEADL 4
#define LOOKL 8
#define FPOS 14
#define RPOS 16
#define LOOK2 8
#define YFL 18
constexpr int HFLAG = 3, YFLAG = YFL, YPOLL = 22, YCHK = 25;
constexpr unsigned JSEQ = 0x6548u, CSEQ = 0x3210u, BSEQ = 0x65487u, WSEQ = 0x7654u;
constexpr int nib(unsigned seq, int i) { return (seq >> (4 * i)) & 15; }

template <int N> DEV void wait_vm() { asm volatile("s_waitcnt vmcnt(%0)" ::"n"(N) : "memory"); }
DEV void wait_lgkm0() { asm volatile("s_waitcnt lgkmcnt(0)" ::: "memory"); }
DEV void bar() { asm volatile("" ::: "memory"); __builtin_amdgcn_s_barrier(); asm volatile("" ::: "memory"); }
#define rawbar() __builtin_amdgcn_s_barrier()
#define BT_DECL
#define BT_END() do {} while (0)
#define BT_PARAM
#define BT_ARG
#define SCHED() __builtin_amdgcn_sched_barrier(0)
#define MFMA(a, b, c) __builtin_amdgcn_mfma_f32_16x16x32_bf16((a), (b), (c), 0, 0, 0)
DEV unsigned pk_bf16(float a, float b) { f32x2 f = {a, b}; return __builtin_bit_cast(unsigned, __builtin_convertvector(f, bf2)); }
DEV float rbf(float a) { return __uint_as_float(pk_bf16(a, 0.f) << 16); }
DEV float bflo(unsigned v) { return __uint_as_float(v << 16); }
DEV float bfhi(unsigned v) { return __uint_as_float(v & 0xffff0000u); }
DEV f32x4 unpk(unsigned a, unsigned b) { return (f32x4){bflo(a), bfhi(a), bflo(b), bfhi(b)}; }
constexpr unsigned ONE2 = 0x3f803f80u;
DEV float dot2(unsigned a, unsigned b, float c) { return __builtin_amdgcn_fdot2_f32_bf16(__builtin_bit_cast(bf2, a), __builtin_bit_cast(bf2, b), c, false); }
DEV bf16x8 ldsb128(LAS char* p) { return *(const LAS bf16x8*)p; }
DEV bf16x8 ldstr(LAS char* p, int second) {
  s16x4 a = __builtin_amdgcn_ds_read_tr16_b64_v4i16((LAS s16x4*)p);
  s16x4 b = __builtin_amdgcn_ds_read_tr16_b64_v4i16((LAS s16x4*)(p + second));
  return __builtin_shufflevector(a, b, 0, 1, 2, 3, 4, 5, 6, 7);
}
DEV unsigned opq(unsigned v) { asm volatile("" : "+v"(v)); return v; }
DEV int fresh_lane() { int l; asm volatile("v_mbcnt_lo_u32_b32 %0, -1, 0\n\tv_mbcnt_hi_u32_b32 %0, -1, %0" : "=v"(l)); return l; }
typedef __amdgpu_buffer_rsrc_t rsrc_t;
DEV rsrc_t mk_rsrc(const void* p, unsigned bytes) { return __builtin_amdgcn_make_buffer_rsrc((void*)p, 0, bytes, 0x00020000); }
DEV u32x4 mk_desc(const void* p, unsigned bytes) { const u64 a = (u64)p; return (u32x4){(unsigned)a, (unsigned)(a >> 32) & 0xffffu, bytes, 0x00020000u}; }
#define DMA4(POL) asm volatile("s_mov_b32 m0, %9\n\ts_nop 0\n\tbuffer_load_dwordx4 %0, %4, %5 offen " POL "lds\n\tbuffer_load_dwordx4 %1, %4, %6 offen offset:1024 " POL "lds\n\t" \
    "buffer_load_dwordx4 %2, %4, %7 offen offset:2048 " POL "lds\n\tbuffer_load_dwordx4 %3, %4, %8 offen offset:3072 " POL "lds" \
    ::"v"(v0), "v"(v1), "v"(v2), "v"(v3), "s"(desc), "s"(s0), "s"(s1 - 1024), "s"(s2 - 2048), "s"(s3 - 3072), "s"(dst) : "memory")
template <bool DEVSCOPE> DEV void dma4(u32x4 desc, unsigned v0, unsigned v1, unsigned v2, unsigned v3, unsigned s0, unsigned s1, unsigned s2, unsigned s3, unsigned dst) {
  if constexpr (DEVSCOPE) DMA4("sc1 "); else DMA4("");
}
DEV u32x4 ld16_async(const char* p) { u32x4 v; asm volatile("global_load_dwordx4 %0, %1, off sc1" : "=v"(v) : "v"(p) : "memory"); return v; }
DEV u32x4 ld16_plain(const char* p) { u32x4 v; asm volatile("global_load_dwordx4 %0, %1, off" : "=v"(v) : "v"(p) : "memory"); return v; }
DEV unsigned ld2_plain(const char* p) { unsigned v; asm volatile("global_load_ushort %0, %1, off" : "=v"(v) : "v"(p) : "memory"); return v; }
DEV u64 ld8_async(const char* p) { u64 v; asm volatile("global_load_dwordx2 %0, %1, off sc1" : "=v"(v) : "v"(p) : "memory"); return v; }
DEV void touch(const char* p, unsigned dump) {
  unsigned keep;
  asm volatile("s_mov_b32 %0, m0\n\ts_mov_b32 m0, %2\n\ts_nop 0\n\tglobal_load_lds_dword %1, off\n\ts_mov_b32 m0, %0" : "=&s"(keep) : "v"(p), "s"(dump) : "memory");
}
template <int CTRL> DEV float dpp_add(float x) { return x + __builtin_bit_cast(float, __builtin_amdgcn_update_dpp(0, __builtin_bit_cast(int, x), CTRL, 0xf, 0xf, false)); }
DEV float sumq(float x) {
  auto r = __builtin_amdgcn_permlane16_swap(__float_as_uint(x), __float_as_uint(x), false, false);
  x = __uint_as_float(r[0]) + __uint_as_float(r[1]);
  r = __builtin_amdgcn_permlane32_swap(__float_as_uint(x), __float_as_uint(x), false, false);
  return __uint_as_float(r[0]) + __uint_as_float(r[1]);
}
DEV float sum16(float x) { x = dpp_add<0xB1>(x); x = dpp_add<0x4E>(x); x = dpp_add<0x141>(x); return dpp_add<0x140>(x); }

#define STAMP(i) do {} while (0)
#define STAMP_END() do {} while (0)
#define DBG_DECL
#define DBG_PARAM
#define DBG_ARG

template <int N> struct IC { static constexpr int value = N; };
template <int B, int E, class F> DEV void static_for(F&& f) { if constexpr (B < E) { f(IC<B>{}); static_for<B + 1, E>(f); } }
constexpr int lead1(int k) { return k >= 6 ? LEADL : LEADE; }
constexpr int sched1(int t) { const int k = t / 16, e0 = t - (k >= 6 ? LOOKL : LOOKE), e1 = 16 * k - lead1(k) + (t - 16 * k) / 2; return k == 0 ? (t - 8 > 0 ? (t - 8) / 2 : 0) : (e0 > e1 ? e0 : e1); }

struct Ctx {
  const char *x, *Cr, *Ci, *Wr, *Wi, *w1, *b1, *w2, *b2; char* out; char* ws; LAS char* lds; const char* pc; unsigned lb;
  int wid, tp, ec, ks;
  int PA, rot1, rot2;
  bool two;
};

template <class P> DEV u64 poll(const char* p, P ok) {
  const u64 t0 = __builtin_amdgcn_s_memrealtime();
  u64 v;
  for (;;) {
    v = __hip_atomic_load((GAS u64*)p, __ATOMIC_RELAXED, __HIP_MEMORY_SCOPE_AGENT);
    if (__all(ok(v))) break;
    if (__builtin_amdgcn_s_memrealtime() - t0 > 20000000ull) break;
    __builtin_amdgcn_s_sleep(1);
  }
  return v;
}
DEV void set_flag(char* ws, int kind, int idx, u64 tag) {
  __hip_atomic_store((GAS u64*)(ws + WS_FLAG + (unsigned)(kind * 256 + idx) * FLAG_STRIDE), tag, __ATOMIC_RELAXED, __HIP_MEMORY_SCOPE_AGENT);
}
DEV u64 uniform64(u64 v) {
  const unsigned lo = __builtin_amdgcn_readfirstlane((unsigned)v), hi = __builtin_amdgcn_readfirstlane((unsigned)(v >> 32));
  return ((u64)hi << 32) | lo;
}
#define PROW(p) (32 * ((p) >> 2) + 16 * ((p) & 1) + 4 * (((p) >> 1) & 1))
#define ROTN(seq, n) do { seq = (seq >> 4) | ((seq & 15) << (4 * (n) - 4)); } while (0)

template <int U = DMAU> DEV void unit_rows(u32x4 d, unsigned v, unsigned so, unsigned rowb, unsigned dst) {
#pragma unroll U
  for (int g = 0; g < 4; ++g) {
    dma4<false>(d, v, v, v, v, so, so + PROW(1) * rowb, so + PROW(2) * rowb, so + PROW(3) * rowb, dst);
    so += 32 * rowb; dst += 4096;
  }
}
template <bool DS, int U = DMAU> DEV void unit_step(u32x4 d, unsigned v0, unsigned v1, unsigned so, unsigned step, unsigned dst, int n4) {
#pragma unroll U
  for (int g = 0; g < n4; ++g) {
    dma4<DS>(d, v0, v1, v0, v1, so, so + step, so + 2 * step, so + 3 * step, dst);
    so += 4 * step; dst += 4096;
  }
}
DEV void issueAB(const Ctx& c, int s, int slot, unsigned vL) {
  const u32x4 d = mk_desc(c.ws + WS_ABT + (size_t)(((c.ks >> 1) * 4 + c.ec) * 64 + (c.ks & 1) * 32) * 8192, 262144);
  unit_step<true>(d, vL, vL, ((s + c.rot2) & 15) * 16384, 1024, c.lb + slot * SLOT, 4);
}
DEV void issueJ(const Ctx& c, int j, unsigned vJ) {
  const u32x4 d = mk_desc(c.w1, D_ * W1R);
#pragma unroll
  for (int h = 0; h < 2; ++h) unit_step<false>(d, vJ, vJ ^ 64, ((2 * j + h + c.rot1) & 7) * (64 * W1R) + (c.PA + (c.two ? 1 : 0)) * 128, 8 * W1R, c.lb + ((JSEQ >> (4 * j)) & 15) * SLOT + h * 8192, 2);
}
template <int NB> DEV void h_epilogue(const Ctx& c, const f32x4 (&acc)[2 * NB][2], int col0, int rg) {
  LAS char* lds = c.lds;
  const int lane = fresh_lane(), q = lane >> 4, cc = lane & 15;
  float mean[2], rstd[2];
#pragma unroll
  for (int u = 0; u < 2; ++u) { mean[u] = *(const LAS float*)(lds + LDS_MEAN + ((2 * rg + u) * 16 + cc) * 4); rstd[u] = *(const LAS float*)(lds + LDS_RSTD + ((2 * rg + u) * 16 + cc) * 4); }
  const rsrc_t rH = mk_rsrc(c.ws + WS_H + (size_t)((((2 * c.tp + (rg >> 1)) * 56 + 2 * c.PA + (col0 >> 5)) * 4 + 2 * (rg & 1)) * 1024), NB * 4096 - 2048);
  LAS char* tb = lds + col0 * 4 + q * 16;
  f32x4 cs[2 * NB], bv[2 * NB];
#pragma unroll
  for (int n = 0; n < 2 * NB; ++n) { cs[n] = *(const LAS f32x4*)(tb + LDS_CS1 + n * 64); bv[n] = *(const LAS f32x4*)(tb + LDS_B1 + n * 64); }
#pragma unroll
  for (int b = 0; b < NB; ++b)
#pragma unroll
    for (int u = 0; u < 2; ++u) {
      unsigned d[4];
#pragma unroll
      for (int h = 0; h < 2; ++h) {
        f32x4 z = (acc[2 * b + h][u] - mean[u] * cs[2 * b + h]) * rstd[u] + bv[2 * b + h];
        z = __builtin_elementwise_max(z, (f32x4){0.f, 0.f, 0.f, 0.f});
        z = z * z;
        d[2 * h] = pk_bf16(z[0], z[1]); d[2 * h + 1] = pk_bf16(z[2], z[3]);
      }
      __builtin_amdgcn_raw_buffer_store_b128((u32x4){d[0], d[1], d[2], d[3]}, rH, lane * 16 + u * 1024 + b * 4096, 0, 16);
    }
}

DEV void loaderA(const Ctx& c) {
  DBG_DECL; BT_DECL;
  const int lane = fresh_lane(), r8 = lane >> 3, p8 = lane & 7;
  char* ws = c.ws;
  const unsigned lb = c.lb, rl = 8 * (r8 >> 2) + (r8 & 3), swz = (unsigned)((p8 ^ r8) * 16);
  const u32x4 dX = mk_desc(c.x + (size_t)c.tp * 131072, 131072);
  const unsigned vX = rl * 1024 + swz;
  auto issueX = [&](int s) { unit_rows(dX, vX, ((s + c.rot1) & 7) * 128, 1024, lb + (s & 3) * SLOT); };
  STAMP(8);
#pragma unroll 1
  for (int s = CSTART; s < NINIT; ++s) issueX(s);
  const u32x4 dC = mk_desc((c.ks >> 1 ? c.Ci : c.Cr) + (size_t)c.tp * 524288 + (c.ks & 1) * 2048, 524288 - 2048);
  const u32x4 dH = mk_desc(ws + WS_H + (size_t)(2 * c.tp + (c.ks >> 1)) * (56 * 4096), 56 * 4096);
  const unsigned vC = rl * 4096 + swz, vL = lane * 16, vJ = r8 * W1R + (p8 ^ (r8 & 2)) * 16;
  auto issueC = [&](int s) { unit_rows(dC, vC, ((s + c.rot2) & 15) * 128, 4096, lb + (s & 3) * SLOT); };
  STAMP(10);
  if (CSTART == 0) wait_vm<16 * (NINIT - 1)>();
  STAMP(11);
  bar();
  if (NINIT == 2) issueX(2);
#pragma unroll 1
  for (int s = 0; s < 5; ++s) { issueX(s + 3); wait_vm<32>(); bar(); }
  issueJ(c, 1, vJ); wait_vm<32>(); bar();
#pragma unroll 1
  for (int s = 0; s < 2; ++s) { issueC(s); wait_vm<32>(); bar(); }
  const u64 tag = uniform64(*(const LAS u64*)(c.lds + LDS_TAG));
  bar();
#pragma unroll 1
  for (int s = 2; s < 4; ++s) { issueC(s); if (s == 2) bar(); }
  {
    const unsigned so = (c.ks >> 1) * 65536 + (c.ec * 128 + (c.ks & 1) * 64) * 2, vo = r8 * 1024 + (p8 ^ ((r8 >> 1) & 1)) * 16;
#pragma unroll
    for (int g = 0; g < 2; ++g) dma4<false>(dX, vo, vo ^ 32, vo ^ 64, vo ^ 96, so + g * 32768, so + g * 32768 + 8192, so + g * 32768 + 16384, so + g * 32768 + 24576, lb + LDS_XT + g * 4096);
  }
  wait_vm<56>();
  bar();
  issueAB(c, 3, nib(BSEQ, 3), vL);
  const char* hfp = ws + WS_FLAG + (unsigned)(1 * 256 + c.tp * 16 + (lane & 15)) * FLAG_STRIDE;
  u64 fv = 0;
#pragma unroll 1
  for (int s = 0; s < 12; ++s) { if (s < 2) wait_vm<56>(); else wait_vm<32>(); bar(); issueC(s + 4); if (s == 8) fv = ld8_async(hfp); }
  wait_vm<32>();
  asm volatile("" : "+v"(fv));
  if (!__all(fv == tag)) poll(hfp, [&](u64 x) { return x == tag; });
#pragma unroll 1
  for (int s = 12; s < 26; ++s) { wait_vm<32>(); bar(); unit_step<true>(dH, vL, vL, (s - 12) * 16384, 1024, lb + (s & 3) * SLOT, 4); }
  wait_vm<32>(); bar();
  wait_vm<16>(); bar();
  wait_vm<0>(); bar();
  STAMP_END(); BT_END();
}

DEV void loaderB(const Ctx& c) {
  DBG_DECL; BT_DECL;
  const int lane = fresh_lane(), r8 = lane >> 3, p8 = lane & 7;
  const unsigned lb = c.lb;
  const u32x4 dW = mk_desc((c.ks >> 1 ? c.Wi : c.Wr) + (size_t)((c.ks & 1) * 4 + c.ec) * 65536, 65536), d1 = mk_desc(c.w1, D_ * W1R);
  const unsigned vW = r8 * 1024 + (p8 ^ r8) * 16, vJ = r8 * W1R + (p8 ^ (r8 & 2)) * 16;
  auto issueB = [&](int s) {
    const int k = (s + c.rot1) & 7;
    unit_step<false>(dW, vW, vW, k * 128, 8192, lb + (4 + (s & 3)) * SLOT, 2);
    unit_step<false>(d1, vJ, vJ ^ 64, k * (64 * W1R) + c.PA * 128, 8 * W1R, lb + (4 + (s & 3)) * SLOT + 8192, 2);
  };
#pragma unroll 1
  for (int s = CSTART; s < NINIT; ++s) issueB(s);
  const u32x4 d2 = mk_desc(c.w2 + (c.ec * 128 + (c.ks & 1) * 64) * 2, F_ * 1024 - 1024 + 128);
  const unsigned vL = lane * 16, v2 = r8 * 1024 + (p8 ^ (r8 & 6)) * 16;
  if (CSTART == 0) wait_vm<16 * (NINIT - 1)>();
  bar();
  if (NINIT == 2) issueB(2);
#pragma unroll 1
  for (int s = 0; s < 5; ++s) { issueB(s + 3); wait_vm<32>(); bar(); }
#pragma unroll 1
  for (int s = 5; s < 8; ++s) { issueJ(c, s == 5 ? 0 : s - 4, vJ); wait_vm<32>(); bar(); }
  const u64 tag = uniform64(*(const LAS u64*)(c.lds + LDS_TAG));
  bar();
  wait_vm<0>();
  bar();
  STAMP(1);
  const int pi = (lane >> 1) & 7;
  const char* afp = c.ws + WS_FLAG + (unsigned)((8 * (c.ks & 1) + ((pi + (c.rot2 >> 1)) & 7)) * 16 + 8 * (c.ks >> 1) + 2 * c.ec + (lane & 1)) * FLAG_STRIDE;
  u64 fv = poll(afp, [&](u64 x) { return x == tag || (FINE && pi > 0); });
  STAMP(2);
  unsigned bseq = BSEQ;
#pragma unroll 1
  for (int s = 0; s < 2; ++s) { issueAB(c, s, bseq & 15, vL); ROTN(bseq, 5); }
  if (!__all(fv == tag || pi > 1)) fv = poll(afp, [&](u64 x) { return x == tag || pi > 1; });
  wait_vm<16>();
  bar();
  STAMP(3);
  issueAB(c, 2, nib(BSEQ, 2), vL);
  ROTN(bseq, 5); ROTN(bseq, 5);
  if (!__all(fv == tag)) {
    fv = ld8_async(afp);
    wait_vm<17>(); bar();
    wait_vm<0>();
    asm volatile("" : "+v"(fv));
    if (!__all(fv == tag)) poll(afp, [&](u64 x) { return x == tag; });
  } else { wait_vm<16>(); bar(); }
  issueAB(c, 4, bseq & 15, vL); ROTN(bseq, 5);
#pragma unroll 1
  for (int s = 1; s < 12; ++s) { if (s < 2) wait_vm<16>(); else wait_vm<32>(); bar(); issueAB(c, s + 4, bseq & 15, vL); ROTN(bseq, 5); }
#pragma unroll 1
  for (int s = 12; s < 26; ++s) { wait_vm<32>(); bar(); unit_step<false>(d2, v2, v2, (s - 12) * 131072, 8192, lb + (4 + (s & 3)) * SLOT, 4); }
  wait_vm<32>(); bar();
  wait_vm<16>(); bar();
  wait_vm<0>(); bar();
  STAMP_END(); BT_END();
}

DEV void helper(const Ctx& c) {
  BT_DECL;
  __builtin_amdgcn_s_setprio(HPRIO);
  const int hw = c.wid - 6, lane = fresh_lane();
  LAS char* lds = c.lds; char* ws = c.ws;
  const int q = lane >> 4, cc = lane & 15, qq = cc >> 2, cp = c.ks * 4 + c.ec;
  const unsigned dump = c.lb + LDS_DUMP;
  const char* Cp = (c.ks >> 1 ? c.Ci : c.Cr) + (size_t)(c.tp * 128 + c.ec * 32 + (lane & 31)) * 4096 + (c.ks & 1) * 2048 + (lane >> 5) * 128;
  unsigned pseq = PSEQ;
  auto prefC = [&](int n) { if (hw == 1) for (int i = 0; i < n; ++i) { touch(Cp + (pseq & 7) * 256, dump); pseq >>= 4; } };
  u64 tag;
  {
    const u64 tagv = __hip_atomic_load((GAS u64*)(ws + WS_EPOCH), __ATOMIC_RELAXED, __HIP_MEMORY_SCOPE_AGENT);
    const int row = c.tp * 128 + cp * 8 + 4 * hw + q, wrow = cp * 64 + c.tp * 4 + 2 * hw + (lane >> 5);
    const char* wp = (c.ks >> 1 ? c.Wi : c.Wr) + (size_t)(wrow & 511) * 1024 + (lane & 31) * 16;
    const char* p1 = c.w1 + (size_t)(c.tp * 32 + (lane >> 3)) * W1R + (c.PA + (c.two ? hw : 0)) * 128 + (lane & 7) * 16;
    char* pubp = cc == 0 ? ws + WS_ROWST + (size_t)row * 8 : ws + WS_CSW + (size_t)wrow * 8;
    const rsrc_t rP = mk_rsrc(ws + WS_CS1 + (size_t)(cp * 16 + c.tp) * 1024 + hw * 512, 512);
    u32x4 xv[4], dv[2], wv[4];
#pragma unroll
    for (int i = 0; i < 4; ++i) xv[i] = *(const u32x4*)(c.x + (size_t)row * 1024 + cc * 16 + i * 256);
    dv[0] = *(const u32x4*)wp; dv[1] = *(const u32x4*)(wp + 512);
#pragma unroll
    for (int i = 0; i < 4; ++i) wv[i] = *(const u32x4*)(p1 + i * (8 * W1R));
    const u16 b1v = *(const u16*)(c.b1 + ((c.PA + (c.two ? hw : 0)) * 64 + lane) * 2);
    const u16 b2v = *(const u16*)(c.b2 + (c.ec * 128 + (c.ks & 1) * 64 + lane) * 2);
#define CHUNK() do { SCHED(); rawbar(); SCHED(); } while (0)
    CHUNK();
    float s0 = 0.f, s1 = 0.f, sw = 0.f;
#pragma unroll
    for (int i = 0; i < 4; ++i) { s0 = dot2(xv[i].x, ONE2, s0); s1 = dot2(xv[i].y, ONE2, s1); s0 = dot2(xv[i].z, ONE2, s0); s1 = dot2(xv[i].w, ONE2, s1); }
#pragma unroll
    for (int i = 0; i < 8; ++i) sw = dot2(dv[i >> 2][i & 3], ONE2, sw);
    const float m = sum16(s0 + s1) * (1.f / 512.f);
    sw = sum16(sw);
    { const auto r = __builtin_amdgcn_permlane16_swap(__float_as_uint(sw), __float_as_uint(sw), false, false); sw = __uint_as_float(r[0]) + __uint_as_float(r[1]); }
    f32x4 z4 = {0.f, 0.f, 0.f, 0.f}, z5 = z4;
#pragma unroll
    for (int i = 0; i < 2; ++i) { const f32x4 d0 = unpk(xv[i].x, xv[i].y) - m, d1 = unpk(xv[i].z, xv[i].w) - m; z4 += d0 * d0; z5 += d1 * d1; }
    CHUNK();
#pragma unroll
    for (int i = 2; i < 4; ++i) { const f32x4 d0 = unpk(xv[i].x, xv[i].y) - m, d1 = unpk(xv[i].z, xv[i].w) - m; z4 += d0 * d0; z5 += d1 * d1; }
    z4 += z5;
    const float var = sum16((z4[0] + z4[1]) + (z4[2] + z4[3])) * (1.f / 512.f);
    const unsigned sv = pk_bf16(m, __builtin_amdgcn_sqrtf(rbf(rbf(var) + 1e-5f)));
    tag = uniform64(tagv) + 1;
    __builtin_amdgcn_s_waitcnt(0x0F70);
    if (cc < 2 && (cc == 0 || (q & 1) == 0))
      __hip_atomic_store((GAS u64*)pubp, ((u64)(unsigned)tag << 32) | (cc == 0 ? sv : __float_as_uint(sw)), __ATOMIC_RELAXED, __HIP_MEMORY_SCOPE_AGENT);
    CHUNK();
    prefC((PM1 >> 2) & 1);
    float t[8];
#pragma unroll
    for (int j = 0; j < 4; ++j) {
      t[2 * j] = (bflo(wv[0][j]) + bflo(wv[1][j])) + (bflo(wv[2][j]) + bflo(wv[3][j]));
      t[2 * j + 1] = (bfhi(wv[0][j]) + bfhi(wv[1][j])) + (bfhi(wv[2][j]) + bfhi(wv[3][j]));
    }
    *(LAS float*)(lds + LDS_B1 + (hw * 64 + lane) * 4) = bflo(b1v);
    *(LAS float*)(lds + LDS_B2 + lane * 4) = bflo(b2v);
    if (hw == 0 && lane == 0) *(LAS u64*)(lds + LDS_TAG) = tag;
    CHUNK();
    float u4[4], u2[2];
#pragma unroll
    for (int j = 0; j < 4; ++j) { const auto r = __builtin_amdgcn_permlane32_swap(__float_as_uint(t[j]), __float_as_uint(t[j + 4]), false, false); u4[j] = __uint_as_float(r[0]) + __uint_as_float(r[1]); }
#pragma unroll
    for (int j = 0; j < 2; ++j) { const auto r = __builtin_amdgcn_permlane16_swap(__float_as_uint(u4[j]), __float_as_uint(u4[j + 2]), false, false); u2[j] = dpp_add<0x128>(__uint_as_float(r[0]) + __uint_as_float(r[1])); }
    if (cc < 8) __builtin_amdgcn_raw_buffer_store_b128((u32x4){__float_as_uint(u2[0]), (unsigned)tag, __float_as_uint(u2[1]), (unsigned)tag}, rP, (cc * 4 + q) * 16, 0, 16);
    const char* stp = ws + WS_ROWST + (size_t)(c.tp * 128 + 64 * hw + 32 * (lane >> 5) + 8 * qq + 4 * (q & 1) + (lane & 3)) * 8;
    const char* cwp = ws + WS_CSW + (size_t)(cp * 64 + lane) * 8;
    wait_lgkm0();
    bar();
    u64 st = ld8_async(stp), cw = ld8_async(cwp);
    bar(); bar();
    const unsigned t32 = (unsigned)tag;
    wait_vm<0>();
    asm volatile("" : "+v"(st), "+v"(cw));
    const bool ok1 = __all((unsigned)(st >> 32) == t32 && (unsigned)(cw >> 32) == t32);
    if (!ok1) { st = ld8_async(stp); cw = ld8_async(cwp); }
    bar();
    if (!ok1) {
      wait_vm<0>();
      asm volatile("" : "+v"(st), "+v"(cw));
      if (!__all((unsigned)(st >> 32) == t32)) st = poll(stp, [&](u64 x) { return (unsigned)(x >> 32) == t32; });
      if (!__all((unsigned)(cw >> 32) == t32)) cw = poll(cwp, [&](u64 x) { return (unsigned)(x >> 32) == t32; });
    }
    *(LAS float*)(lds + LDS_MEAN + hw * 256 + lane * 4) = bflo((unsigned)st);
    *(LAS float*)(lds + LDS_RSTD + hw * 256 + lane * 4) = __builtin_amdgcn_rcpf(bfhi((unsigned)st));
    *(LAS unsigned*)(lds + LDS_CSW + lane * 4) = (unsigned)cw;
    const char* pvp = ws + WS_CS1 + (size_t)(cp * 16 + (lane >> 5)) * 1024 + hw * 512 + (lane & 31) * 16;
    u32x4 pv[8];
#pragma unroll
    for (int i = 0; i < 8; ++i) pv[i] = ld16_async(pvp + i * 2048);
    wait_lgkm0();
    bar();
    const u64 t0 = __builtin_amdgcn_s_memrealtime();
    for (;;) {
      wait_vm<0>();
      asm volatile("" : "+v"(pv[0]), "+v"(pv[1]), "+v"(pv[2]), "+v"(pv[3]), "+v"(pv[4]), "+v"(pv[5]), "+v"(pv[6]), "+v"(pv[7]));
      bool ok = true;
#pragma unroll
      for (int i = 0; i < 8; ++i) ok = ok && pv[i].y == t32 && pv[i].w == t32;
      if (__all(ok) || __builtin_amdgcn_s_memrealtime() - t0 > 20000000ull) break;
#pragma unroll
      for (int i = 0; i < 8; ++i) pv[i] = ld16_async(pvp + i * 2048);
    }
    float a0 = 0.f, a1 = 0.f;
#pragma unroll
    for (int i = 0; i < 8; ++i) { a0 += __uint_as_float(pv[i].x); a1 += __uint_as_float(pv[i].z); }
    auto r = __builtin_amdgcn_permlane32_swap(__float_as_uint(a0), __float_as_uint(a0), false, false);
    a0 = __uint_as_float(r[0]) + __uint_as_float(r[1]);
    r = __builtin_amdgcn_permlane32_swap(__float_as_uint(a1), __float_as_uint(a1), false, false);
    a1 = __uint_as_float(r[0]) + __uint_as_float(r[1]);
    *(LAS f32x2*)(lds + LDS_CS1 + hw * 256 + (lane & 31) * 8) = (f32x2){a0, a1};
    wait_lgkm0();
  }
  bar();
  if (hw == 0 && lane == 0) set_flag(ws, 0, c.tp * 16 + cp, tag);
  bar();
  prefC(PMF);
  {
    const char* Wp = c.w2 + (c.ec * 128 + (c.ks & 1) * 64) * 2 + (size_t)((c.tp & 7) * 224 + hw * 112 + (lane < 56 ? lane : 0)) * 1024;
    const bool ew = blockIdx.x == 0 && hw == 1;
    const char* fp = ws + WS_FLAG + (unsigned)lane * FLAG_STRIDE;
    u64 f0 = 0, f1 = 0, f2 = 0, f3 = 0;
    auto run = [&](int s0, int s1) {
#pragma unroll 1
      for (int s = s0; s < s1; ++s) {
        if (PREFW && s == PREFW + WSP * hw) touch(Wp, dump);
        if (PREFW && s == PREFW + WSP * (2 + hw)) touch(Wp + 56 * 1024, dump);
        prefC((PM2 >> (s + 1)) & 1);
        bar();
        if (hw == 0 && lane == 0) {
          if (s == HFLAG) set_flag(ws, 1, c.tp * 16 + cp, tag);
          if (s == YFLAG) set_flag(ws, 2, (c.tp * 4 + c.ec) * 4 + c.ks, tag);
        }
      }
    };
    run(-1, 10);
    if (ew) { f0 = ld8_async(fp); f1 = ld8_async(fp + 64 * FLAG_STRIDE); f2 = ld8_async(fp + 128 * FLAG_STRIDE); f3 = ld8_async(fp + 192 * FLAG_STRIDE); }
    run(10, 15);
    wait_vm<0>();
    asm volatile("" : "+v"(f0), "+v"(f1), "+v"(f2), "+v"(f3));
    const bool early = ew && __all(f0 == tag && f1 == tag && f2 == tag && f3 == tag);
    if (early && lane == 0) __hip_atomic_store((GAS u64*)(ws + WS_EPOCH), tag, __ATOMIC_RELAXED, __HIP_MEMORY_SCOPE_AGENT);
    run(15, YPOLL + 1);
    const char* yfp = ws + WS_FLAG + (unsigned)(2 * 256 + (c.tp * 4 + c.ec) * 4 + (lane & 3)) * FLAG_STRIDE;
    f0 = ld8_async(yfp);
    run(YPOLL + 1, YPOLL + 2);
    f1 = ld8_async(yfp);
    run(YPOLL + 2, YCHK);
    wait_vm<1>();
    asm volatile("" : "+v"(f0));
    if (!__all(f0 == tag)) {
      wait_vm<0>();
      asm volatile("" : "+v"(f1));
      if (!__all(f1 == tag)) poll(yfp, [&](u64 x) { return x == tag; });
    }
    run(YCHK, 29);
    wait_vm<0>();
    asm volatile("" : "+v"(f1));
    if (ew && !early) {
#pragma unroll 1
      for (int i = 0; i < 4; ++i) poll(fp + i * 64 * FLAG_STRIDE, [&](u64 x) { return x == tag; });
      if (lane == 0) __hip_atomic_store((GAS u64*)(ws + WS_EPOCH), tag, __ATOMIC_RELAXED, __HIP_MEMORY_SCOPE_AGENT);
    }
    wait_vm<0>();
  }
  BT_END();
}

DEV void consumer2(const Ctx& c DBG_PARAM BT_PARAM) {
  const int w = c.wid, lane = fresh_lane();
  LAS char* lds = c.lds;
  const int q = lane >> 4, cc = lane & 15, qq = cc >> 2, pp = lane & 3;
  const unsigned wa0 = cc * 128 + ((q ^ (cc & 7)) * 16), wa1 = cc * 128 + (((4 + q) ^ (cc & 7)) * 16);
  const int wt = w >> 1, we = w & 1;
  unsigned wb[2];
#pragma unroll
  for (int n = 0; n < 2; ++n) wb[n] = (4 * q + qq) * 128 + (((2 * (2 * we + n) + (pp >> 1)) ^ ((4 * q + qq) & 6)) * 16) + 8 * (pp & 1);
  const unsigned hb = wt * 2048 + lane * 16;
  bf16x8 hA[4][2], wA[4][2], hB[4][2], wB[4][2];
  wait_lgkm0();
  bar();
  STAMP(5);
#define PREAD(HN, WN, SO, SW) if (i < 12) { if (i % 3 == 2) { HN[i / 3][0] = ldsb128(lds + (SO) + hb + (i / 3) * 4096); HN[i / 3][1] = ldsb128(lds + (SO) + hb + (i / 3) * 4096 + 1024); } \
                                            else WN[i / 3][i % 3] = ldstr(lds + (SW) + wb[i % 3] + (i / 3) * 4096, 2048); SCHED(); }
  {
    f32x4 acc[4][4];
#pragma unroll
    for (int m = 0; m < 4; ++m)
#pragma unroll
      for (int n = 0; n < 4; ++n) acc[m][n] = (f32x4){0, 0, 0, 0};
    const unsigned a0 = wt * 8192 + wa0, a1 = wt * 8192 + wa1, b0 = we * 4096 + lane * 16;
    bf16x8 fa0[4], fb0[4], fa1[4], fb1[4];
#pragma unroll
    for (int m = 0; m < 4; ++m) { fa0[m] = ldsb128(lds + nib(CSEQ, 0) * SLOT + a0 + m * 2048); fb0[m] = ldsb128(lds + nib(BSEQ, 0) * SLOT + b0 + m * 1024); }
#define HALF(FA, FB, NA_, NB_, AOFF, BOFF) do { _Pragma("unroll") for (int i = 0; i < 16; ++i) { \
      acc[i >> 2][i & 3] = MFMA(FB[i & 3], FA[i >> 2], acc[i >> 2][i & 3]); SCHED(); \
      if (i < 8) { if (i == 0) NB_[0] = ldsb128(lds + (BOFF)); else if (i == 1) NA_[0] = ldsb128(lds + (AOFF)); \
        else if (i < 5) NB_[i - 1] = ldsb128(lds + (BOFF) + (i - 1) * 1024); else NA_[i - 4] = ldsb128(lds + (AOFF) + (i - 4) * 2048); SCHED(); } } } while (0)
    unsigned so = nib(CSEQ, 0) * SLOT, sb = nib(BSEQ, 0) * SLOT, cseq = CSEQ, bseq = BSEQ;
#pragma unroll 1
    for (int seg = 0; seg < 2; ++seg) {
      const int n = seg ? 15 - HFLAG : HFLAG;
#pragma unroll 1
      for (int s = 0; s < n; ++s) {
        HALF(fa0, fb0, fa1, fb1, so + a1, sb + b0 + 8192);
        wait_lgkm0();
        bar();
        ROTN(bseq, 5); ROTN(cseq, 4); sb = (bseq & 15) * SLOT; so = (cseq & 15) * SLOT;
        HALF(fa1, fb1, fa0, fb0, so + a0, sb + b0);
      }
      wait_vm<0>();
    }
    HALF(fa0, fb0, fa1, fb1, so + a1, sb + b0 + 8192);
    wait_lgkm0();
    bar();
#pragma unroll
    for (int i = 0; i < 16; ++i) {
      acc[i >> 2][i & 3] = MFMA(fb1[i & 3], fa1[i >> 2], acc[i >> 2][i & 3]); SCHED();
      PREAD(hA, wA, nib(CSEQ, 0) * SLOT, nib(WSEQ, 0) * SLOT)
    }
    STAMP(6);
    f32x4 ao[2][2];
#pragma unroll
    for (int m = 0; m < 2; ++m) { ao[m][0] = (f32x4){0, 0, 0, 0}; ao[m][1] = (f32x4){0, 0, 0, 0}; }
    unsigned hseq = CSEQ, wseq = WSEQ;
#define PBAR() do { wait_lgkm0(); bar(); SCHED(); } while (0)
#define PMMA(HC, WC, HN, WN) do { ROTN(hseq, 4); ROTN(wseq, 4); const unsigned sh = (hseq & 15) * SLOT, sw = (wseq & 15) * SLOT; SCHED(); \
      _Pragma("unroll") for (int i = 0; i < 16; ++i) { \
        ao[(i >> 1) & 1][i & 1] = MFMA(WC[i >> 2][i & 1], HC[i >> 2][(i >> 1) & 1], ao[(i >> 1) & 1][i & 1]); SCHED(); \
        PREAD(HN, WN, sh, sw) } } while (0)
#define PSTEP(HC, WC, HN, WN) do { PBAR(); PMMA(HC, WC, HN, WN); } while (0)
    auto own = [&](int m0) {
      if (w == c.ks) {
#pragma unroll
        for (int m = m0; m < m0 + 2; ++m)
#pragma unroll
          for (int n = 0; n < 4; ++n) *(LAS f32x4*)(lds + 8 * SLOT + (m * 4 + n) * 1024 + lane * 16) = acc[m][n];
      }
    };
    PBAR();
    if (w != c.ks) {
      const rsrc_t rY = mk_rsrc(c.ws + WS_YP + (size_t)(((c.tp * 4 + c.ec) * 4 + c.ks) * 4 + w) * 8192, 8192);
#pragma unroll
      for (int m = 0; m < 4; ++m)
#pragma unroll
        for (int j = 0; j < 2; ++j) {
          const f32x4 e = acc[m][2 * j], o = acc[m][2 * j + 1];
          __builtin_amdgcn_raw_buffer_store_b128((u32x4){pk_bf16(e[0], e[1]), pk_bf16(e[2], e[3]), pk_bf16(o[0], o[1]), pk_bf16(o[2], o[3])}, rY, lane * 16 + (m * 2 + j) * 1024, 0, 16);
        }
    }
    own(0);
    SCHED();
    STAMP(7);
    PMMA(hA, wA, hB, wB);
    PBAR();
    own(2);
    SCHED();
    PMMA(hB, wB, hA, wA);
    static_assert((YFLAG == 18 || YFLAG == 20) && YCHK == 25, "schedule");
    if (YFLAG == 18) wait_vm<0>();
#pragma unroll 1
    for (int s = 0; s < 4; ++s) { PSTEP(hA, wA, hB, wB); PSTEP(hB, wB, hA, wA); wait_vm<0>(); }
    const rsrc_t rY = mk_rsrc(c.ws + WS_YP + (size_t)((c.tp * 4 + c.ec) * 16 + c.ks) * 8192, 4 * 32768 - 3 * 8192);
    u32x4 yp[3][2];
    const unsigned yv = lane * 16 + (4 * wt + we) * 1024;
#pragma unroll
    for (int i = 0; i < 3; ++i) yp[i][0] = __builtin_amdgcn_raw_buffer_load_b128(rY, yv, ((c.ks + 1 + i) & 3) * 32768, 16);
    SCHED();
    PSTEP(hA, wA, hB, wB);
#pragma unroll
    for (int i = 0; i < 3; ++i) yp[i][1] = __builtin_amdgcn_raw_buffer_load_b128(rY, yv + 2048, ((c.ks + 1 + i) & 3) * 32768, 16);
    const int tl = 32 * wt + 8 * qq + (cc & 3), el = 32 * we + 4 * q;
    f32x4 y[2][2], bv[2];
    u32x2 xr[2][2];
#pragma unroll
    for (int n = 0; n < 2; ++n) {
      bv[n] = *(const LAS f32x4*)(lds + LDS_B2 + (el + 16 * n) * 4);
#pragma unroll
      for (int m = 0; m < 2; ++m) {
        y[m][n] = *(const LAS f32x4*)(lds + 8 * SLOT + ((2 * wt + m) * 4 + 2 * we + n) * 1024 + lane * 16);
        xr[m][n] = *(const LAS u32x2*)(lds + LDS_XT + (tl + 4 * m) * 128 + (((el + 16 * n) * 2) ^ ((2 * qq + ((cc >> 1) & 1)) * 16)));
      }
    }
    SCHED();
    PSTEP(hB, wB, hA, wA);
    STAMP(8);
#pragma unroll
    for (int i = 0; i < 3; ++i) { y[0][0] += unpk(yp[i][0].x, yp[i][0].y); y[0][1] += unpk(yp[i][0].z, yp[i][0].w); }
    char* op = c.out + (size_t)(c.tp * 128 + (c.ks >> 1) * 64 + tl) * 1024 + (c.ec * 128 + (c.ks & 1) * 64 + el) * 2;
    f32x4 xo[2][2];
#pragma unroll
    for (int m = 0; m < 2; ++m)
#pragma unroll
      for (int n = 0; n < 2; ++n) { xo[m][n] = unpk(xr[m][n].x, xr[m][n].y); ao[m][n] += bv[n]; }
    SCHED();
    PSTEP(hA, wA, hB, wB);
#pragma unroll
    for (int i = 0; i < 3; ++i) { y[1][0] += unpk(yp[i][1].x, yp[i][1].y); y[1][1] += unpk(yp[i][1].z, yp[i][1].w); }
    STAMP(9);
    SCHED();
#pragma unroll
    for (int i = 0; i < 16; ++i) ao[i >> 3][(i >> 2) & 1] = MFMA(wB[i & 3][(i >> 2) & 1], hB[i & 3][i >> 3], ao[i >> 3][(i >> 2) & 1]);
#pragma unroll
    for (int m = 0; m < 2; ++m)
#pragma unroll
      for (int n = 0; n < 2; ++n) {
        f32x4 o = xo[m][n];
#pragma unroll
        for (int r = 0; r < 4; ++r) { const float z = ao[m][n][r]; o[r] += y[m][n][r] * (z * __builtin_amdgcn_rcpf(1.f + __expf(-z))); }
        *(u32x2*)(op + m * 4096 + n * 32) = (u32x2){pk_bf16(o[0], o[1]), pk_bf16(o[2], o[3])};
      }
    STAMP(10);
    STAMP_END(); BT_END();
  }
}

DEV void consumer(const Ctx& c) {
  DBG_DECL; BT_DECL;
  const int w = c.wid;
  LAS char* lds = c.lds;
  STAMP(0);
  unsigned ja[4], bx0, bx1, bw0, bw1;
  {
    const int lane = fresh_lane(), q = lane >> 4, cc = lane & 15, qq = cc >> 2, pp = lane & 3;
    if (PREFI) touch(c.pc + (w * 64 + lane < PREFI ? w * 64 + lane : 0) * 128, c.lb + LDS_DUMP);
    const unsigned wa0 = cc * 128 + ((q ^ (cc & 7)) * 16), wa1 = cc * 128 + (((4 + q) ^ (cc & 7)) * 16);
#pragma unroll
    for (int n = 0; n < 4; ++n) ja[n] = opq(4 * SLOT + (8 * q + qq) * 128 + (((2 * n + (pp >> 1)) ^ ((qq & 2) | ((q & 1) << 2))) * 16) + 8 * (pp & 1));
    bx0 = opq(wa0 + w * 4096); bx1 = opq(wa1 + w * 4096); bw0 = opq(wa0 + 4 * SLOT); bw1 = opq(wa1 + 4 * SLOT);
  }
  bf16x8 xf[2][16];
  if (CSTART) wait_vm<8 * (CSTART > 0 ? CSTART - 1 : 0) + (PREFI != 0)>();
  bar();
  STAMP(1);
  f32x4 ach[4][2];
  {
    f32x4 acc[4][2];
#pragma unroll
    for (int n = 0; n < 4; ++n) { acc[n][0] = acc[n][1] = ach[n][0] = ach[n][1] = (f32x4){0, 0, 0, 0}; }
    bf16x8 fr[128];
    auto rd = [&](auto t_) {
      constexpr int t = decltype(t_)::value, kk = t / 8, n = t % 8, so = ((kk >> 1) & 3) * SLOT;
      if constexpr (n == 0) { xf[0][kk] = ldsb128(lds + ((kk & 1) ? bx1 : bx0) + so); xf[1][kk] = ldsb128(lds + ((kk & 1) ? bx1 : bx0) + so + 2048); }
      if constexpr ((n & 1) == 0) fr[t] = ldsb128(lds + ((kk & 1) ? bw1 : bw0) + so + (n >> 1) * 2048);
      else fr[t] = ldstr(lds + ja[n >> 1] + so + 8192 + (kk & 1) * 4096, 512);
    };
    static_for<0, 8>(rd);
    SCHED();
    static_for<0, 128>([&](auto p_) {
      constexpr int p = decltype(p_)::value, kk = p / 8, n = p % 8;
      constexpr int kn = p / 16 + 1;
      if constexpr (p == 16 * kn - lead1(kn)) {
        wait_lgkm0();
        if constexpr (kn < CSTART) wait_vm<8 * (CSTART - 1 - kn) + (PREFI != 0)>();
        bar(); SCHED();
      }
      if constexpr ((n & 1) == 0) acc[n >> 1][0] = MFMA(xf[0][kk], fr[p], acc[n >> 1][0]); else ach[n >> 1][0] = MFMA(fr[p], xf[0][kk], ach[n >> 1][0]);
      SCHED();
      static_for<p + 1, p + 16>([&](auto t_) {
        constexpr int t = decltype(t_)::value;
        if constexpr (t < 128 && t >= 8 && sched1(t) == p) { rd(t_); SCHED(); }
      });
      if constexpr ((n & 1) == 0) acc[n >> 1][1] = MFMA(xf[1][kk], fr[p], acc[n >> 1][1]); else ach[n >> 1][1] = MFMA(fr[p], xf[1][kk], ach[n >> 1][1]);
      SCHED();
    });
    STAMP(2);
    const int lane = fresh_lane(), q = lane >> 4, cc = lane & 15;
    f32x4 mr[2], rr[2];
    const float sgn = (c.ks >> 1) ? -1.f : 1.f;
#pragma unroll
    for (int u = 0; u < 2; ++u) {
      mr[u] = *(const LAS f32x4*)(lds + LDS_MEAN + ((2 * w + u) * 16 + 4 * q) * 4);
      rr[u] = *(const LAS f32x4*)(lds + LDS_RSTD + ((2 * w + u) * 16 + 4 * q) * 4) * sgn;
    }
    float cs[4];
#pragma unroll
    for (int n = 0; n < 4; ++n) cs[n] = *(const LAS float*)(lds + LDS_CSW + (n * 16 + cc) * 4);
    const rsrc_t rA = mk_rsrc(c.ws + WS_ABT + (size_t)((((c.ks * 2 + (c.ec >> 1)) * 64 + c.tp * 4 + w) * 8 + (c.ec & 1) * 4) * 1024), 4096);
#pragma unroll
    for (int n = 0; n < 4; ++n) {
      const f32x4 o0 = (acc[n][0] - mr[0] * cs[n]) * rr[0], o1 = (acc[n][1] - mr[1] * cs[n]) * rr[1];
      u32x4 d = {pk_bf16(o0[0], o0[1]), pk_bf16(o0[2], o0[3]), pk_bf16(o1[0], o1[1]), pk_bf16(o1[2], o1[3])};
      __builtin_amdgcn_raw_buffer_store_b128(d, rA, lane * 16 + n * 1024, 0, 16);
    }
  }
  SCHED();
  STAMP(3);
  if (c.two) {
    static_assert(RPOS >= 16 && RPOS + LOOK2 <= 32 && FPOS < RPOS, "R follows the last use of J0 and precedes the first read of J2");
    bf16x8 g[64];
    f32x4 ac2[4][2];
    auto rd2 = [&](auto t_) {
      constexpr int t = decltype(t_)::value;
      g[t] = ldstr(lds + (nib(JSEQ, t / 16) - 4) * SLOT + ja[t & 3] + ((t >> 2) & 3) * 4096, 512);
    };
    static_for<0, LOOK2>(rd2);
#pragma unroll
    for (int n = 0; n < 4; ++n) { ac2[n][0] = (f32x4){0, 0, 0, 0}; ac2[n][1] = (f32x4){0, 0, 0, 0}; }
    SCHED();
    static_for<0, 64>([&](auto p_) {
      constexpr int p = decltype(p_)::value, kk = p / 4, n = p & 3;
      if constexpr (p == FPOS) { wait_vm<0>(); bar(); SCHED(); }
      if constexpr (p == RPOS) { bar(); SCHED(); }
      ac2[n][0] = MFMA(g[p], xf[0][kk], ac2[n][0]);
      SCHED();
      if constexpr (p + LOOK2 < 64) { rd2(IC<p + LOOK2>{}); SCHED(); }
      ac2[n][1] = MFMA(g[p], xf[1][kk], ac2[n][1]);
      SCHED();
    });
    STAMP(11);
    h_epilogue<2>(c, ac2, 64, w);
  } else {
    wait_vm<0>(); bar(); bar();
    STAMP(11);
  }
  h_epilogue<2>(c, ach, 0, w);
  STAMP(4);
  consumer2(c DBG_ARG BT_ARG);
}

__global__ __launch_bounds__(512, 2) void fnet_mlp_fused(const char* x, const char* Cr, const char* Ci, const char* Wr, const char* Wi,
                                                         const char* w1, const char* b1, const char* w2, const char* b2, char* out, char* ws) {
  extern __shared__ __attribute__((aligned(16))) char shm[];
  Ctx c;
  { u64 pc; asm volatile("s_getpc_b64 %0" : "=s"(pc)); c.pc = (const char*)(pc & ~(u64)255); }
  c.x = x; c.Cr = Cr; c.Ci = Ci; c.Wr = Wr; c.Wi = Wi; c.w1 = w1; c.b1 = b1; c.w2 = w2; c.b2 = b2; c.out = out; c.ws = ws;
  c.lds = (LAS char*)shm; c.lb = (unsigned)(size_t)c.lds;
  c.wid = __builtin_amdgcn_readfirstlane(threadIdx.x >> 6);
  const int b = blockIdx.x, xcd = b & 7, r = b >> 3;
  c.ks = xcd >> 1; c.tp = (xcd & 1) * 8 + (r >> 2); c.ec = r & 3;
  c.two = c.ec != 3;
  c.PA = 7 * c.ks + 2 * c.ec;
  c.rot1 = ROTM == 8 ? (2 * c.ec + (r >> 2)) & 7 : ROTM == 4 ? 2 * ((c.ec + (r >> 2)) & 3) : ROTM == 2 ? 4 * ((c.ec + (r >> 2)) & 1) : ROTM == 3 ? 3 * ((c.ec + (r >> 2)) % 3) : 0; c.rot2 = ROT2M == 2 ? 8 * ((c.ec + (r >> 2)) & 1) : ROT2M == 4 ? 4 * ((c.ec + (r >> 2)) & 3) : 0;
  if (c.wid == 4) loaderA(c); else if (c.wid == 5) loaderB(c); else if (c.wid >= 6) helper(c); else consumer(c);
}

extern "C" void kernel_launch(void* const* d_in, const int* in_sizes, int n_in, void* d_out, int out_size, void* d_ws, size_t ws_size, hipStream_t stream) {
  (void)hipFuncSetAttribute((const void*)fnet_mlp_fused, hipFuncAttributeMaxDynamicSharedMemorySize, LDS_BYTES);
  fnet_mlp_fused<<<256, 512, LDS_BYTES, stream>>>((const char*)d_in[0], (const char*)d_in[1], (const char*)d_in[2], (const char*)d_in[3], (const char*)d_in[4],
                                                  (const char*)d_in[6], (const char*)d_in[7], (const char*)d_in[8], (const char*)d_in[9], (char*)d_out, (char*)d_ws);
}
```

```cpp
#include <hip/hip_runtime.h>
typedef unsigned short u16;
typedef unsigned long long u64;
typedef short bf16x8 __attribute__((ext_vector_type(8)));
typedef short s16x4 __attribute__((ext_vector_type(4)));
typedef float f32x4 __attribute__((ext_vector_type(4)));
typedef float f32x2 __attribute__((ext_vector_type(2)));
typedef unsigned u32x4 __attribute__((ext_vector_type(4)));
typedef unsigned u32x2 __attribute__((ext_vector_type(2)));
typedef __bf16 bf2 __attribute__((ext_vector_type(2)));
#define LAS __attribute__((address_space(3)))
#define GAS __attribute__((address_space(1)))
#define DEV __device__ __forceinline__

constexpr int D_ = 512, F_ = 1792, W1R = F_ * 2;
constexpr unsigned WS_EPOCH = 0, WS_FLAG = 4096, FLAG_STRIDE = 128, WS_ROWST = 256u << 10, WS_CSW = 288u << 10, WS_CS1 = 512u << 10;
constexpr unsigned WS_ABT = 1u << 20, WS_H = 8u << 20, WS_YP = 16u << 20;
constexpr int SLOT = 16384, NSLOT = 9;
constexpr int LDS_XT = NSLOT * SLOT;
constexpr int LDS_MEAN = LDS_XT + 8192, LDS_RSTD = LDS_MEAN + 512, LDS_CSW = LDS_RSTD + 512, LDS_CS1 = LDS_CSW + 256;
constexpr int LDS_B1 = LDS_CS1 + 512, LDS_B2 = LDS_B1 + 512, LDS_TAG = LDS_B2 + 256;
constexpr int LDS_DUMP = LDS_TAG + 32, LDS_BYTES = LDS_DUMP + 256;
static_assert(LDS_BYTES <= 160 * 1024, "LDS");
constexpr int LEADE = 7, LOOKE = 14, LEADL = 4, LOOKL = 8;
constexpr int LOOK2 = 8, FPOS = 14, RPOS = 16, EPOS = 24, ESTEP = 8;
constexpr int PREFI = 24;
constexpr int PREFW = 8, PM2 = 0x12, HPOLL = 8;
constexpr int HFLAG = 3, YFLAG = 18, YPOLL = 22, YCHK = 25;
constexpr unsigned JSEQ = 0x6548u, CSEQ = 0x3210u, BSEQ = 0x65487u, WSEQ = 0x7654u;
constexpr int nib(unsigned seq, int i) { return (seq >> (4 * i)) & 15; }

template <int N> DEV void wait_vm() { asm volatile("s_waitcnt vmcnt(%0)" ::"n"(N) : "memory"); }
DEV void wait_lgkm0() { asm volatile("s_waitcnt lgkmcnt(0)" ::: "memory"); }
DEV void bar() { asm volatile("" ::: "memory"); __builtin_amdgcn_s_barrier(); asm volatile("" ::: "memory"); }
#define rawbar() __builtin_amdgcn_s_barrier()
#define SCHED() __builtin_amdgcn_sched_barrier(0)
#define MFMA(a, b, c) __builtin_amdgcn_mfma_f32_16x16x32_bf16((a), (b), (c), 0, 0, 0)
DEV unsigned pk_bf16(float a, float b) { f32x2 f = {a, b}; return __builtin_bit_cast(unsigned, __builtin_convertvector(f, bf2)); }
DEV float rbf(float a) { return __uint_as_float(pk_bf16(a, 0.f) << 16); }
DEV float bflo(unsigned v) { return __uint_as_float(v << 16); }
DEV float bfhi(unsigned v) { return __uint_as_float(v & 0xffff0000u); }
DEV f32x4 unpk(unsigned a, unsigned b) { return (f32x4){bflo(a), bfhi(a), bflo(b), bfhi(b)}; }
constexpr unsigned ONE2 = 0x3f803f80u;
DEV float dot2(unsigned a, unsigned b, float c) { return __builtin_amdgcn_fdot2_f32_bf16(__builtin_bit_cast(bf2, a), __builtin_bit_cast(bf2, b), c, false); }
DEV bf16x8 ldsb128(LAS char* p) { return *(const LAS bf16x8*)p; }
DEV bf16x8 ldstr(LAS char* p, int second) {
  s16x4 a = __builtin_amdgcn_ds_read_tr16_b64_v4i16((LAS s16x4*)p);
  s16x4 b = __builtin_amdgcn_ds_read_tr16_b64_v4i16((LAS s16x4*)(p + second));
  return __builtin_shufflevector(a, b, 0, 1, 2, 3, 4, 5, 6, 7);
}
DEV unsigned opq(unsigned v) { asm volatile("" : "+v"(v)); return v; }
DEV int fresh_lane() { int l; asm volatile("v_mbcnt_lo_u32_b32 %0, -1, 0\n\tv_mbcnt_hi_u32_b32 %0, -1, %0" : "=v"(l)); return l; }
typedef __amdgpu_buffer_rsrc_t rsrc_t;
DEV rsrc_t mk_rsrc(const void* p, unsigned bytes) { return __builtin_amdgcn_make_buffer_rsrc((void*)p, 0, bytes, 0x00020000); }
DEV u32x4 mk_desc(const void* p, unsigned bytes) { const u64 a = (u64)p; return (u32x4){(unsigned)a, (unsigned)(a >> 32) & 0xffffu, bytes, 0x00020000u}; }
#define DMA4(POL) asm volatile("s_mov_b32 m0, %9\n\ts_nop 0\n\tbuffer_load_dwordx4 %0, %4, %5 offen " POL "lds\n\tbuffer_load_dwordx4 %1, %4, %6 offen offset:1024 " POL "lds\n\t" \
    "buffer_load_dwordx4 %2, %4, %7 offen offset:2048 " POL "lds\n\tbuffer_load_dwordx4 %3, %4, %8 offen offset:3072 " POL "lds" \
    ::"v"(v0), "v"(v1), "v"(v2), "v"(v3), "s"(desc), "s"(s0), "s"(s1 - 1024), "s"(s2 - 2048), "s"(s3 - 3072), "s"(dst) : "memory")
template <bool DEVSCOPE> DEV void dma4(u32x4 desc, unsigned v0, unsigned v1, unsigned v2, unsigned v3, unsigned s0, unsigned s1, unsigned s2, unsigned s3, unsigned dst) {
  if constexpr (DEVSCOPE) DMA4("sc1 "); else DMA4("");
}
DEV u32x4 ld16_async(const char* p) { u32x4 v; asm volatile("global_load_dwordx4 %0, %1, off sc1" : "=v"(v) : "v"(p) : "memory"); return v; }
DEV u64 ld8_async(const char* p) { u64 v; asm volatile("global_load_dwordx2 %0, %1, off sc1" : "=v"(v) : "v"(p) : "memory"); return v; }
DEV void touch(const char* p, unsigned dump) {
  unsigned keep;
  asm volatile("s_mov_b32 %0, m0\n\ts_mov_b32 m0, %2\n\ts_nop 0\n\tglobal_load_lds_dword %1, off\n\ts_mov_b32 m0, %0" : "=&s"(keep) : "v"(p), "s"(dump) : "memory");
}
template <int CTRL> DEV float dpp_add(float x) { return x + __builtin_bit_cast(float, __builtin_amdgcn_update_dpp(0, __builtin_bit_cast(int, x), CTRL, 0xf, 0xf, false)); }
DEV float sum16(float x) { x = dpp_add<0xB1>(x); x = dpp_add<0x4E>(x); x = dpp_add<0x141>(x); return dpp_add<0x140>(x); }


template <int N> struct IC { static constexpr int value = N; };
template <int B, int E, class F> DEV void static_for(F&& f) { if constexpr (B < E) { f(IC<B>{}); static_for<B + 1, E>(f); } }
constexpr int lead1(int k) { return k >= 6 ? LEADL : LEADE; }
constexpr int sched1(int t) { const int k = t / 16, e0 = t - (k >= 6 ? LOOKL : LOOKE), e1 = 16 * k - lead1(k) + (t - 16 * k) / 2; return k == 0 ? (t - 8 > 0 ? (t - 8) / 2 : 0) : (e0 > e1 ? e0 : e1); }

struct Ctx {
  const char *x, *Cr, *Ci, *Wr, *Wi, *w1, *b1, *w2, *b2; char* out; char* ws; LAS char* lds; const char* pc; unsigned lb;
  int wid, tp, ec, ks;
  int PA, rot1;
  bool two;
};

template <class P> DEV u64 poll(const char* p, P ok) {
  const u64 t0 = __builtin_amdgcn_s_memrealtime();
  u64 v;
  for (;;) {
    v = __hip_atomic_load((GAS u64*)p, __ATOMIC_RELAXED, __HIP_MEMORY_SCOPE_AGENT);
    if (__all(ok(v))) break;
    if (__builtin_amdgcn_s_memrealtime() - t0 > 20000000ull) break;
    __builtin_amdgcn_s_sleep(1);
  }
  return v;
}
DEV void set_flag(char* ws, int kind, int idx, u64 tag) {
  __hip_atomic_store((GAS u64*)(ws + WS_FLAG + (unsigned)(kind * 256 + idx) * FLAG_STRIDE), tag, __ATOMIC_RELAXED, __HIP_MEMORY_SCOPE_AGENT);
}
DEV u64 uniform64(u64 v) {
  const unsigned lo = __builtin_amdgcn_readfirstlane((unsigned)v), hi = __builtin_amdgcn_readfirstlane((unsigned)(v >> 32));
  return ((u64)hi << 32) | lo;
}
#define PROW(p) (32 * ((p) >> 2) + 16 * ((p) & 1) + 4 * (((p) >> 1) & 1))
#define ROTN(seq, n) do { seq = (seq >> 4) | ((seq & 15) << (4 * (n) - 4)); } while (0)

DEV void unit_rows(u32x4 d, unsigned v, unsigned so, unsigned rowb, unsigned dst) {
#pragma unroll
  for (int g = 0; g < 4; ++g) {
    dma4<false>(d, v, v, v, v, so, so + PROW(1) * rowb, so + PROW(2) * rowb, so + PROW(3) * rowb, dst);
    so += 32 * rowb; dst += 4096;
  }
}
template <bool DS> DEV void unit_step(u32x4 d, unsigned v0, unsigned v1, unsigned so, unsigned step, unsigned dst, int n4) {
#pragma unroll
  for (int g = 0; g < n4; ++g) {
    dma4<DS>(d, v0, v1, v0, v1, so, so + step, so + 2 * step, so + 3 * step, dst);
    so += 4 * step; dst += 4096;
  }
}
DEV void issueAB(const Ctx& c, int s, int slot, unsigned vL) {
  const u32x4 d = mk_desc(c.ws + WS_ABT + (size_t)(((c.ks >> 1) * 4 + c.ec) * 64 + (c.ks & 1) * 32) * 8192, 262144);
  unit_step<true>(d, vL, vL, s * 16384, 1024, c.lb + slot * SLOT, 4);
}
DEV void issueJ(const Ctx& c, int j, unsigned vJ) {
  const u32x4 d = mk_desc(c.w1, D_ * W1R);
#pragma unroll
  for (int h = 0; h < 2; ++h) unit_step<false>(d, vJ, vJ ^ 64, ((2 * j + h + c.rot1) & 7) * (64 * W1R) + (c.PA + (c.two ? 1 : 0)) * 128, 8 * W1R, c.lb + ((JSEQ >> (4 * j)) & 15) * SLOT + h * 8192, 2);
}
DEV void h_pack(const Ctx& c, const f32x4 (&acc)[4][2], int col0, int rg, u32x4 (&d)[4]) {
  LAS char* lds = c.lds;
  const int lane = fresh_lane(), q = lane >> 4, cc = lane & 15;
  float mean[2], rstd[2];
#pragma unroll
  for (int u = 0; u < 2; ++u) { mean[u] = *(const LAS float*)(lds + LDS_MEAN + ((2 * rg + u) * 16 + cc) * 4); rstd[u] = *(const LAS float*)(lds + LDS_RSTD + ((2 * rg + u) * 16 + cc) * 4); }
  LAS char* tb = lds + col0 * 4 + q * 16;
  f32x4 cs[4], bv[4];
#pragma unroll
  for (int n = 0; n < 4; ++n) { cs[n] = *(const LAS f32x4*)(tb + LDS_CS1 + n * 64); bv[n] = *(const LAS f32x4*)(tb + LDS_B1 + n * 64); }
#pragma unroll
  for (int b = 0; b < 2; ++b)
#pragma unroll
    for (int u = 0; u < 2; ++u)
#pragma unroll
      for (int h = 0; h < 2; ++h) {
        f32x4 z = (acc[2 * b + h][u] - mean[u] * cs[2 * b + h]) * rstd[u] + bv[2 * b + h];
        z = __builtin_elementwise_max(z, (f32x4){0.f, 0.f, 0.f, 0.f});
        z = z * z;
        d[2 * b + u][2 * h] = pk_bf16(z[0], z[1]); d[2 * b + u][2 * h + 1] = pk_bf16(z[2], z[3]);
      }
}
DEV void h_store(const Ctx& c, u32x4 d, int col0, int rg, int i, unsigned vL) {
  const rsrc_t rH = mk_rsrc(c.ws + WS_H + (size_t)((((2 * c.tp + (rg >> 1)) * 56 + 2 * c.PA + (col0 >> 5)) * 4 + 2 * (rg & 1)) * 1024), 8192 - 2048);
  __builtin_amdgcn_raw_buffer_store_b128(d, rH, vL + (i & 1) * 1024 + (i >> 1) * 4096, 0, 16);
}

DEV void loaderA(const Ctx& c) {
  const int lane = fresh_lane(), r8 = lane >> 3, p8 = lane & 7;
  char* ws = c.ws;
  const unsigned lb = c.lb, rl = 8 * (r8 >> 2) + (r8 & 3), swz = (unsigned)((p8 ^ r8) * 16);
  const u32x4 dX = mk_desc(c.x + (size_t)c.tp * 131072, 131072);
  const unsigned vX = rl * 1024 + swz;
  auto issueX = [&](int s) { unit_rows(dX, vX, ((s + c.rot1) & 7) * 128, 1024, lb + (s & 3) * SLOT); };
#pragma unroll 1
  for (int s = 0; s < 3; ++s) issueX(s);
  const u32x4 dC = mk_desc((c.ks >> 1 ? c.Ci : c.Cr) + (size_t)c.tp * 524288 + (c.ks & 1) * 2048, 524288 - 2048);
  const u32x4 dH = mk_desc(ws + WS_H + (size_t)(2 * c.tp + (c.ks >> 1)) * (56 * 4096), 56 * 4096);
  const unsigned vC = rl * 4096 + swz, vL = lane * 16, vJ = r8 * W1R + (p8 ^ (r8 & 2)) * 16;
  auto issueC = [&](int s) { unit_rows(dC, vC, s * 128, 4096, lb + (s & 3) * SLOT); };
  wait_vm<32>();
  bar();
#pragma unroll 1
  for (int s = 0; s < 5; ++s) { issueX(s + 3); wait_vm<32>(); bar(); }
  issueJ(c, 1, vJ); wait_vm<32>(); bar();
#pragma unroll 1
  for (int s = 0; s < 2; ++s) { issueC(s); wait_vm<32>(); bar(); }
  const u64 tag = uniform64(*(const LAS u64*)(c.lds + LDS_TAG));
  bar();
#pragma unroll 1
  for (int s = 2; s < 4; ++s) { issueC(s); if (s == 2) bar(); }
  {
    const unsigned so = (c.ks >> 1) * 65536 + (c.ec * 128 + (c.ks & 1) * 64) * 2, vo = r8 * 1024 + (p8 ^ ((r8 >> 1) & 1)) * 16;
#pragma unroll
    for (int g = 0; g < 2; ++g) dma4<false>(dX, vo, vo ^ 32, vo ^ 64, vo ^ 96, so + g * 32768, so + g * 32768 + 8192, so + g * 32768 + 16384, so + g * 32768 + 24576, lb + LDS_XT + g * 4096);
  }
  wait_vm<56>();
  bar();
  issueAB(c, 3, nib(BSEQ, 3), vL);
  const char* hfp = ws + WS_FLAG + (unsigned)(1 * 256 + c.tp * 16 + (lane & 15)) * FLAG_STRIDE;
  u64 fv = 0;
#pragma unroll 1
  for (int s = 0; s < 12; ++s) { if (s < 2) wait_vm<56>(); else wait_vm<32>(); bar(); issueC(s + 4); if (s == HPOLL) fv = ld8_async(hfp); }
  wait_vm<32>();
  asm volatile("" : "+v"(fv));
  if (!__all(fv == tag)) poll(hfp, [&](u64 x) { return x == tag; });
#pragma unroll 1
  for (int s = 12; s < 26; ++s) { wait_vm<32>(); bar(); unit_step<true>(dH, vL, vL, (s - 12) * 16384, 1024, lb + (s & 3) * SLOT, 4); }
  wait_vm<32>(); bar();
  wait_vm<16>(); bar();
  wait_vm<0>(); bar();
}

DEV void loaderB(const Ctx& c) {
  const int lane = fresh_lane(), r8 = lane >> 3, p8 = lane & 7;
  const unsigned lb = c.lb;
  const u32x4 dW = mk_desc((c.ks >> 1 ? c.Wi : c.Wr) + (size_t)((c.ks & 1) * 4 + c.ec) * 65536, 65536), d1 = mk_desc(c.w1, D_ * W1R);
  const unsigned vW = r8 * 1024 + (p8 ^ r8) * 16, vJ = r8 * W1R + (p8 ^ (r8 & 2)) * 16;
  auto issueB = [&](int s) {
    const int k = (s + c.rot1) & 7;
    unit_step<false>(dW, vW, vW, k * 128, 8192, lb + (4 + (s & 3)) * SLOT, 2);
    unit_step<false>(d1, vJ, vJ ^ 64, k * (64 * W1R) + c.PA * 128, 8 * W1R, lb + (4 + (s & 3)) * SLOT + 8192, 2);
  };
#pragma unroll 1
  for (int s = 0; s < 3; ++s) issueB(s);
  const u32x4 d2 = mk_desc(c.w2 + (c.ec * 128 + (c.ks & 1) * 64) * 2, F_ * 1024 - 1024 + 128);
  const unsigned vL = lane * 16, v2 = r8 * 1024 + (p8 ^ (r8 & 6)) * 16;
  wait_vm<32>();
  bar();
#pragma unroll 1
  for (int s = 0; s < 5; ++s) { issueB(s + 3); wait_vm<32>(); bar(); }
#pragma unroll 1
  for (int s = 5; s < 8; ++s) { issueJ(c, s == 5 ? 0 : s - 4, vJ); wait_vm<32>(); bar(); }
  const u64 tag = uniform64(*(const LAS u64*)(c.lds + LDS_TAG));
  bar();
  wait_vm<0>();
  bar();
  const int pi = (lane >> 1) & 7;
  const char* afp = c.ws + WS_FLAG + (unsigned)((8 * (c.ks & 1) + pi) * 16 + 8 * (c.ks >> 1) + 2 * c.ec + (lane & 1)) * FLAG_STRIDE;
  u64 fv = poll(afp, [&](u64 x) { return x == tag || pi > 0; });
  unsigned bseq = BSEQ;
#pragma unroll 1
  for (int s = 0; s < 2; ++s) { issueAB(c, s, bseq & 15, vL); ROTN(bseq, 5); }
  if (!__all(fv == tag || pi > 1)) fv = poll(afp, [&](u64 x) { return x == tag || pi > 1; });
  wait_vm<16>();
  bar();
  issueAB(c, 2, nib(BSEQ, 2), vL);
  ROTN(bseq, 5); ROTN(bseq, 5);
  if (!__all(fv == tag)) {
    fv = ld8_async(afp);
    wait_vm<17>(); bar();
    wait_vm<0>();
    asm volatile("" : "+v"(fv));
    if (!__all(fv == tag)) poll(afp, [&](u64 x) { return x == tag; });
  } else { wait_vm<16>(); bar(); }
  issueAB(c, 4, bseq & 15, vL); ROTN(bseq, 5);
#pragma unroll 1
  for (int s = 1; s < 12; ++s) { if (s < 2) wait_vm<16>(); else wait_vm<32>(); bar(); issueAB(c, s + 4, bseq & 15, vL); ROTN(bseq, 5); }
#pragma unroll 1
  for (int s = 12; s < 26; ++s) { wait_vm<32>(); bar(); unit_step<false>(d2, v2, v2, (s - 12) * 131072, 8192, lb + (4 + (s & 3)) * SLOT, 4); }
  wait_vm<32>(); bar();
  wait_vm<16>(); bar();
  wait_vm<0>(); bar();
}

DEV void helper(const Ctx& c) {
  __builtin_amdgcn_s_setprio(3);
  const int hw = c.wid - 6, lane = fresh_lane();
  LAS char* lds = c.lds; char* ws = c.ws;
  const int q = lane >> 4, cc = lane & 15, qq = cc >> 2, cp = c.ks * 4 + c.ec;
  const unsigned dump = c.lb + LDS_DUMP;
  const char* Cp = (c.ks >> 1 ? c.Ci : c.Cr) + (size_t)(c.tp * 128 + c.ec * 32 + (lane >> 2)) * 4096 + (c.ks & 1) * 2048 + (lane & 3) * 128;
  auto prefC = [&]() { if (hw == 1) { touch(Cp, dump); touch(Cp + 16 * 4096, dump); Cp += 512; } };
  u64 tag;
  {
    const u64 tagv = __hip_atomic_load((GAS u64*)(ws + WS_EPOCH), __ATOMIC_RELAXED, __HIP_MEMORY_SCOPE_AGENT);
    const int row = c.tp * 128 + cp * 8 + 4 * hw + q, wrow = cp * 64 + c.tp * 4 + 2 * hw + (lane >> 5);
    const char* wp = (c.ks >> 1 ? c.Wi : c.Wr) + (size_t)(wrow & 511) * 1024 + (lane & 31) * 16;
    const char* p1 = c.w1 + (size_t)(c.tp * 32 + (lane >> 3)) * W1R + (c.PA + (c.two ? hw : 0)) * 128 + (lane & 7) * 16;
    char* pubp = cc == 0 ? ws + WS_ROWST + (size_t)row * 8 : ws + WS_CSW + (size_t)wrow * 8;
    const rsrc_t rP = mk_rsrc(ws + WS_CS1 + (size_t)(cp * 16 + c.tp) * 1024 + hw * 512, 512);
    u32x4 xv[4], dv[2], wv[4];
#pragma unroll
    for (int i = 0; i < 4; ++i) xv[i] = *(const u32x4*)(c.x + (size_t)row * 1024 + cc * 16 + i * 256);
    dv[0] = *(const u32x4*)wp; dv[1] = *(const u32x4*)(wp + 512);
#pragma unroll
    for (int i = 0; i < 4; ++i) wv[i] = *(const u32x4*)(p1 + i * (8 * W1R));
    const u16 b1v = *(const u16*)(c.b1 + ((c.PA + (c.two ? hw : 0)) * 64 + lane) * 2);
    const u16 b2v = *(const u16*)(c.b2 + (c.ec * 128 + (c.ks & 1) * 64 + lane) * 2);
#define CHUNK() do { SCHED(); rawbar(); SCHED(); } while (0)
    CHUNK();
    float s0 = 0.f, s1 = 0.f, sw = 0.f;
#pragma unroll
    for (int i = 0; i < 4; ++i) { s0 = dot2(xv[i].x, ONE2, s0); s1 = dot2(xv[i].y, ONE2, s1); s0 = dot2(xv[i].z, ONE2, s0); s1 = dot2(xv[i].w, ONE2, s1); }
#pragma unroll
    for (int i = 0; i < 8; ++i) sw = dot2(dv[i >> 2][i & 3], ONE2, sw);
    const float m = sum16(s0 + s1) * (1.f / 512.f);
    sw = sum16(sw);
    { const auto r = __builtin_amdgcn_permlane16_swap(__float_as_uint(sw), __float_as_uint(sw), false, false); sw = __uint_as_float(r[0]) + __uint_as_float(r[1]); }
    f32x4 z4 = {0.f, 0.f, 0.f, 0.f}, z5 = z4;
#pragma unroll
    for (int i = 0; i < 2; ++i) { const f32x4 d0 = unpk(xv[i].x, xv[i].y) - m, d1 = unpk(xv[i].z, xv[i].w) - m; z4 += d0 * d0; z5 += d1 * d1; }
    CHUNK();
#pragma unroll
    for (int i = 2; i < 4; ++i) { const f32x4 d0 = unpk(xv[i].x, xv[i].y) - m, d1 = unpk(xv[i].z, xv[i].w) - m; z4 += d0 * d0; z5 += d1 * d1; }
    z4 += z5;
    const float var = sum16((z4[0] + z4[1]) + (z4[2] + z4[3])) * (1.f / 512.f);
    const unsigned sv = pk_bf16(m, __builtin_amdgcn_sqrtf(rbf(rbf(var) + 1e-5f)));
    tag = uniform64(tagv) + 1;
    __builtin_amdgcn_s_waitcnt(0x0F70);
    if (cc < 2 && (cc == 0 || (q & 1) == 0))
      __hip_atomic_store((GAS u64*)pubp, ((u64)(unsigned)tag << 32) | (cc == 0 ? sv : __float_as_uint(sw)), __ATOMIC_RELAXED, __HIP_MEMORY_SCOPE_AGENT);
    CHUNK();
    prefC();
    float t[8];
#pragma unroll
    for (int j = 0; j < 4; ++j) {
      t[2 * j] = (bflo(wv[0][j]) + bflo(wv[1][j])) + (bflo(wv[2][j]) + bflo(wv[3][j]));
      t[2 * j + 1] = (bfhi(wv[0][j]) + bfhi(wv[1][j])) + (bfhi(wv[2][j]) + bfhi(wv[3][j]));
    }
    *(LAS float*)(lds + LDS_B1 + (hw * 64 + lane) * 4) = bflo(b1v);
    *(LAS float*)(lds + LDS_B2 + lane * 4) = bflo(b2v);
    if (hw == 0 && lane == 0) *(LAS u64*)(lds + LDS_TAG) = tag;
    CHUNK();
    float u4[4], u2[2];
#pragma unroll
    for (int j = 0; j < 4; ++j) { const auto r = __builtin_amdgcn_permlane32_swap(__float_as_uint(t[j]), __float_as_uint(t[j + 4]), false, false); u4[j] = __uint_as_float(r[0]) + __uint_as_float(r[1]); }
#pragma unroll
    for (int j = 0; j < 2; ++j) { const auto r = __builtin_amdgcn_permlane16_swap(__float_as_uint(u4[j]), __float_as_uint(u4[j + 2]), false, false); u2[j] = dpp_add<0x128>(__uint_as_float(r[0]) + __uint_as_float(r[1])); }
    if (cc < 8) __builtin_amdgcn_raw_buffer_store_b128((u32x4){__float_as_uint(u2[0]), (unsigned)tag, __float_as_uint(u2[1]), (unsigned)tag}, rP, (cc * 4 + q) * 16, 0, 16);
    const char* stp = ws + WS_ROWST + (size_t)(c.tp * 128 + 64 * hw + 32 * (lane >> 5) + 8 * qq + 4 * (q & 1) + (lane & 3)) * 8;
    const char* cwp = ws + WS_CSW + (size_t)(cp * 64 + lane) * 8;
    wait_lgkm0();
    bar();
    u64 st = ld8_async(stp), cw = ld8_async(cwp);
    bar(); bar();
    const unsigned t32 = (unsigned)tag;
    wait_vm<0>();
    asm volatile("" : "+v"(st), "+v"(cw));
    const bool ok1 = __all((unsigned)(st >> 32) == t32 && (unsigned)(cw >> 32) == t32);
    if (!ok1) { st = ld8_async(stp); cw = ld8_async(cwp); }
    bar();
    if (!ok1) {
      wait_vm<0>();
      asm volatile("" : "+v"(st), "+v"(cw));
      if (!__all((unsigned)(st >> 32) == t32)) st = poll(stp, [&](u64 x) { return (unsigned)(x >> 32) == t32; });
      if (!__all((unsigned)(cw >> 32) == t32)) cw = poll(cwp, [&](u64 x) { return (unsigned)(x >> 32) == t32; });
    }
    *(LAS float*)(lds + LDS_MEAN + hw * 256 + lane * 4) = bflo((unsigned)st);
    *(LAS float*)(lds + LDS_RSTD + hw * 256 + lane * 4) = __builtin_amdgcn_rcpf(bfhi((unsigned)st));
    *(LAS unsigned*)(lds + LDS_CSW + lane * 4) = (unsigned)cw;
    const char* pvp = ws + WS_CS1 + (size_t)(cp * 16 + (lane >> 5)) * 1024 + hw * 512 + (lane & 31) * 16;
    u32x4 pv[8];
#pragma unroll
    for (int i = 0; i < 8; ++i) pv[i] = ld16_async(pvp + i * 2048);
    wait_lgkm0();
    bar();
    const u64 t0 = __builtin_amdgcn_s_memrealtime();
    for (;;) {
      wait_vm<0>();
      asm volatile("" : "+v"(pv[0]), "+v"(pv[1]), "+v"(pv[2]), "+v"(pv[3]), "+v"(pv[4]), "+v"(pv[5]), "+v"(pv[6]), "+v"(pv[7]));
      bool ok = true;
#pragma unroll
      for (int i = 0; i < 8; ++i) ok = ok && pv[i].y == t32 && pv[i].w == t32;
      if (__all(ok) || __builtin_amdgcn_s_memrealtime() - t0 > 20000000ull) break;
#pragma unroll
      for (int i = 0; i < 8; ++i) pv[i] = ld16_async(pvp + i * 2048);
    }
    float a0 = 0.f, a1 = 0.f;
#pragma unroll
    for (int i = 0; i < 8; ++i) { a0 += __uint_as_float(pv[i].x); a1 += __uint_as_float(pv[i].z); }
    auto r = __builtin_amdgcn_permlane32_swap(__float_as_uint(a0), __float_as_uint(a0), false, false);
    a0 = __uint_as_float(r[0]) + __uint_as_float(r[1]);
    r = __builtin_amdgcn_permlane32_swap(__float_as_uint(a1), __float_as_uint(a1), false, false);
    a1 = __uint_as_float(r[0]) + __uint_as_float(r[1]);
    *(LAS f32x2*)(lds + LDS_CS1 + hw * 256 + (lane & 31) * 8) = (f32x2){a0, a1};
    wait_lgkm0();
  }
  bar();
  if (hw == 0 && lane == 0) set_flag(ws, 0, c.tp * 16 + cp, tag);
  bar();
  prefC();
  {
    const char* Wp = c.w2 + (c.ec * 128 + (c.ks & 1) * 64) * 2 + (size_t)((c.tp & 7) * 224 + hw * 112 + (lane < 56 ? lane : 0)) * 1024;
    const bool ew = blockIdx.x == 0 && hw == 1;
    const char* fp = ws + WS_FLAG + (unsigned)lane * FLAG_STRIDE;
    u64 f0 = 0, f1 = 0, f2 = 0, f3 = 0;
    auto run = [&](int s0, int s1) {
#pragma unroll 1
      for (int s = s0; s < s1; ++s) {
        if (s == PREFW) { touch(Wp, dump); touch(Wp + 56 * 1024, dump); }
        if ((PM2 >> (s + 1)) & 1) prefC();
        bar();
        if (hw == 0 && lane == 0) {
          if (s == HFLAG) set_flag(ws, 1, c.tp * 16 + cp, tag);
          if (s == YFLAG) set_flag(ws, 2, (c.tp * 4 + c.ec) * 4 + c.ks, tag);
        }
      }
    };
    run(-1, 10);
    if (ew) { f0 = ld8_async(fp); f1 = ld8_async(fp + 64 * FLAG_STRIDE); f2 = ld8_async(fp + 128 * FLAG_STRIDE); f3 = ld8_async(fp + 192 * FLAG_STRIDE); }
    run(10, 15);
    wait_vm<0>();
    asm volatile("" : "+v"(f0), "+v"(f1), "+v"(f2), "+v"(f3));
    const bool early = ew && __all(f0 == tag && f1 == tag && f2 == tag && f3 == tag);
    if (early && lane == 0) __hip_atomic_store((GAS u64*)(ws + WS_EPOCH), tag, __ATOMIC_RELAXED, __HIP_MEMORY_SCOPE_AGENT);
    run(15, YPOLL + 1);
    const char* yfp = ws + WS_FLAG + (unsigned)(2 * 256 + (c.tp * 4 + c.ec) * 4 + (lane & 3)) * FLAG_STRIDE;
    f0 = ld8_async(yfp);
    run(YPOLL + 1, YPOLL + 2);
    f1 = ld8_async(yfp);
    run(YPOLL + 2, YCHK);
    wait_vm<1>();
    asm volatile("" : "+v"(f0));
    if (!__all(f0 == tag)) {
      wait_vm<0>();
      asm volatile("" : "+v"(f1));
      if (!__all(f1 == tag)) poll(yfp, [&](u64 x) { return x == tag; });
    }
    run(YCHK, 29);
    wait_vm<0>();
    asm volatile("" : "+v"(f1));
    if (ew && !early) {
#pragma unroll 1
      for (int i = 0; i < 4; ++i) poll(fp + i * 64 * FLAG_STRIDE, [&](u64 x) { return x == tag; });
      if (lane == 0) __hip_atomic_store((GAS u64*)(ws + WS_EPOCH), tag, __ATOMIC_RELAXED, __HIP_MEMORY_SCOPE_AGENT);
    }
    wait_vm<0>();
  }
}

DEV void consumer2(const Ctx& c) {
  const int w = c.wid, lane = fresh_lane();
  LAS char* lds = c.lds;
  const int q = lane >> 4, cc = lane & 15, qq = cc >> 2, pp = lane & 3;
  const unsigned wa0 = cc * 128 + ((q ^ (cc & 7)) * 16), wa1 = cc * 128 + (((4 + q) ^ (cc & 7)) * 16);
  const int wt = w >> 1, we = w & 1;
  unsigned wb[2];
#pragma unroll
  for (int n = 0; n < 2; ++n) wb[n] = (4 * q + qq) * 128 + (((2 * (2 * we + n) + (pp >> 1)) ^ ((4 * q + qq) & 6)) * 16) + 8 * (pp & 1);
  const unsigned hb = wt * 2048 + lane * 16;
  bf16x8 hA[4][2], wA[4][2], hB[4][2], wB[4][2];
  wait_lgkm0();
  bar();
#define PREAD(HN, WN, SO, SW) if (i < 12) { if (i % 3 == 2) { HN[i / 3][0] = ldsb128(lds + (SO) + hb + (i / 3) * 4096); HN[i / 3][1] = ldsb128(lds + (SO) + hb + (i / 3) * 4096 + 1024); } \
                                            else WN[i / 3][i % 3] = ldstr(lds + (SW) + wb[i % 3] + (i / 3) * 4096, 2048); SCHED(); }
  {
    f32x4 acc[4][4];
#pragma unroll
    for (int m = 0; m < 4; ++m)
#pragma unroll
      for (int n = 0; n < 4; ++n) acc[m][n] = (f32x4){0, 0, 0, 0};
    const unsigned a0 = wt * 8192 + wa0, a1 = wt * 8192 + wa1, b0 = we * 4096 + lane * 16;
    bf16x8 fa0[4], fb0[4], fa1[4], fb1[4];
#pragma unroll
    for (int m = 0; m < 4; ++m) { fa0[m] = ldsb128(lds + nib(CSEQ, 0) * SLOT + a0 + m * 2048); fb0[m] = ldsb128(lds + nib(BSEQ, 0) * SLOT + b0 + m * 1024); }
#define HALF(FA, FB, NA_, NB_, AOFF, BOFF) do { _Pragma("unroll") for (int i = 0; i < 16; ++i) { \
      acc[i >> 2][i & 3] = MFMA(FB[i & 3], FA[i >> 2], acc[i >> 2][i & 3]); SCHED(); \
      if (i < 8) { if (i == 0) NB_[0] = ldsb128(lds + (BOFF)); else if (i == 1) NA_[0] = ldsb128(lds + (AOFF)); \
        else if (i < 5) NB_[i - 1] = ldsb128(lds + (BOFF) + (i - 1) * 1024); else NA_[i - 4] = ldsb128(lds + (AOFF) + (i - 4) * 2048); SCHED(); } } } while (0)
    unsigned so = nib(CSEQ, 0) * SLOT, sb = nib(BSEQ, 0) * SLOT, cseq = CSEQ, bseq = BSEQ;
#define YSTAGE() do { HALF(fa0, fb0, fa1, fb1, so + a1, sb + b0 + 8192); wait_lgkm0(); bar(); \
        ROTN(bseq, 5); ROTN(cseq, 4); sb = (bseq & 15) * SLOT; so = (cseq & 15) * SLOT; HALF(fa1, fb1, fa0, fb0, so + a0, sb + b0); } while (0)
#pragma unroll 1
    for (int seg = 0; seg < 2; ++seg) {
      const int n = seg ? 15 - HFLAG : HFLAG;
#pragma unroll 1
      for (int s = 0; s < n; ++s) YSTAGE();
      wait_vm<0>();
    }
    HALF(fa0, fb0, fa1, fb1, so + a1, sb + b0 + 8192);
    wait_lgkm0();
    bar();
#pragma unroll
    for (int i = 0; i < 16; ++i) {
      acc[i >> 2][i & 3] = MFMA(fb1[i & 3], fa1[i >> 2], acc[i >> 2][i & 3]); SCHED();
      PREAD(hA, wA, nib(CSEQ, 0) * SLOT, nib(WSEQ, 0) * SLOT)
    }
    f32x4 ao[2][2];
#pragma unroll
    for (int m = 0; m < 2; ++m) { ao[m][0] = (f32x4){0, 0, 0, 0}; ao[m][1] = (f32x4){0, 0, 0, 0}; }
    unsigned hseq = CSEQ, wseq = WSEQ;
#define PBAR() do { wait_lgkm0(); bar(); SCHED(); } while (0)
#define PMMA(HC, WC, HN, WN) do { ROTN(hseq, 4); ROTN(wseq, 4); const unsigned sh = (hseq & 15) * SLOT, sw = (wseq & 15) * SLOT; SCHED(); \
      _Pragma("unroll") for (int i = 0; i < 16; ++i) { \
        ao[(i >> 1) & 1][i & 1] = MFMA(WC[i >> 2][i & 1], HC[i >> 2][(i >> 1) & 1], ao[(i >> 1) & 1][i & 1]); SCHED(); \
        PREAD(HN, WN, sh, sw) } } while (0)
#define PSTEP(HC, WC, HN, WN) do { PBAR(); PMMA(HC, WC, HN, WN); } while (0)
    auto own = [&](int m0) {
      if (w == c.ks) {
#pragma unroll
        for (int m = m0; m < m0 + 2; ++m)
#pragma unroll
          for (int n = 0; n < 4; ++n) *(LAS f32x4*)(lds + 8 * SLOT + (m * 4 + n) * 1024 + lane * 16) = acc[m][n];
      }
    };
    PBAR();
    if (w != c.ks) {
      const rsrc_t rY = mk_rsrc(c.ws + WS_YP + (size_t)(((c.tp * 4 + c.ec) * 4 + c.ks) * 4 + w) * 8192, 8192);
#pragma unroll
      for (int m = 0; m < 4; ++m)
#pragma unroll
        for (int j = 0; j < 2; ++j) {
          const f32x4 e = acc[m][2 * j], o = acc[m][2 * j + 1];
          __builtin_amdgcn_raw_buffer_store_b128((u32x4){pk_bf16(e[0], e[1]), pk_bf16(e[2], e[3]), pk_bf16(o[0], o[1]), pk_bf16(o[2], o[3])}, rY, lane * 16 + (m * 2 + j) * 1024, 0, 16);
        }
    }
    own(0);
    SCHED();
    PMMA(hA, wA, hB, wB);
    PBAR();
    own(2);
    SCHED();
    PMMA(hB, wB, hA, wA);
    static_assert(YFLAG == 18 && YCHK == 25, "schedule");
    wait_vm<0>();
#pragma unroll 1
    for (int s = 0; s < 4; ++s) { PSTEP(hA, wA, hB, wB); PSTEP(hB, wB, hA, wA); }
    const rsrc_t rY = mk_rsrc(c.ws + WS_YP + (size_t)((c.tp * 4 + c.ec) * 16 + c.ks) * 8192, 4 * 32768 - 3 * 8192);
    u32x4 yp[3][2];
    const unsigned yv = lane * 16 + (4 * wt + we) * 1024;
#pragma unroll
    for (int i = 0; i < 3; ++i) yp[i][0] = __builtin_amdgcn_raw_buffer_load_b128(rY, yv, ((c.ks + 1 + i) & 3) * 32768, 16);
    SCHED();
    PSTEP(hA, wA, hB, wB);
#pragma unroll
    for (int i = 0; i < 3; ++i) yp[i][1] = __builtin_amdgcn_raw_buffer_load_b128(rY, yv + 2048, ((c.ks + 1 + i) & 3) * 32768, 16);
    const int tl = 32 * wt + 8 * qq + (cc & 3), el = 32 * we + 4 * q;
    f32x4 y[2][2], bv[2];
    u32x2 xr[2][2];
#pragma unroll
    for (int n = 0; n < 2; ++n) {
      bv[n] = *(const LAS f32x4*)(lds + LDS_B2 + (el + 16 * n) * 4);
#pragma unroll
      for (int m = 0; m < 2; ++m) {
        y[m][n] = *(const LAS f32x4*)(lds + 8 * SLOT + ((2 * wt + m) * 4 + 2 * we + n) * 1024 + lane * 16);
        xr[m][n] = *(const LAS u32x2*)(lds + LDS_XT + (tl + 4 * m) * 128 + (((el + 16 * n) * 2) ^ ((2 * qq + ((cc >> 1) & 1)) * 16)));
      }
    }
    SCHED();
    PSTEP(hB, wB, hA, wA);
#pragma unroll
    for (int i = 0; i < 3; ++i) { y[0][0] += unpk(yp[i][0].x, yp[i][0].y); y[0][1] += unpk(yp[i][0].z, yp[i][0].w); }
    char* op = c.out + (size_t)(c.tp * 128 + (c.ks >> 1) * 64 + tl) * 1024 + (c.ec * 128 + (c.ks & 1) * 64 + el) * 2;
    f32x4 xo[2][2];
#pragma unroll
    for (int m = 0; m < 2; ++m)
#pragma unroll
      for (int n = 0; n < 2; ++n) { xo[m][n] = unpk(xr[m][n].x, xr[m][n].y); ao[m][n] += bv[n]; }
    SCHED();
    PSTEP(hA, wA, hB, wB);
#pragma unroll
    for (int i = 0; i < 3; ++i) { y[1][0] += unpk(yp[i][1].x, yp[i][1].y); y[1][1] += unpk(yp[i][1].z, yp[i][1].w); }
    SCHED();
#pragma unroll
    for (int i = 0; i < 16; ++i) ao[i >> 3][(i >> 2) & 1] = MFMA(wB[i & 3][(i >> 2) & 1], hB[i & 3][i >> 3], ao[i >> 3][(i >> 2) & 1]);
#pragma unroll
    for (int m = 0; m < 2; ++m)
#pragma unroll
      for (int n = 0; n < 2; ++n) {
        f32x4 o = xo[m][n];
#pragma unroll
        for (int r = 0; r < 4; ++r) { const float z = ao[m][n][r]; o[r] += y[m][n][r] * (z * __builtin_amdgcn_rcpf(1.f + __expf(-z))); }
        *(u32x2*)(op + m * 4096 + n * 32) = (u32x2){pk_bf16(o[0], o[1]), pk_bf16(o[2], o[3])};
      }
  }
}

DEV void consumer(const Ctx& c) {
  const int w = c.wid;
  LAS char* lds = c.lds;
  unsigned ja[4], bx0, bx1, bw0, bw1;
  {
    const int lane = fresh_lane(), q = lane >> 4, cc = lane & 15, qq = cc >> 2, pp = lane & 3;
    touch(c.pc + (PREFI + w * 64 + lane) * 128, c.lb + LDS_DUMP);
    const unsigned wa0 = cc * 128 + ((q ^ (cc & 7)) * 16), wa1 = cc * 128 + (((4 + q) ^ (cc & 7)) * 16);
#pragma unroll
    for (int n = 0; n < 4; ++n) ja[n] = opq(4 * SLOT + (8 * q + qq) * 128 + (((2 * n + (pp >> 1)) ^ ((qq & 2) | ((q & 1) << 2))) * 16) + 8 * (pp & 1));
    bx0 = opq(wa0 + w * 4096); bx1 = opq(wa1 + w * 4096); bw0 = opq(wa0 + 4 * SLOT); bw1 = opq(wa1 + 4 * SLOT);
  }
  bf16x8 xf[2][16];
  bar();
  f32x4 ach[4][2];
  {
    f32x4 acc[4][2];
#pragma unroll
    for (int n = 0; n < 4; ++n) { acc[n][0] = acc[n][1] = ach[n][0] = ach[n][1] = (f32x4){0, 0, 0, 0}; }
    bf16x8 fr[128];
    auto rd = [&](auto t_) {
      constexpr int t = decltype(t_)::value, kk = t / 8, n = t % 8, so = ((kk >> 1) & 3) * SLOT;
      if constexpr (n == 0) { xf[0][kk] = ldsb128(lds + ((kk & 1) ? bx1 : bx0) + so); xf[1][kk] = ldsb128(lds + ((kk & 1) ? bx1 : bx0) + so + 2048); }
      if constexpr ((n & 1) == 0) fr[t] = ldsb128(lds + ((kk & 1) ? bw1 : bw0) + so + (n >> 1) * 2048);
      else fr[t] = ldstr(lds + ja[n >> 1] + so + 8192 + (kk & 1) * 4096, 512);
    };
    static_for<0, 8>(rd);
    SCHED();
    static_for<0, 128>([&](auto p_) {
      constexpr int p = decltype(p_)::value, kk = p / 8, n = p % 8;
      constexpr int kn = p / 16 + 1;
      if constexpr (p == 16 * kn - lead1(kn)) {
        wait_lgkm0();
        bar(); SCHED();
      }
      if constexpr ((n & 1) == 0) acc[n >> 1][0] = MFMA(xf[0][kk], fr[p], acc[n >> 1][0]); else ach[n >> 1][0] = MFMA(fr[p], xf[0][kk], ach[n >> 1][0]);
      SCHED();
      static_for<p + 1, p + 16>([&](auto t_) {
        constexpr int t = decltype(t_)::value;
        if constexpr (t < 128 && t >= 8 && sched1(t) == p) { rd(t_); SCHED(); }
      });
      if constexpr ((n & 1) == 0) acc[n >> 1][1] = MFMA(xf[1][kk], fr[p], acc[n >> 1][1]); else ach[n >> 1][1] = MFMA(fr[p], xf[1][kk], ach[n >> 1][1]);
      SCHED();
    });
    const int lane = fresh_lane(), q = lane >> 4, cc = lane & 15;
    f32x4 mr[2], rr[2];
    const float sgn = (c.ks >> 1) ? -1.f : 1.f;
#pragma unroll
    for (int u = 0; u < 2; ++u) {
      mr[u] = *(const LAS f32x4*)(lds + LDS_MEAN + ((2 * w + u) * 16 + 4 * q) * 4);
      rr[u] = *(const LAS f32x4*)(lds + LDS_RSTD + ((2 * w + u) * 16 + 4 * q) * 4) * sgn;
    }
    float cs[4];
#pragma unroll
    for (int n = 0; n < 4; ++n) cs[n] = *(const LAS float*)(lds + LDS_CSW + (n * 16 + cc) * 4);
    const rsrc_t rA = mk_rsrc(c.ws + WS_ABT + (size_t)((((c.ks * 2 + (c.ec >> 1)) * 64 + c.tp * 4 + w) * 8 + (c.ec & 1) * 4) * 1024), 4096);
#pragma unroll
    for (int n = 0; n < 4; ++n) {
      const f32x4 o0 = (acc[n][0] - mr[0] * cs[n]) * rr[0], o1 = (acc[n][1] - mr[1] * cs[n]) * rr[1];
      u32x4 d = {pk_bf16(o0[0], o0[1]), pk_bf16(o0[2], o0[3]), pk_bf16(o1[0], o1[1]), pk_bf16(o1[2], o1[3])};
      __builtin_amdgcn_raw_buffer_store_b128(d, rA, lane * 16 + n * 1024, 0, 16);
    }
  }
  SCHED();
  u32x4 hd[4];
  if (c.two) {
    unsigned vL = 0;
    static_assert(RPOS >= 16 && RPOS + LOOK2 <= 32 && FPOS < RPOS, "R follows the last use of J0 and precedes the first read of J2");
    bf16x8 g[64];
    f32x4 ac2[4][2];
    auto rd2 = [&](auto t_) {
      constexpr int t = decltype(t_)::value;
      g[t] = ldstr(lds + (nib(JSEQ, t / 16) - 4) * SLOT + ja[t & 3] + ((t >> 2) & 3) * 4096, 512);
    };
    static_for<0, LOOK2>(rd2);
#pragma unroll
    for (int n = 0; n < 4; ++n) { ac2[n][0] = (f32x4){0, 0, 0, 0}; ac2[n][1] = (f32x4){0, 0, 0, 0}; }
    SCHED();
    static_for<0, 64>([&](auto p_) {
      constexpr int p = decltype(p_)::value, kk = p / 4, n = p & 3;
      if constexpr (p == FPOS) { wait_vm<0>(); bar(); SCHED(); }
      if constexpr (p == RPOS) { bar(); SCHED(); }
      if constexpr (p == EPOS) { h_pack(c, ach, 0, w, hd); vL = fresh_lane() * 16; SCHED(); }
      if constexpr (p >= EPOS && p < EPOS + 4 * ESTEP && (p - EPOS) % ESTEP == 0) { h_store(c, hd[(p - EPOS) / ESTEP], 0, w, (p - EPOS) / ESTEP, vL); SCHED(); }
      ac2[n][0] = MFMA(g[p], xf[0][kk], ac2[n][0]);
      SCHED();
      if constexpr (p + LOOK2 < 64) { rd2(IC<p + LOOK2>{}); SCHED(); }
      ac2[n][1] = MFMA(g[p], xf[1][kk], ac2[n][1]);
      SCHED();
    });
    h_pack(c, ac2, 64, w, hd);
  } else {
    wait_vm<0>(); bar(); bar();
    h_pack(c, ach, 0, w, hd);
  }
  {
    const unsigned vL = fresh_lane() * 16;
#pragma unroll
    for (int i = 0; i < 4; ++i) h_store(c, hd[i], c.two ? 64 : 0, w, i, vL);
  }
  consumer2(c);
}

__global__ __launch_bounds__(512, 2) void fnet_mlp_fused(const char* x, const char* Cr, const char* Ci, const char* Wr, const char* Wi,
                                                         const char* w1, const char* b1, const char* w2, const char* b2, char* out, char* ws) {
  extern __shared__ __attribute__((aligned(16))) char shm[];
  Ctx c;
  { u64 pc; asm volatile("s_getpc_b64 %0" : "=s"(pc)); c.pc = (const char*)(pc & ~(u64)255); }
  c.x = x; c.Cr = Cr; c.Ci = Ci; c.Wr = Wr; c.Wi = Wi; c.w1 = w1; c.b1 = b1; c.w2 = w2; c.b2 = b2; c.out = out; c.ws = ws;
  c.lds = (LAS char*)shm; c.lb = (unsigned)(size_t)c.lds;
  c.wid = __builtin_amdgcn_readfirstlane(threadIdx.x >> 6);
  const int b = blockIdx.x, xcd = b & 7, r = b >> 3;
  c.ks = xcd >> 1; c.tp = (xcd & 1) * 8 + (r >> 2); c.ec = r & 3;
  c.two = c.ec != 3;
  c.PA = 7 * c.ks + 2 * c.ec;
  c.rot1 = 4 * ((c.ec + (r >> 2)) & 1);
  if (c.wid == 4) loaderA(c); else if (c.wid == 5) loaderB(c); else if (c.wid >= 6) helper(c); else consumer(c);
}

extern "C" void kernel_launch(void* const* d_in, const int* in_sizes, int n_in, void* d_out, int out_size, void* d_ws, size_t ws_size, hipStream_t stream) {
  (void)hipFuncSetAttribute((const void*)fnet_mlp_fused, hipFuncAttributeMaxDynamicSharedMemorySize, LDS_BYTES);
  fnet_mlp_fused<<<256, 512, LDS_BYTES, stream>>>((const char*)d_in[0], (const char*)d_in[1], (const char*)d_in[2], (const char*)d_in[3], (const char*)d_in[4],
                                                  (const char*)d_in[6], (const char*)d_in[7], (const char*)d_in[8], (const char*)d_in[9], (char*)d_out, (char*)d_ws);
}
```
